```python
import torch

def _softmax(x, axis=-1):
    return torch.softmax(x, dim=axis)
def _gelu(x):
    return torch.nn.functional.gelu(x, approximate="tanh")
def _silu(x):
    return torch.nn.functional.silu(x)
def _sigmoid(x):
    return torch.sigmoid(x)
def _layernorm(x, axis=-1, eps=1e-5):
    m = x.mean(dim=axis, keepdim=True)
    v = x.var(dim=axis, keepdim=True, unbiased=False)
    return (x - m) / torch.sqrt(v + eps)
def _scatter_add(tgt, idx, src, axis=0):
    return tgt.clone().index_add_(axis, idx, src)
def _segment_sum(src, seg, n):
    out = torch.zeros((n,) + src.shape[1:], dtype=src.dtype)
    return out.index_add_(0, seg, src)
def _pad(x, pw):
    flat = tuple(v for pair in reversed(pw) for v in pair)
    return torch.nn.functional.pad(x, flat)

kernel_name = 'gather_scatter__cheap_equivariant_egnn__graph'

def setup_inputs():

    _w = (lambda a: (lambda t: t.to(torch.bfloat16) if t.is_floating_point() else t)(torch.as_tensor(a)))
    N, deg, D, De = 10000, 64, 64, max(0, 1)
    E = N * deg
    return {
        "h": _w((lambda *s: torch.randn(*s, dtype=torch.bfloat16) * 0.1)(N, D)),
        "pos": _w((lambda *s: torch.randn(*s, dtype=torch.bfloat16) * 0.8)(N, 3)),
        "e_attr": _w((lambda *s: torch.randn(*s, dtype=torch.bfloat16) * 0.8)(E, De)),
        "src": _w((lambda lo, hi, *s: torch.randint(lo, hi, s))(0, N, E).to(torch.int64)),
        "dst": _w((lambda lo, hi, *s: torch.randint(lo, hi, s))(0, N, E).to(torch.int64)),
        "w_e": _w(0.1 * (lambda *s: torch.randn(*s, dtype=torch.bfloat16) * 0.8)(2 * D + 1 + De, D)),
        "w_x": _w(0.1 * (lambda *s: torch.randn(*s, dtype=torch.bfloat16) * 0.1)(D, 1)),
        "w_h": _w(0.1 * (lambda *s: torch.randn(*s, dtype=torch.bfloat16) * 0.1)(2 * D, D)),
    }

def reference(h, pos, e_attr, src, dst, w_e, w_x, w_h):
    N, D = h.shape[0], h.shape[1]
    diff = pos[dst] - pos[src]
    dist2 = torch.sum(diff * diff, axis=-1, keepdims=True)
    e_in = torch.concatenate([h[dst], h[src], dist2, e_attr], axis=-1)
    m_ij = _silu(e_in @ w_e)
    coord_scale = m_ij @ w_x
    pos_new = pos + _segment_sum(diff * coord_scale, dst, N)
    m_i = _segment_sum(m_ij, dst, N)
    h_new = h + _silu(torch.concatenate([h, m_i], axis=-1) @ w_h)
    return h_new, pos_new
```

```cpp
#include <hip/hip_runtime.h>
#include <hip/hip_bf16.h>
#include <stdint.h>

static __device__ __forceinline__ float bf2f(uint16_t v) { return __uint_as_float(((uint32_t)v) << 16); }
static __device__ __forceinline__ uint16_t f2bf(float f) { __hip_bfloat16 b = __float2bfloat16(f); return *(uint16_t*)&b; }
static __device__ __forceinline__ float rbf(float f) { return bf2f(f2bf(f)); }

__global__ void edge_kernel(const uint16_t* h, const uint16_t* pos, const uint16_t* ea, const int* src, const int* dst,
                            const uint16_t* we, const uint16_t* wx, float* macc, float* pacc, int E) {
    int lane = threadIdx.x & 63;
    int e = blockIdx.x * (blockDim.x >> 6) + (threadIdx.x >> 6);
    if (e >= E) return;
    int s = src[e], d = dst[e];
    float df[3]; float d2 = 0.f;
    for (int c = 0; c < 3; ++c) { df[c] = rbf(bf2f(pos[d * 3 + c]) - bf2f(pos[s * 3 + c])); d2 += rbf(df[c] * df[c]); }
    d2 = rbf(d2);
    float acc = 0.f;
    for (int k = 0; k < 64; ++k) acc += bf2f(h[d * 64 + k]) * bf2f(we[k * 64 + lane]);
    for (int k = 0; k < 64; ++k) acc += bf2f(h[s * 64 + k]) * bf2f(we[(64 + k) * 64 + lane]);
    acc += d2 * bf2f(we[128 * 64 + lane]);
    acc += bf2f(ea[e]) * bf2f(we[129 * 64 + lane]);
    acc = rbf(acc);
    float m = rbf(acc / (1.f + __expf(-acc)));
    atomicAdd(&macc[d * 64 + lane], m);
    float cs = m * bf2f(wx[lane]);
    for (int o = 32; o > 0; o >>= 1) cs += __shfl_xor(cs, o);
    cs = rbf(cs);
    if (lane < 3) atomicAdd(&pacc[d * 3 + lane], rbf(df[lane] * cs));
}

__global__ void node_kernel(const uint16_t* h, const uint16_t* pos, const uint16_t* wh, const float* macc, const float* pacc,
                            uint16_t* out, int N) {
    int lane = threadIdx.x & 63;
    int n = blockIdx.x * (blockDim.x >> 6) + (threadIdx.x >> 6);
    if (n >= N) return;
    float acc = 0.f;
    for (int k = 0; k < 64; ++k) acc += bf2f(h[n * 64 + k]) * bf2f(wh[k * 64 + lane]);
    for (int k = 0; k < 64; ++k) acc += rbf(macc[n * 64 + k]) * bf2f(wh[(64 + k) * 64 + lane]);
    acc = rbf(acc);
    float s = rbf(acc / (1.f + __expf(-acc)));
    out[n * 64 + lane] = f2bf(bf2f(h[n * 64 + lane]) + s);
    if (lane < 3) out[N * 64 + n * 3 + lane] = f2bf(bf2f(pos[n * 3 + lane]) + rbf(pacc[n * 3 + lane]));
}

extern "C" void kernel_launch(void* const* d_in, const int* in_sizes, int n_in,
                              void* d_out, int out_size, void* d_ws, size_t ws_size,
                              hipStream_t stream) {
    const uint16_t* h = (const uint16_t*)d_in[0];
    const uint16_t* pos = (const uint16_t*)d_in[1];
    const uint16_t* ea = (const uint16_t*)d_in[2];
    const int* src = (const int*)d_in[3];
    const int* dst = (const int*)d_in[4];
    const uint16_t* we = (const uint16_t*)d_in[5];
    const uint16_t* wx = (const uint16_t*)d_in[6];
    const uint16_t* wh = (const uint16_t*)d_in[7];
    int N = in_sizes[0] / 64;
    int E = in_sizes[3];
    float* macc = (float*)d_ws;
    float* pacc = macc + (size_t)N * 64;
    hipMemsetAsync(d_ws, 0, ((size_t)N * 64 + N * 3 + 16) * 4, stream);
    edge_kernel<<<(E + 3) / 4, 256, 0, stream>>>(h, pos, ea, src, dst, we, wx, macc, pacc, E);
    node_kernel<<<(N + 3) / 4, 256, 0, stream>>>(h, pos, wh, macc, pacc, (uint16_t*)d_out, N);
}
```

```cpp
#include <hip/hip_runtime.h>
#include <stdint.h>

namespace egnn {

typedef short    bf16x8 __attribute__((ext_vector_type(8)));
typedef short    s16x4  __attribute__((ext_vector_type(4)));
typedef float    f32x16 __attribute__((ext_vector_type(16)));
typedef float    f32x4  __attribute__((ext_vector_type(4)));
typedef float    f32x2  __attribute__((ext_vector_type(2)));
typedef __bf16   bf16x2 __attribute__((ext_vector_type(2)));
typedef unsigned u32x4  __attribute__((ext_vector_type(4)));
typedef unsigned u32x2  __attribute__((ext_vector_type(2)));
#define LDS_AS __attribute__((address_space(3)))
#define DI __device__ __forceinline__

constexpr int NN = 10000;
constexpr int NE = 640000;
constexpr int BN = 40;
constexpr int NBK = 250;
constexpr int CE = 2560;
constexpr int NCH = 250;
constexpr float NEG_LOG2E = -1.4426950408889634f;
constexpr float NEG_LN2 = -0.6931471805599453f;

constexpr size_t W_RECS = 0;
constexpr size_t W_T    = 5u << 20;
constexpr size_t W_B    = (5u << 20) + (512u << 10);
constexpr size_t W_A    = 7u << 20;
constexpr size_t W_P    = (8u << 20) + (512u << 10);

DI float bf2f(unsigned v16) { return __uint_as_float(v16 << 16); }
DI unsigned pk_bf16(float lo, float hi) {
    f32x2 x = {lo, hi};
    return __builtin_bit_cast(unsigned, __builtin_convertvector(x, bf16x2));
}
DI float silu_f(float x) {
    return x * __builtin_amdgcn_rcpf(1.0f + __builtin_amdgcn_exp2f(x * NEG_LOG2E));
}
DI s16x4 tr_read(const char* p) {
    return __builtin_amdgcn_ds_read_tr16_b64_v4i16((LDS_AS s16x4*)p);
}
DI bf16x8 cat8(s16x4 a, s16x4 b) { return __builtin_shufflevector(a, b, 0, 1, 2, 3, 4, 5, 6, 7); }
DI void wait_vm0() { asm volatile("s_waitcnt vmcnt(0)" ::: "memory"); }
DI void wait_lgkm0() { asm volatile("s_waitcnt lgkmcnt(0)" ::: "memory"); }
DI void bar() { asm volatile("s_waitcnt lgkmcnt(0)\n\ts_barrier" ::: "memory"); }

DI void dma1(unsigned ldsaddr, const void* gbase, unsigned voff) {
    unsigned keep;
    asm volatile("s_mov_b32 %0, m0\n\ts_mov_b32 m0, %1\n\ts_nop 0\n\t"
                 "global_load_lds_dwordx4 %2, %3\n\t"
                 "s_mov_b32 m0, %0"
                 : "=&s"(keep) : "s"(ldsaddr), "v"(voff), "s"(gbase) : "memory");
}
DI void dma4(unsigned ldsaddr, const void* gbase, unsigned v0, unsigned v1, unsigned v2, unsigned v3) {
    unsigned keep;
    asm volatile("s_mov_b32 %0, m0\n\ts_mov_b32 m0, %1\n\ts_nop 0\n\t"
                 "global_load_lds_dwordx4 %2, %6\n\t"
                 "global_load_lds_dwordx4 %3, %6 offset:1024\n\t"
                 "global_load_lds_dwordx4 %4, %6 offset:2048\n\t"
                 "global_load_lds_dwordx4 %5, %6 offset:3072\n\t"
                 "s_mov_b32 m0, %0"
                 : "=&s"(keep) : "s"(ldsaddr), "v"(v0), "v"(v1), "v"(v2), "v"(v3), "s"(gbase) : "memory");
}
template <int CTRL, int ROWMASK> DI unsigned dpp_add(unsigned x) {
    return x + (unsigned)__builtin_amdgcn_update_dpp(0, (int)x, CTRL, ROWMASK, 0xf, true);
}
DI unsigned scan32(unsigned x) {
    x = dpp_add<0x111, 0xf>(x); x = dpp_add<0x112, 0xf>(x); x = dpp_add<0x114, 0xf>(x); x = dpp_add<0x118, 0xf>(x);
    x = dpp_add<0x142, 0xa>(x);
    return x;
}
DI unsigned scan64(unsigned x) { x = scan32(x); x = dpp_add<0x143, 0xc>(x); return x; }

constexpr int K1_T = 640;
constexpr int S_CNT = 0, S_OFF = 1024, S_STG = 2048, S_W = 22528, S_H = 38912, S_O = 47104, S_END = 57344;

__global__ __launch_bounds__(K1_T) void k_sort(const int* __restrict__ src, const int* __restrict__ dst,
                                               const uint16_t* __restrict__ ea, const uint16_t* __restrict__ h,
                                               const uint16_t* __restrict__ pos, const uint16_t* __restrict__ we,
                                               char* __restrict__ ws) {
    __shared__ __attribute__((aligned(16))) char sm[S_END];
    const int tid = threadIdx.x, lane = tid & 63;
    const int wv = __builtin_amdgcn_readfirstlane(tid >> 6);
    const int c = blockIdx.x;
    const unsigned smb = (unsigned)(size_t)(LDS_AS char*)sm;

    const int e0 = c * CE + 4 * tid;
    const u32x4 d4 = *(const u32x4*)(dst + e0);
    const u32x4 s4 = *(const u32x4*)(src + e0);
    const u32x2 a4 = *(const u32x2*)(ea + e0);

    for (int p = wv; p < 21; p += 10) {
        if (p < 16) {
            dma1(smb + S_W + p * 1024, we, p * 1024 + lane * 16);
        } else {
            const int row = (p - 16) * 8 + (lane >> 3);
            const int ch = (lane & 7) ^ ((row >> 1) & 7);
            dma1(smb + S_H + (p - 16) * 1024, h, (c * BN + row) * 128 + ch * 16);
        }
    }
    if (tid < 256) ((unsigned*)(sm + S_CNT))[tid] = 0;
    if (tid < BN) {
        const int n = c * BN + tid;
        f32x4 p = {bf2f(pos[n * 3]), bf2f(pos[n * 3 + 1]), bf2f(pos[n * 3 + 2]), 0.f};
        *(f32x4*)(ws + W_P + (size_t)n * 16) = p;
    }
    if (c == 0 && tid >= 64 && tid < 64 + 9) {
        const u32x4 z = {0, 0, 0, 0};
        if (tid < 72) *(u32x4*)(ws + W_B + (size_t)NN * 128 + (tid - 64) * 16) = z;
        else *(u32x4*)(ws + W_P + (size_t)NN * 16) = z;
    }
    bar();

    unsigned bk[4], rk[4];
#pragma unroll
    for (int i = 0; i < 4; ++i) {
        bk[i] = (d4[i] * 52429u) >> 21;
        rk[i] = atomicAdd((unsigned*)(sm + S_CNT) + bk[i], 1u);
    }
    wait_vm0();
    bar();

    if (wv < 8) {
        const int m = wv >> 2, ct = (wv >> 1) & 1, nt = wv & 1;
        const int r = lane & 31, hh = lane >> 5, blk = (lane >> 4) & 1, q = (lane & 15) >> 2, p = lane & 3;
        f32x16 acc;
#pragma unroll
        for (int i = 0; i < 16; ++i) acc[i] = 0.f;
        const int node = nt * 32 + r;
#pragma unroll
        for (int s = 0; s < 4; ++s) {
            const char* wa = sm + S_W + (64 * m + 16 * s + 8 * hh + q) * 128 + (32 * ct + 16 * blk + 4 * p) * 2;
            const bf16x8 a = cat8(tr_read(wa), tr_read(wa + 4 * 128));
            const bf16x8 b = *(const bf16x8*)(sm + S_H + node * 128 + 16 * ((2 * s + hh) ^ ((node >> 1) & 7)));
            acc = __builtin_amdgcn_mfma_f32_32x32x16_bf16(a, b, acc, 0, 0, 0);
        }
        if (node < BN) {
#pragma unroll
            for (int g = 0; g < 4; ++g) {
                u32x2 o = {pk_bf16(acc[4 * g] * NEG_LOG2E, acc[4 * g + 1] * NEG_LOG2E),
                           pk_bf16(acc[4 * g + 2] * NEG_LOG2E, acc[4 * g + 3] * NEG_LOG2E)};
                *(u32x2*)(sm + S_O + m * (BN * 128) + node * 128 + 16 * ((4 * ct + g) ^ (node & 7)) + 8 * hh) = o;
            }
        }
    } else if (wv == 9) {
        const u32x4 cn = *(const u32x4*)(sm + S_CNT + lane * 16);
        const unsigned tot = cn[0] + cn[1] + cn[2] + cn[3];
        const unsigned ex = scan64(tot) - tot;
        const u32x4 of = {ex, ex + cn[0], ex + cn[0] + cn[1], ex + cn[0] + cn[1] + cn[2]};
        *(u32x4*)(sm + S_OFF + lane * 16) = of;
        unsigned* T = (unsigned*)(ws + W_T);
        const int cs = (c & 7) * 32 + (c >> 3);
#pragma unroll
        for (int i = 0; i < 4; ++i) {
            const int b = 4 * lane + i;
            if (b < NBK) T[b * 256 + cs] = of[i] | (cn[i] << 16);
        }
    }
    bar();

#pragma unroll
    for (int i = 0; i < 4; ++i) {
        const unsigned pos_i = ((const unsigned*)(sm + S_OFF))[bk[i]] + rk[i];
        const unsigned dl = d4[i] - bk[i] * BN;
        const unsigned av = (i & 1) ? (a4[i >> 1] >> 16) : (a4[i >> 1] & 0xffffu);
        u32x2 rec = {s4[i] | (dl << 16), av};
        *(u32x2*)(sm + S_STG + pos_i * 8) = rec;
    }
    {
        const int m = tid / 320, x = tid - m * 320, node = x >> 3, ch = (x & 7) ^ (node & 7);
        const u32x4 v = *(const u32x4*)(sm + S_O + tid * 16);
        *(u32x4*)(ws + (m ? W_B : W_A) + (size_t)(c * BN + node) * 128 + ch * 16) = v;
    }
    bar();
#pragma unroll
    for (int i = 0; i < 2; ++i) {
        const int x = tid + i * K1_T;
        const u32x4 v = *(const u32x4*)(sm + S_STG + x * 16);
        *(u32x4*)(ws + W_RECS + (size_t)c * (CE * 8) + x * 16) = v;
    }
}

constexpr int RCAP = 3072;
constexpr int L_SLOT = 0;
constexpr int L_STAGE = 65536;
constexpr int L_RECS = 98304;
constexpr int L_AIMG = 123392;
constexpr int L_HIMG = 129536;
constexpr int L_MIMG = 135680;
constexpr int L_PD = 141824;
constexpr int L_OH = 142592;
constexpr int L_BINS = 142848;
constexpr int L_OFFS = 143488;
constexpr int L_WIN = 144128;
constexpr int L_WX = 144192;
constexpr int L_WF = 144448;
constexpr int L_WH = 144512;
constexpr int L_END = 160896;
constexpr unsigned NULL_REC = (unsigned)NN | (8u << 16);

struct Geo { unsigned w0, w1, ua, vb, saddr; bool valid; };

DI void silu_pack(const f32x16& x, unsigned (&pk)[8]) {
#pragma unroll
    for (int i = 0; i < 8; ++i) {
        const f32x2 y = {x[2 * i], x[2 * i + 1]};
        f32x2 d = {__builtin_amdgcn_exp2f(y[0]), __builtin_amdgcn_exp2f(y[1])};
        d = d + 1.0f;
        const f32x2 r = {__builtin_amdgcn_rcpf(d[0]), __builtin_amdgcn_rcpf(d[1])};
        const f32x2 m = y * r;
        pk[i] = __builtin_bit_cast(unsigned, __builtin_convertvector(m, bf16x2));
    }
}
DI bf16x8 as_frag(unsigned a, unsigned b, unsigned c, unsigned d) {
    u32x4 v = {a, b, c, d};
    return __builtin_bit_cast(bf16x8, v);
}

__global__ __launch_bounds__(1024) void k_edge(const uint16_t* __restrict__ h, const uint16_t* __restrict__ pos,
                                               const uint16_t* __restrict__ we, const uint16_t* __restrict__ wx,
                                               const uint16_t* __restrict__ wh, const char* __restrict__ ws,
                                               uint16_t* __restrict__ out) {
    extern __shared__ __attribute__((aligned(16))) char sm[];
    const int tid = threadIdx.x, lane = tid & 63;
    const int wv = __builtin_amdgcn_readfirstlane(tid >> 6);
    const int b = (blockIdx.x & 7) * 32 + (blockIdx.x >> 3);
    if (b >= NBK) return;
    const unsigned smb = (unsigned)(size_t)(LDS_AS char*)sm;
    const u32x2* recs = (const u32x2*)(ws + W_RECS);
    const char* Btab = ws + W_B;
    const char* Ptab = ws + W_P;

    const int cs = 16 * wv + (lane >> 2);
    const int ch_id = (cs & 31) * 8 + (cs >> 5);
    unsigned tval = ((const unsigned*)(ws + W_T))[b * 256 + cs];

    if (wv < 5) {
        dma1(smb + L_AIMG + wv * 1024, ws + W_A, (b * BN + wv * 8) * 128 + lane * 16);
    } else if (wv < 10) {
        const int row = (wv - 5) * 8 + (lane >> 3);
        dma1(smb + L_HIMG + (wv - 5) * 1024, h, (b * BN + row) * 128 + (((lane & 7) ^ ((row >> 1) & 7)) * 16));
    }
    {
        const int k = wv * 8 + (lane >> 3);
        const int sg = (k & 2) | ((k >> 1) & 4);
        dma1(smb + L_WH + wv * 1024, wh, k * 128 + (((lane & 7) ^ sg) * 16));
    }
    {
        const u32x4 z = {0, 0, 0, 0};
        *(u32x4*)(sm + L_STAGE + wv * 2048 + lane * 16) = z;
        *(u32x4*)(sm + L_STAGE + wv * 2048 + 1024 + lane * 16) = z;
        const u32x4 nr = {NULL_REC, 0, NULL_REC, 0};
        *(u32x4*)(sm + L_RECS + tid * 16) = nr;
        if (tid < (RCAP + 64) / 2 - 1024) *(u32x4*)(sm + L_RECS + (1024 + tid) * 16) = nr;
        if (tid < 160) ((unsigned*)(sm + L_BINS))[tid] = 0;
        if (tid >= 192 && tid < 192 + 48) {
            const int n = tid - 192;
            f32x4 p = {0.f, 0.f, 0.f, 0.f};
            if (n < BN) { const int gn = (b * BN + n) * 3; p[0] = bf2f(pos[gn]); p[1] = bf2f(pos[gn + 1]); p[2] = bf2f(pos[gn + 2]); }
            *(f32x4*)(sm + L_PD + n * 16) = p;
        }
        if (tid >= 256 && tid < 256 + 36) {
            const int x = tid - 256, n = x >> 2, j = x & 3;
            ((unsigned*)(sm + L_OH))[x] = (n < 8 && (n >> 1) == j) ? (0x3F80u << (16 * (n & 1))) : 0u;
        }
        if (tid >= 320 && tid < 320 + 64) ((float*)(sm + L_WX))[tid - 320] = bf2f(wx[tid - 320]) * NEG_LN2;
        if (tid >= 384 && tid < 384 + 64) {
            const int x = tid - 384;
            u32x4 v = z;
            if (x < 16) v = *(const u32x4*)(we + 128 * 64 + x * 8);
            *(u32x4*)(sm + L_AIMG + BN * 128 + x * 16) = v;
        }
    }

    unsigned cnt = ch_id < NCH ? (tval >> 16) : 0u;
    const unsigned incl = scan64((lane & 3) == 0 ? cnt : 0u);
    const unsigned mw = __builtin_amdgcn_readlane(incl, 63);
    bool slow_w = mw > 256;
    unsigned* map = (unsigned*)(sm + L_SLOT + wv * 4096);
    if (!slow_w) {
        const unsigned g0 = ch_id * CE + (tval & 0xffffu), ex = incl - cnt;
        for (unsigned i = lane & 3; i < cnt; i += 4) map[ex + i] = g0 + i;
    }
    if (lane == 0) ((unsigned*)(sm + L_WF))[wv] = slow_w ? 1u : 0u;
    u32x2 rec[4];
    bool have[4];
#pragma unroll
    for (int k = 0; k < 4; ++k) {
        have[k] = !slow_w && (unsigned)(64 * k + lane) < mw;
        rec[k] = u32x2{0, 0};
        if (have[k]) rec[k] = recs[map[64 * k + lane]];
    }
    bar();

    unsigned bin[4], rk[4];
#pragma unroll
    for (int k = 0; k < 4; ++k) {
        bin[k] = ((rec[k][0] >> 19) & 7u) * 32 + (lane & 31);
        rk[k] = 0;
        if (have[k]) rk[k] = atomicAdd((unsigned*)(sm + L_BINS) + bin[k], 1u);
    }
    wait_vm0();
    bar();

    if (wv == 0) {
        const unsigned* bins = (const unsigned*)(sm + L_BINS);
        const int j = lane & 31;
        unsigned c5[5], in5[5], start = 0;
#pragma unroll
        for (int w = 0; w < 5; ++w) { c5[w] = bins[w * 32 + j]; in5[w] = scan32(c5[w]); }
#pragma unroll
        for (int w = 0; w < 5; ++w) {
            const unsigned tot = __builtin_amdgcn_readlane(in5[w], 31);
            const unsigned tiles = (tot + 31) >> 5;
            ((unsigned*)(sm + L_OFFS))[w * 32 + j] = start + in5[w] - c5[w];
            if (lane == 0) { ((unsigned*)(sm + L_WIN))[w] = start; ((unsigned*)(sm + L_WIN))[5 + w] = tiles; }
            start += tiles * 32;
        }
        const bool any_slow = __any(((const unsigned*)(sm + L_WF))[lane & 15] != 0);
        if (lane == 0) ((unsigned*)(sm + L_WIN))[10] = (any_slow || start > RCAP) ? 1u : 0u;
    }
    bar();
    const bool slow = __builtin_amdgcn_readfirstlane(((const unsigned*)(sm + L_WIN))[10]) != 0;

    const int r = lane & 31, hh = lane >> 5;
    const int win = wv < 4 ? 4 : (wv - 4) / 3;
    char* part = sm + L_SLOT + wv * 4096;

    if (!slow) {
#pragma unroll
        for (int k = 0; k < 4; ++k) {
            if (have[k]) {
                const unsigned p = ((const unsigned*)(sm + L_OFFS))[bin[k]] + rk[k];
                u32x2 v = {rec[k][0] & 0x0007ffffu, rec[k][1]};
                *(u32x2*)(sm + L_RECS + p * 8) = v;
            }
        }
        bar();

        const int nw = wv < 4 ? 4 : 3, jw = wv < 4 ? wv : (wv - 4) - 3 * win;
        const int wstart = __builtin_amdgcn_readfirstlane(((const unsigned*)(sm + L_WIN))[win]);
        const int wtiles = __builtin_amdgcn_readfirstlane(((const unsigned*)(sm + L_WIN))[5 + win]);
        const int ta = (wtiles * jw) / nw, tb = (wtiles * (jw + 1)) / nw;

        const int blk = (lane >> 4) & 1, q = (lane & 15) >> 2, p = lane & 3;
        bf16x8 e0, e1;
        {
            const int row = hh ? BN + q : 8 * win + q;
            const char* ab = sm + L_AIMG + row * 128 + (16 * blk + 4 * p) * 2;
            e0 = cat8(tr_read(ab), tr_read(ab + 512));
            e1 = cat8(tr_read(ab + 64), tr_read(ab + 64 + 512));
        }
        bf16x8 i0, i1;
#pragma unroll
        for (int j = 0; j < 8; ++j) {
            i0[j] = (r == 8 * hh + j) ? (short)0x3F80 : (short)0;
            i1[j] = (r == 16 + 8 * hh + j) ? (short)0x3F80 : (short)0;
        }
        const int rho = 8 * (r & 3) + (r >> 2);
        const int fsw = (r & 3) ^ (((r >> 3) & 1) << 2);
        const char* aad[4];
#pragma unroll
        for (int s = 0; s < 4; ++s) aad[s] = sm + L_SLOT + wv * 4096 + rho * 128 + 16 * ((2 * s + hh) ^ fsw);
        unsigned kk[4];
#pragma unroll
        for (int i = 0; i < 4; ++i)
            kk[i] = 16 * ((lane & 7) ^ i ^ (((lane >> 4) & 1) << 2)) + (3072 - 1024 * i);
        const char* gB = Btab - 3072;
        const unsigned slot_a = smb + L_SLOT + wv * 4096;
        char* stage = sm + L_STAGE + wv * 2048;
        const char* trb = stage + (4 * hh + q) * 64 + blk * 32 + p * 8;
        const char* pdw = sm + L_PD + win * 128;

        f32x16 z0, z1;
#pragma unroll
        for (int i = 0; i < 16; ++i) { z0[i] = 0.f; z1[i] = 0.f; }

        auto issue_rows = [&](int rec_index) {
            const unsigned* rp = (const unsigned*)(sm + L_RECS + (rec_index + 4 * (lane >> 3)) * 8);
            const unsigned s0 = rp[0], s1 = rp[2], s2 = rp[4], s3 = rp[6];
            wait_lgkm0();
            dma4(slot_a, gB, (s0 & 0xffffu) * 128 + kk[0], (s1 & 0xffffu) * 128 + kk[1],
                 (s2 & 0xffffu) * 128 + kk[2], (s3 & 0xffffu) * 128 + kk[3]);
        };
        u32x2 rr;
        f32x4 ps;
        auto tile = [&](unsigned u, unsigned w0, unsigned w1, char* saddr, bool wr, int next_rec, bool has_next, bool next_pair) {
            wait_vm0();
            const bf16x8 a0 = *(const bf16x8*)aad[0], a1 = *(const bf16x8*)aad[1];
            const bf16x8 a2 = *(const bf16x8*)aad[2], a3 = *(const bf16x8*)aad[3];
            u32x4 ex = *(const u32x4*)(sm + (hh ? (unsigned)L_OH : u));
            ex[0] = hh ? u : ex[0];
            if (wr) *(u32x2*)saddr = u32x2{w0, w1};
            const bf16x8 wf0 = cat8(tr_read(trb), tr_read(trb + 512));
            const bf16x8 wf1 = cat8(tr_read(trb + 1024), tr_read(trb + 1536));
            if (wr) *(u32x2*)saddr = u32x2{0, 0};
            if (has_next) {
                if (next_pair) rr = *(const u32x2*)(sm + L_RECS + (next_rec + lane) * 8);
                issue_rows(next_rec);
                if (next_pair) ps = *(const f32x4*)(Ptab + (rr[0] & 0xffffu) * 16);
            }
            const bf16x8 ef = __builtin_bit_cast(bf16x8, ex);
            f32x16 zero;
#pragma unroll
            for (int i = 0; i < 16; ++i) zero[i] = 0.f;
            unsigned pk[8];
            f32x16 x = __builtin_amdgcn_mfma_f32_32x32x16_bf16(a0, i0, zero, 0, 0, 0);
            x = __builtin_amdgcn_mfma_f32_32x32x16_bf16(a1, i1, x, 0, 0, 0);
            x = __builtin_amdgcn_mfma_f32_32x32x16_bf16(ef, e0, x, 0, 0, 0);
            f32x16 y = __builtin_amdgcn_mfma_f32_32x32x16_bf16(a2, i0, zero, 0, 0, 0);
            y = __builtin_amdgcn_mfma_f32_32x32x16_bf16(a3, i1, y, 0, 0, 0);
            y = __builtin_amdgcn_mfma_f32_32x32x16_bf16(ef, e1, y, 0, 0, 0);
            silu_pack(x, pk);
            z0 = __builtin_amdgcn_mfma_f32_32x32x16_bf16(as_frag(pk[0], pk[1], pk[2], pk[3]), wf0, z0, 0, 0, 0);
            z0 = __builtin_amdgcn_mfma_f32_32x32x16_bf16(as_frag(pk[4], pk[5], pk[6], pk[7]), wf1, z0, 0, 0, 0);
            silu_pack(y, pk);
            z1 = __builtin_amdgcn_mfma_f32_32x32x16_bf16(as_frag(pk[0], pk[1], pk[2], pk[3]), wf0, z1, 0, 0, 0);
            z1 = __builtin_amdgcn_mfma_f32_32x32x16_bf16(as_frag(pk[4], pk[5], pk[6], pk[7]), wf1, z1, 0, 0, 0);
        };

        if (ta < tb) {
            int base = wstart + 32 * ta;
            const int end = wstart + 32 * tb;
            rr = *(const u32x2*)(sm + L_RECS + (base + lane) * 8);
            issue_rows(base);
            ps = *(const f32x4*)(Ptab + (rr[0] & 0xffffu) * 16);
            while (true) {
                const unsigned n = (rr[0] >> 16) & 15u;
                const f32x4 pd = *(const f32x4*)(pdw + n * 16);
                const float dx = pd[0] - ps[0], dy = pd[1] - ps[1], dz = pd[2] - ps[2];
                const float d2 = (dx * dx + dy * dy + dz * dz) * NEG_LOG2E;
                const unsigned w0 = pk_bf16(1.0f, dx), w1 = pk_bf16(dy, dz);
                const unsigned ext = pk_bf16(d2, bf2f(rr[1]) * NEG_LOG2E);
                const auto sw = __builtin_amdgcn_permlane32_swap(L_OH + n * 16, ext, false, false);
                char* saddr = stage + r * 64 + n * 8;
                const bool valid = n < 8;
                const bool hasb = base + 32 < end, hasn = base + 64 < end;
                tile(sw[0], w0, w1, saddr, valid && hh == 0, base + 32, hasb, false);
                if (!hasb) break;
                tile(sw[1], w0, w1, saddr, valid && hh == 1, base + 64, hasn, true);
                if (!hasn) break;
                base += 64;
            }
        }

        float cs0 = 0.f, cs1 = 0.f;
#pragma unroll
        for (int g = 0; g < 4; ++g) {
            const f32x4 w0 = *(const f32x4*)(sm + L_WX + (8 * g + 4 * hh) * 4);
            const f32x4 w1 = *(const f32x4*)(sm + L_WX + (32 + 8 * g + 4 * hh) * 4);
#pragma unroll
            for (int i = 0; i < 4; ++i) { cs0 += z0[4 * g + i] * w0[i]; cs1 += z1[4 * g + i] * w1[i]; }
        }
        const float csh = cs0 + cs1;
        const auto cw = __builtin_amdgcn_permlane32_swap(__float_as_uint(csh), __float_as_uint(csh), false, false);
        const float cst = __uint_as_float(cw[0]) + __uint_as_float(cw[1]);
        if (hh == 0) ((float*)(part + 2048))[r] = cst;
        if ((lane & 3) == 0) {
            const int n = r >> 2;
#pragma unroll
            for (int g = 0; g < 4; ++g) {
                *(f32x4*)(part + n * 256 + (8 * g + 4 * hh) * 4) = f32x4{z0[4 * g], z0[4 * g + 1], z0[4 * g + 2], z0[4 * g + 3]};
                *(f32x4*)(part + n * 256 + (32 + 8 * g + 4 * hh) * 4) = f32x4{z1[4 * g], z1[4 * g + 1], z1[4 * g + 2], z1[4 * g + 3]};
            }
        }
    } else {
        {
            const u32x4 z = {0, 0, 0, 0};
            *(u32x4*)(part + lane * 16) = z;
            *(u32x4*)(part + 1024 + lane * 16) = z;
            if (lane < 8) *(u32x4*)(part + 2048 + lane * 16) = z;
        }
        bar();
        const float wd = bf2f(we[128 * 64 + lane]), wa = bf2f(we[129 * 64 + lane]);
        const float wxl = ((const float*)(sm + L_WX))[lane];
        const unsigned* T = (const unsigned*)(ws + W_T) + b * 256;
        for (int sidx = wv; sidx < 256; sidx += 16) {
            const int cid = (sidx & 31) * 8 + (sidx >> 5);
            if (cid >= NCH) continue;
            const unsigned tv = T[sidx];
            const unsigned g0 = cid * CE + (tv & 0xffffu), cn = tv >> 16;
            for (unsigned i = 0; i < cn; ++i) {
                const u32x2 rc = recs[g0 + i];
                const unsigned s = rc[0] & 0xffffu, dl = rc[0] >> 16, w = dl >> 3, n = dl & 7;
                const f32x4 ps = *(const f32x4*)(Ptab + s * 16);
                const f32x4 pd = *(const f32x4*)(sm + L_PD + dl * 16);
                const float dx = pd[0] - ps[0], dy = pd[1] - ps[1], dz = pd[2] - ps[2];
                const unsigned e = pk_bf16((dx * dx + dy * dy + dz * dz) * NEG_LOG2E, bf2f(rc[1]) * NEG_LOG2E);
                const unsigned dxy = pk_bf16(dx, dy), dz0 = pk_bf16(dz, 0.f);
                float y = bf2f(((const uint16_t*)(sm + L_AIMG))[dl * 64 + lane]) + bf2f(((const uint16_t*)Btab)[s * 64 + lane]);
                y += bf2f(e & 0xffffu) * wd + bf2f(e >> 16) * wa;
                const float m = bf2f(pk_bf16(y * __builtin_amdgcn_rcpf(1.0f + __builtin_amdgcn_exp2f(y)), 0.f) & 0xffffu);
                char* tgt = sm + L_SLOT + (w == 4 ? 0 : 4 + 3 * w) * 4096;
                atomicAdd((float*)(tgt + n * 256) + lane, m);
                float t = m * wxl;
                for (int o = 32; o > 0; o >>= 1) t += __shfl_xor(t, o);
                if (lane < 3) {
                    const float dv = lane == 0 ? bf2f(dxy & 0xffffu) : lane == 1 ? bf2f(dxy >> 16) : bf2f(dz0 & 0xffffu);
                    atomicAdd((float*)(tgt + 2048) + 4 * n + 1 + lane, dv * t);
                }
            }
        }
    }
    bar();

    if (tid < 640) {
        const int nl = tid >> 4, f4 = (tid & 15) * 4, w = nl >> 3, n = nl & 7;
        const int w0 = w == 4 ? 0 : 4 + 3 * w;
        const char* pb = sm + L_SLOT + w0 * 4096 + n * 256 + f4 * 4;
        f32x4 s = *(const f32x4*)pb + *(const f32x4*)(pb + 4096) + *(const f32x4*)(pb + 8192);
        if (w == 4) s += *(const f32x4*)(pb + 12288);
        s *= NEG_LN2;
        u32x2 o = {pk_bf16(s[0], s[1]), pk_bf16(s[2], s[3])};
        *(u32x2*)(sm + L_MIMG + nl * 128 + 16 * ((f4 >> 3) ^ ((nl >> 1) & 7)) + 8 * ((f4 >> 2) & 1)) = o;
    } else if (tid < 640 + 3 * BN) {
        const int x = tid - 640, nl = x / 3, cc = x - 3 * nl, w = nl >> 3, n = nl & 7;
        const int w0 = w == 4 ? 0 : 4 + 3 * w;
        const char* pb = sm + L_SLOT + w0 * 4096 + 2048 + (4 * n + 1 + cc) * 4;
        float s = *(const float*)pb + *(const float*)(pb + 4096) + *(const float*)(pb + 8192);
        if (w == 4) s += *(const float*)(pb + 12288);
        const float p0 = ((const float*)(sm + L_PD))[nl * 4 + cc];
        out[NN * 64 + (b * BN + nl) * 3 + cc] = (uint16_t)(pk_bf16(p0 + s, 0.f) & 0xffffu);
    }
    bar();

    if (wv < 12) {
        const int rt = wv >> 2, ct = wv & 3;
        const int g = lane >> 4, q = (lane & 15) >> 2, p = lane & 3, node = 16 * rt + (lane & 15);
        f32x4 acc = {0.f, 0.f, 0.f, 0.f};
#pragma unroll
        for (int s = 0; s < 4; ++s) {
            const int k0 = 32 * s + 8 * g + q, k1 = k0 + 4;
            const int chn = 2 * ct + (p >> 1);
            const char* a0 = sm + L_WH + k0 * 128 + 16 * (chn ^ ((k0 & 2) | ((k0 >> 1) & 4))) + 8 * (p & 1);
            const char* a1 = sm + L_WH + k1 * 128 + 16 * (chn ^ ((k1 & 2) | ((k1 >> 1) & 4))) + 8 * (p & 1);
            const bf16x8 a = cat8(tr_read(a0), tr_read(a1));
            const char* img = sm + (s < 2 ? L_HIMG : L_MIMG) + node * 128;
            const bf16x8 bb = *(const bf16x8*)(img + 16 * ((4 * (s & 1) + g) ^ ((node >> 1) & 7)));
            acc = __builtin_amdgcn_mfma_f32_16x16x32_bf16(a, bb, acc, 0, 0, 0);
        }
        if (node < BN) {
            const u32x2 hv = *(const u32x2*)(sm + L_HIMG + node * 128 + 16 * ((2 * ct + (g >> 1)) ^ ((node >> 1) & 7)) + 8 * (g & 1));
            const float o0 = bf2f(hv[0] & 0xffffu) + silu_f(acc[0]), o1 = bf2f(hv[0] >> 16) + silu_f(acc[1]);
            const float o2 = bf2f(hv[1] & 0xffffu) + silu_f(acc[2]), o3 = bf2f(hv[1] >> 16) + silu_f(acc[3]);
            *(u32x2*)(out + (b * BN + node) * 64 + 16 * ct + 4 * g) = u32x2{pk_bf16(o0, o1), pk_bf16(o2, o3)};
        }
    }
}

}

extern "C" void kernel_launch(void* const* d_in, const int* in_sizes, int n_in, void* d_out, int out_size, void* d_ws,
                              size_t ws_size, hipStream_t stream) {
    using namespace egnn;
    const uint16_t* h = (const uint16_t*)d_in[0];
    const uint16_t* pos = (const uint16_t*)d_in[1];
    const uint16_t* ea = (const uint16_t*)d_in[2];
    const int* src = (const int*)d_in[3];
    const int* dst = (const int*)d_in[4];
    const uint16_t* we = (const uint16_t*)d_in[5];
    const uint16_t* wx = (const uint16_t*)d_in[6];
    const uint16_t* wh = (const uint16_t*)d_in[7];
    (void)hipFuncSetAttribute((const void*)k_edge, hipFuncAttributeMaxDynamicSharedMemorySize, L_END);
    k_sort<<<NCH, K1_T, 0, stream>>>(src, dst, ea, h, pos, we, (char*)d_ws);
    k_edge<<<256, 1024, L_END, stream>>>(h, pos, we, wx, wh, (const char*)d_ws, (uint16_t*)d_out);
}
```

```cpp
#include <hip/hip_runtime.h>
#include <stdint.h>

namespace egnn {

typedef short    bf16x8 __attribute__((ext_vector_type(8)));
typedef short    s16x4  __attribute__((ext_vector_type(4)));
typedef float    f32x16 __attribute__((ext_vector_type(16)));
typedef float    f32x4  __attribute__((ext_vector_type(4)));
typedef float    f32x2  __attribute__((ext_vector_type(2)));
typedef __bf16   bf16x2 __attribute__((ext_vector_type(2)));
typedef unsigned u32x4  __attribute__((ext_vector_type(4)));
typedef unsigned u32x2  __attribute__((ext_vector_type(2)));
#define LDS_AS __attribute__((address_space(3)))
#define DI __device__ __forceinline__

constexpr int NN = 10000;
constexpr int NE = 640000;
constexpr int BN = 40;
constexpr int NBK = 250;
constexpr int CE = 2560;
constexpr int NCH = 250;
constexpr float NEG_LOG2E = -1.4426950408889634f;
constexpr float NEG_LN2 = -0.6931471805599453f;

constexpr size_t W_RECS = 0;
constexpr size_t W_T    = 5u << 20;
constexpr size_t W_B    = (5u << 20) + (512u << 10);
constexpr size_t W_A    = 7u << 20;
constexpr size_t W_P    = (8u << 20) + (512u << 10);

DI float bf2f(unsigned v16) { return __uint_as_float(v16 << 16); }
DI unsigned pk_bf16(float lo, float hi) {
    f32x2 x = {lo, hi};
    return __builtin_bit_cast(unsigned, __builtin_convertvector(x, bf16x2));
}
DI float silu_f(float x) {
    return x * __builtin_amdgcn_rcpf(1.0f + __builtin_amdgcn_exp2f(x * NEG_LOG2E));
}
DI s16x4 tr_read(const char* p) {
    return __builtin_amdgcn_ds_read_tr16_b64_v4i16((LDS_AS s16x4*)p);
}
DI bf16x8 cat8(s16x4 a, s16x4 b) { return __builtin_shufflevector(a, b, 0, 1, 2, 3, 4, 5, 6, 7); }
DI void wait_vm0() { asm volatile("s_waitcnt vmcnt(0)" ::: "memory"); }
DI void wait_lgkm0() { asm volatile("s_waitcnt lgkmcnt(0)" ::: "memory"); }
DI void bar() { asm volatile("s_waitcnt lgkmcnt(0)\n\ts_barrier" ::: "memory"); }

DI void dma1(unsigned ldsaddr, const void* gbase, unsigned voff) {
    unsigned keep;
    asm volatile("s_mov_b32 %0, m0\n\ts_mov_b32 m0, %1\n\ts_nop 0\n\t"
                 "global_load_lds_dwordx4 %2, %3\n\t"
                 "s_mov_b32 m0, %0"
                 : "=&s"(keep) : "s"(ldsaddr), "v"(voff), "s"(gbase) : "memory");
}
DI void dma4(unsigned ldsaddr, const void* gbase, unsigned v0, unsigned v1, unsigned v2, unsigned v3) {
    unsigned keep;
    asm volatile("s_mov_b32 %0, m0\n\ts_mov_b32 m0, %1\n\ts_nop 0\n\t"
                 "global_load_lds_dwordx4 %2, %6\n\t"
                 "global_load_lds_dwordx4 %3, %6 offset:1024\n\t"
                 "global_load_lds_dwordx4 %4, %6 offset:2048\n\t"
                 "global_load_lds_dwordx4 %5, %6 offset:3072\n\t"
                 "s_mov_b32 m0, %0"
                 : "=&s"(keep) : "s"(ldsaddr), "v"(v0), "v"(v1), "v"(v2), "v"(v3), "s"(gbase) : "memory");
}
template <int CTRL, int ROWMASK> DI unsigned dpp_add(unsigned x) {
    return x + (unsigned)__builtin_amdgcn_update_dpp(0, (int)x, CTRL, ROWMASK, 0xf, true);
}
DI unsigned scan32(unsigned x) {
    x = dpp_add<0x111, 0xf>(x); x = dpp_add<0x112, 0xf>(x); x = dpp_add<0x114, 0xf>(x); x = dpp_add<0x118, 0xf>(x);
    x = dpp_add<0x142, 0xa>(x);
    return x;
}
DI unsigned scan64(unsigned x) { x = scan32(x); x = dpp_add<0x143, 0xc>(x); return x; }

#define STAMP_DECL
#define STAMP(i)
#define CSTAMP(i)
#define STAMP_OUT(kern, wsp)

constexpr int K1_T = 640;
constexpr int S_CNT = 0, S_OFF = 1024, S_STG = 2048, S_W = 22528, S_H = 38912, S_O = 47104, S_END = 57344;

__global__ __launch_bounds__(K1_T) void k_sort(const int* __restrict__ src, const int* __restrict__ dst,
                                               const uint16_t* __restrict__ ea, const uint16_t* __restrict__ h,
                                               const uint16_t* __restrict__ pos, const uint16_t* __restrict__ we,
                                               char* __restrict__ ws) {
    __shared__ __attribute__((aligned(16))) char sm[S_END];
    const int tid = threadIdx.x, lane = tid & 63;
    const int wv = __builtin_amdgcn_readfirstlane(tid >> 6);
    const int c = blockIdx.x;
    const unsigned smb = (unsigned)(size_t)(LDS_AS char*)sm;
    STAMP_DECL
    STAMP(0);

    const int e0 = c * CE + 4 * tid;
    const u32x4 d4 = *(const u32x4*)(dst + e0);
    const u32x4 s4 = *(const u32x4*)(src + e0);
    const u32x2 a4 = *(const u32x2*)(ea + e0);

    for (int p = wv; p < 21; p += 10) {
        if (p < 16) {
            dma1(smb + S_W + p * 1024, we, p * 1024 + lane * 16);
        } else {
            const int row = (p - 16) * 8 + (lane >> 3);
            const int ch = (lane & 7) ^ ((row >> 1) & 7);
            dma1(smb + S_H + (p - 16) * 1024, h, (c * BN + row) * 128 + ch * 16);
        }
    }
    if (tid < 256) ((unsigned*)(sm + S_CNT))[tid] = 0;
    if (tid < BN) {
        const int n = c * BN + tid;
        f32x4 p = {bf2f(pos[n * 3]), bf2f(pos[n * 3 + 1]), bf2f(pos[n * 3 + 2]), 0.f};
        *(f32x4*)(ws + W_P + (size_t)n * 16) = p;
    }
    if (c == 0 && tid >= 64 && tid < 64 + 9) {
        const u32x4 z = {0, 0, 0, 0};
        if (tid < 72) *(u32x4*)(ws + W_B + (size_t)NN * 128 + (tid - 64) * 16) = z;
        else *(u32x4*)(ws + W_P + (size_t)NN * 16) = z;
    }
    bar();
    STAMP(1);

    unsigned bk[4], rk[4];
#pragma unroll
    for (int i = 0; i < 4; ++i) {
        bk[i] = (d4[i] * 52429u) >> 21;
        rk[i] = atomicAdd((unsigned*)(sm + S_CNT) + bk[i], 1u);
    }
    STAMP(2);
    wait_vm0();
    bar();
    STAMP(3);

    if (wv < 8) {
        const int m = wv >> 2, ct = (wv >> 1) & 1, nt = wv & 1;
        const int r = lane & 31, hh = lane >> 5, blk = (lane >> 4) & 1, q = (lane & 15) >> 2, p = lane & 3;
        f32x16 acc;
#pragma unroll
        for (int i = 0; i < 16; ++i) acc[i] = 0.f;
        const int node = nt * 32 + r;
#pragma unroll
        for (int s = 0; s < 4; ++s) {
            const char* wa = sm + S_W + (64 * m + 16 * s + 8 * hh + q) * 128 + (32 * ct + 16 * blk + 4 * p) * 2;
            const bf16x8 a = cat8(tr_read(wa), tr_read(wa + 4 * 128));
            const bf16x8 b = *(const bf16x8*)(sm + S_H + node * 128 + 16 * ((2 * s + hh) ^ ((node >> 1) & 7)));
            acc = __builtin_amdgcn_mfma_f32_32x32x16_bf16(a, b, acc, 0, 0, 0);
        }
        if (node < BN) {
#pragma unroll
            for (int g = 0; g < 4; ++g) {
                u32x2 o = {pk_bf16(acc[4 * g] * NEG_LOG2E, acc[4 * g + 1] * NEG_LOG2E),
                           pk_bf16(acc[4 * g + 2] * NEG_LOG2E, acc[4 * g + 3] * NEG_LOG2E)};
                *(u32x2*)(sm + S_O + m * (BN * 128) + node * 128 + 16 * ((4 * ct + g) ^ (node & 7)) + 8 * hh) = o;
            }
        }
    } else if (wv == 9) {
        const u32x4 cn = *(const u32x4*)(sm + S_CNT + lane * 16);
        const unsigned tot = cn[0] + cn[1] + cn[2] + cn[3];
        const unsigned ex = scan64(tot) - tot;
        const u32x4 of = {ex, ex + cn[0], ex + cn[0] + cn[1], ex + cn[0] + cn[1] + cn[2]};
        *(u32x4*)(sm + S_OFF + lane * 16) = of;
        unsigned* T = (unsigned*)(ws + W_T);
        const int cs = (c & 7) * 32 + (c >> 3);
#pragma unroll
        for (int i = 0; i < 4; ++i) {
            const int b = 4 * lane + i;
            if (b < NBK) T[b * 256 + cs] = of[i] | (cn[i] << 16);
        }
    }
    STAMP(4);
    bar();
    STAMP(5);

#pragma unroll
    for (int i = 0; i < 4; ++i) {
        const unsigned pos_i = ((const unsigned*)(sm + S_OFF))[bk[i]] + rk[i];
        const unsigned dl = d4[i] - bk[i] * BN;
        const unsigned av = (i & 1) ? (a4[i >> 1] >> 16) : (a4[i >> 1] & 0xffffu);
        u32x2 rec = {s4[i] | (dl << 16), av};
        *(u32x2*)(sm + S_STG + pos_i * 8) = rec;
    }
    {
        const int m = tid / 320, x = tid - m * 320, node = x >> 3, ch = (x & 7) ^ (node & 7);
        const u32x4 v = *(const u32x4*)(sm + S_O + tid * 16);
        *(u32x4*)(ws + (m ? W_B : W_A) + (size_t)(c * BN + node) * 128 + ch * 16) = v;
    }
    bar();
    STAMP(6);
#pragma unroll
    for (int i = 0; i < 2; ++i) {
        const int x = tid + i * K1_T;
        const u32x4 v = *(const u32x4*)(sm + S_STG + x * 16);
        *(u32x4*)(ws + W_RECS + (size_t)c * (CE * 8) + x * 16) = v;
    }
    STAMP(7);
    STAMP(8);
    STAMP_OUT(0, ws);
}

constexpr int RCAP = 3072;
constexpr int L_SLOT = 0;
constexpr int L_STAGE = 65536;
constexpr int L_RECS = 98304;
constexpr int L_AIMG = 123392;
constexpr int L_HIMG = 129536;
constexpr int L_MIMG = 135680;
constexpr int L_PD = 141824;
constexpr int L_OH = 142592;
constexpr int L_BINS = 142848;
constexpr int L_OFFS = 143488;
constexpr int L_WIN = 144128;
constexpr int L_WX = 144192;
constexpr int L_WF = 144448;
constexpr int L_WH = 144512;
constexpr int L_END = 160896;
constexpr unsigned NULL_REC = (unsigned)NN | (8u << 16);

struct Geo { unsigned w0, w1, ua, vb, saddr; bool valid; };

DI void silu_pack(const f32x16& x, unsigned (&pk)[8]) {
#pragma unroll
    for (int i = 0; i < 8; ++i) {
        const f32x2 y = {x[2 * i], x[2 * i + 1]};
        f32x2 d = {__builtin_amdgcn_exp2f(y[0]), __builtin_amdgcn_exp2f(y[1])};
        d = d + 1.0f;
        const f32x2 r = {__builtin_amdgcn_rcpf(d[0]), __builtin_amdgcn_rcpf(d[1])};
        const f32x2 m = y * r;
        pk[i] = __builtin_bit_cast(unsigned, __builtin_convertvector(m, bf16x2));
    }
}
DI bf16x8 as_frag(unsigned a, unsigned b, unsigned c, unsigned d) {
    u32x4 v = {a, b, c, d};
    return __builtin_bit_cast(bf16x8, v);
}

__global__ __launch_bounds__(1024) void k_edge(const uint16_t* __restrict__ h, const uint16_t* __restrict__ pos,
                                               const uint16_t* __restrict__ we, const uint16_t* __restrict__ wx,
                                               const uint16_t* __restrict__ wh, const char* __restrict__ ws,
                                               uint16_t* __restrict__ out) {
    extern __shared__ __attribute__((aligned(16))) char sm[];
    const int tid = threadIdx.x, lane = tid & 63;
    const int wv = __builtin_amdgcn_readfirstlane(tid >> 6);
    const int b = (blockIdx.x & 7) * 32 + (blockIdx.x >> 3);
    if (b >= NBK) return;
    const unsigned smb = (unsigned)(size_t)(LDS_AS char*)sm;
    const u32x2* recs = (const u32x2*)(ws + W_RECS);
    const char* Btab = ws + W_B;
    const char* Ptab = ws + W_P;
    STAMP_DECL
    STAMP(0);

    const int cs = 16 * wv + (lane >> 2);
    const int ch_id = (cs & 31) * 8 + (cs >> 5);
    unsigned tval = ((const unsigned*)(ws + W_T))[b * 256 + cs];

    if (wv < 5) {
        dma1(smb + L_AIMG + wv * 1024, ws + W_A, (b * BN + wv * 8) * 128 + lane * 16);
    } else if (wv < 10) {
        const int row = (wv - 5) * 8 + (lane >> 3);
        dma1(smb + L_HIMG + (wv - 5) * 1024, h, (b * BN + row) * 128 + (((lane & 7) ^ ((row >> 1) & 7)) * 16));
    }
    {
        const int k = wv * 8 + (lane >> 3);
        const int sg = (k & 2) | ((k >> 1) & 4);
        dma1(smb + L_WH + wv * 1024, wh, k * 128 + (((lane & 7) ^ sg) * 16));
    }
    {
        const u32x4 z = {0, 0, 0, 0};
        *(u32x4*)(sm + L_STAGE + wv * 2048 + lane * 16) = z;
        *(u32x4*)(sm + L_STAGE + wv * 2048 + 1024 + lane * 16) = z;
        const u32x4 nr = {NULL_REC, 0, NULL_REC, 0};
        *(u32x4*)(sm + L_RECS + tid * 16) = nr;
        if (tid < (RCAP + 64) / 2 - 1024) *(u32x4*)(sm + L_RECS + (1024 + tid) * 16) = nr;
        if (tid < 160) ((unsigned*)(sm + L_BINS))[tid] = 0;
        if (tid >= 192 && tid < 192 + 48) {
            const int n = tid - 192;
            f32x4 p = {0.f, 0.f, 0.f, 0.f};
            if (n < BN) { const int gn = (b * BN + n) * 3; p[0] = bf2f(pos[gn]); p[1] = bf2f(pos[gn + 1]); p[2] = bf2f(pos[gn + 2]); }
            *(f32x4*)(sm + L_PD + n * 16) = p;
        }
        if (tid >= 256 && tid < 256 + 36) {
            const int x = tid - 256, n = x >> 2, j = x & 3;
            ((unsigned*)(sm + L_OH))[x] = (n < 8 && (n >> 1) == j) ? (0x3F80u << (16 * (n & 1))) : 0u;
        }
        if (tid >= 320 && tid < 320 + 64) ((float*)(sm + L_WX))[tid - 320] = bf2f(wx[tid - 320]) * NEG_LN2;
        if (tid >= 384 && tid < 384 + 64) {
            const int x = tid - 384;
            u32x4 v = z;
            if (x < 16) v = *(const u32x4*)(we + 128 * 64 + x * 8);
            *(u32x4*)(sm + L_AIMG + BN * 128 + x * 16) = v;
        }
    }

    STAMP(1);
    unsigned cnt = ch_id < NCH ? (tval >> 16) : 0u;
    const unsigned incl = scan64((lane & 3) == 0 ? cnt : 0u);
    const unsigned mw = __builtin_amdgcn_readlane(incl, 63);
    bool slow_w = mw > 256;
    unsigned* map = (unsigned*)(sm + L_SLOT + wv * 4096);
    if (!slow_w) {
        const unsigned g0 = ch_id * CE + (tval & 0xffffu), ex = incl - cnt;
        for (unsigned i = lane & 3; i < cnt; i += 4) map[ex + i] = g0 + i;
    }
    if (lane == 0) ((unsigned*)(sm + L_WF))[wv] = slow_w ? 1u : 0u;
    u32x2 rec[4];
    bool have[4];
#pragma unroll
    for (int k = 0; k < 4; ++k) {
        have[k] = !slow_w && (unsigned)(64 * k + lane) < mw;
        rec[k] = u32x2{0, 0};
        if (have[k]) rec[k] = recs[map[64 * k + lane]];
    }
    STAMP(2);
    bar();

    unsigned bin[4], rk[4];
#pragma unroll
    for (int k = 0; k < 4; ++k) {
        bin[k] = ((rec[k][0] >> 19) & 7u) * 32 + (lane & 31);
        rk[k] = 0;
        if (have[k]) rk[k] = atomicAdd((unsigned*)(sm + L_BINS) + bin[k], 1u);
    }
    wait_vm0();
    STAMP(3);
    bar();

    if (wv == 0) {
        const unsigned* bins = (const unsigned*)(sm + L_BINS);
        const int j = lane & 31;
        unsigned c5[5], in5[5], start = 0;
#pragma unroll
        for (int w = 0; w < 5; ++w) { c5[w] = bins[w * 32 + j]; in5[w] = scan32(c5[w]); }
#pragma unroll
        for (int w = 0; w < 5; ++w) {
            const unsigned tot = __builtin_amdgcn_readlane(in5[w], 31);
            const unsigned tiles = (tot + 31) >> 5;
            ((unsigned*)(sm + L_OFFS))[w * 32 + j] = start + in5[w] - c5[w];
            if (lane == 0) { ((unsigned*)(sm + L_WIN))[w] = start; ((unsigned*)(sm + L_WIN))[5 + w] = tiles; }
            start += tiles * 32;
        }
        const bool any_slow = __any(((const unsigned*)(sm + L_WF))[lane & 15] != 0);
        if (lane == 0) ((unsigned*)(sm + L_WIN))[10] = (any_slow || start > RCAP) ? 1u : 0u;
    }
    bar();
    STAMP(4);
    const bool slow = __builtin_amdgcn_readfirstlane(((const unsigned*)(sm + L_WIN))[10]) != 0;

    const int r = lane & 31, hh = lane >> 5;
    const int win = wv < 4 ? 4 : (wv - 4) / 3;
    char* part = sm + L_SLOT + wv * 4096;

    if (!slow) {
#pragma unroll
        for (int k = 0; k < 4; ++k) {
            if (have[k]) {
                const unsigned p = ((const unsigned*)(sm + L_OFFS))[bin[k]] + rk[k];
                u32x2 v = {rec[k][0] & 0x0007ffffu, rec[k][1]};
                *(u32x2*)(sm + L_RECS + p * 8) = v;
            }
        }
        bar();
        STAMP(5);

        const int nw = wv < 4 ? 4 : 3, jw = wv < 4 ? wv : (wv - 4) - 3 * win;
        const int wstart = __builtin_amdgcn_readfirstlane(((const unsigned*)(sm + L_WIN))[win]);
        const int wtiles = __builtin_amdgcn_readfirstlane(((const unsigned*)(sm + L_WIN))[5 + win]);
        const int ta = (wtiles * jw) / nw, tb = (wtiles * (jw + 1)) / nw;

        const int blk = (lane >> 4) & 1, q = (lane & 15) >> 2, p = lane & 3;
        bf16x8 e0, e1;
        {
            const int row = hh ? BN + q : 8 * win + q;
            const char* ab = sm + L_AIMG + row * 128 + (16 * blk + 4 * p) * 2;
            e0 = cat8(tr_read(ab), tr_read(ab + 512));
            e1 = cat8(tr_read(ab + 64), tr_read(ab + 64 + 512));
        }
        bf16x8 i0, i1;
#pragma unroll
        for (int j = 0; j < 8; ++j) {
            i0[j] = (r == 8 * hh + j) ? (short)0x3F80 : (short)0;
            i1[j] = (r == 16 + 8 * hh + j) ? (short)0x3F80 : (short)0;
        }
        const int rho = 8 * (r & 3) + (r >> 2);
        const int fsw = (r & 3) ^ (((r >> 3) & 1) << 2);
        const char* aad[4];
#pragma unroll
        for (int s = 0; s < 4; ++s) aad[s] = sm + L_SLOT + wv * 4096 + rho * 128 + 16 * ((2 * s + hh) ^ fsw);
        unsigned kk[4];
#pragma unroll
        for (int i = 0; i < 4; ++i)
            kk[i] = 16 * ((lane & 7) ^ i ^ (((lane >> 4) & 1) << 2)) + (3072 - 1024 * i);
        const char* gB = Btab - 3072;
        const unsigned slot_a = smb + L_SLOT + wv * 4096;
        char* stage = sm + L_STAGE + wv * 2048;
        const char* trb = stage + (4 * hh + q) * 64 + blk * 32 + p * 8;
        const char* pdw = sm + L_PD + win * 128;

        f32x16 z0, z1;
#pragma unroll
        for (int i = 0; i < 16; ++i) { z0[i] = 0.f; z1[i] = 0.f; }

        auto issue_rows = [&](int rec_index) {
            const unsigned* rp = (const unsigned*)(sm + L_RECS + (rec_index + 4 * (lane >> 3)) * 8);
            const unsigned s0 = rp[0], s1 = rp[2], s2 = rp[4], s3 = rp[6];
            wait_lgkm0();
            dma4(slot_a, gB, (s0 & 0xffffu) * 128 + kk[0], (s1 & 0xffffu) * 128 + kk[1],
                 (s2 & 0xffffu) * 128 + kk[2], (s3 & 0xffffu) * 128 + kk[3]);
        };
        u32x2 rr;
        f32x4 ps;
        auto tile = [&](unsigned u, unsigned w0, unsigned w1, char* saddr, bool wr, int next_rec, bool has_next, bool next_pair) {
            wait_vm0();
            const bf16x8 a0 = *(const bf16x8*)aad[0], a1 = *(const bf16x8*)aad[1];
            const bf16x8 a2 = *(const bf16x8*)aad[2], a3 = *(const bf16x8*)aad[3];
            u32x4 ex = *(const u32x4*)(sm + (hh ? (unsigned)L_OH : u));
            ex[0] = hh ? u : ex[0];
            if (wr) *(u32x2*)saddr = u32x2{w0, w1};
            const bf16x8 wf0 = cat8(tr_read(trb), tr_read(trb + 512));
            const bf16x8 wf1 = cat8(tr_read(trb + 1024), tr_read(trb + 1536));
            if (wr) *(u32x2*)saddr = u32x2{0, 0};
            if (has_next) {
                if (next_pair) rr = *(const u32x2*)(sm + L_RECS + (next_rec + lane) * 8);
                issue_rows(next_rec);
                if (next_pair) ps = *(const f32x4*)(Ptab + (rr[0] & 0xffffu) * 16);
            }
            const bf16x8 ef = __builtin_bit_cast(bf16x8, ex);
            f32x16 zero;
#pragma unroll
            for (int i = 0; i < 16; ++i) zero[i] = 0.f;
            unsigned pk[8];
            f32x16 x = __builtin_amdgcn_mfma_f32_32x32x16_bf16(a0, i0, zero, 0, 0, 0);
            x = __builtin_amdgcn_mfma_f32_32x32x16_bf16(a1, i1, x, 0, 0, 0);
            x = __builtin_amdgcn_mfma_f32_32x32x16_bf16(ef, e0, x, 0, 0, 0);
            f32x16 y = __builtin_amdgcn_mfma_f32_32x32x16_bf16(a2, i0, zero, 0, 0, 0);
            y = __builtin_amdgcn_mfma_f32_32x32x16_bf16(a3, i1, y, 0, 0, 0);
            y = __builtin_amdgcn_mfma_f32_32x32x16_bf16(ef, e1, y, 0, 0, 0);
            silu_pack(x, pk);
            z0 = __builtin_amdgcn_mfma_f32_32x32x16_bf16(as_frag(pk[0], pk[1], pk[2], pk[3]), wf0, z0, 0, 0, 0);
            z0 = __builtin_amdgcn_mfma_f32_32x32x16_bf16(as_frag(pk[4], pk[5], pk[6], pk[7]), wf1, z0, 0, 0, 0);
            silu_pack(y, pk);
            z1 = __builtin_amdgcn_mfma_f32_32x32x16_bf16(as_frag(pk[0], pk[1], pk[2], pk[3]), wf0, z1, 0, 0, 0);
            z1 = __builtin_amdgcn_mfma_f32_32x32x16_bf16(as_frag(pk[4], pk[5], pk[6], pk[7]), wf1, z1, 0, 0, 0);
        };

        STAMP(6);
        CSTAMP(12);
        if (ta < tb) {
            int base = wstart + 32 * ta;
            const int end = wstart + 32 * tb;
            rr = *(const u32x2*)(sm + L_RECS + (base + lane) * 8);
            issue_rows(base);
            ps = *(const f32x4*)(Ptab + (rr[0] & 0xffffu) * 16);
            while (true) {
                const unsigned n = (rr[0] >> 16) & 15u;
                const f32x4 pd = *(const f32x4*)(pdw + n * 16);
                const float dx = pd[0] - ps[0], dy = pd[1] - ps[1], dz = pd[2] - ps[2];
                const float d2 = (dx * dx + dy * dy + dz * dz) * NEG_LOG2E;
                const unsigned w0 = pk_bf16(1.0f, dx), w1 = pk_bf16(dy, dz);
                const unsigned ext = pk_bf16(d2, bf2f(rr[1]) * NEG_LOG2E);
                const auto sw = __builtin_amdgcn_permlane32_swap(L_OH + n * 16, ext, false, false);
                char* saddr = stage + r * 64 + n * 8;
                const bool valid = n < 8;
                const bool hasb = base + 32 < end, hasn = base + 64 < end;
                tile(sw[0], w0, w1, saddr, valid && hh == 0, base + 32, hasb, false);
                if (!hasb) break;
                tile(sw[1], w0, w1, saddr, valid && hh == 1, base + 64, hasn, true);
                if (!hasn) break;
                base += 64;
            }
        }

        CSTAMP(13);
        STAMP(7);
        float cs0 = 0.f, cs1 = 0.f;
#pragma unroll
        for (int g = 0; g < 4; ++g) {
            const f32x4 w0 = *(const f32x4*)(sm + L_WX + (8 * g + 4 * hh) * 4);
            const f32x4 w1 = *(const f32x4*)(sm + L_WX + (32 + 8 * g + 4 * hh) * 4);
#pragma unroll
            for (int i = 0; i < 4; ++i) { cs0 += z0[4 * g + i] * w0[i]; cs1 += z1[4 * g + i] * w1[i]; }
        }
        const float csh = cs0 + cs1;
        const auto cw = __builtin_amdgcn_permlane32_swap(__float_as_uint(csh), __float_as_uint(csh), false, false);
        const float cst = __uint_as_float(cw[0]) + __uint_as_float(cw[1]);
        if (hh == 0) ((float*)(part + 2048))[r] = cst;
        if ((lane & 3) == 0) {
            const int n = r >> 2;
#pragma unroll
            for (int g = 0; g < 4; ++g) {
                *(f32x4*)(part + n * 256 + (8 * g + 4 * hh) * 4) = f32x4{z0[4 * g], z0[4 * g + 1], z0[4 * g + 2], z0[4 * g + 3]};
                *(f32x4*)(part + n * 256 + (32 + 8 * g + 4 * hh) * 4) = f32x4{z1[4 * g], z1[4 * g + 1], z1[4 * g + 2], z1[4 * g + 3]};
            }
        }
    } else {
        {
            const u32x4 z = {0, 0, 0, 0};
            *(u32x4*)(part + lane * 16) = z;
            *(u32x4*)(part + 1024 + lane * 16) = z;
            if (lane < 8) *(u32x4*)(part + 2048 + lane * 16) = z;
        }
        bar();
        const float wd = bf2f(we[128 * 64 + lane]), wa = bf2f(we[129 * 64 + lane]);
        const float wxl = ((const float*)(sm + L_WX))[lane];
        const unsigned* T = (const unsigned*)(ws + W_T) + b * 256;
        for (int sidx = wv; sidx < 256; sidx += 16) {
            const int cid = (sidx & 31) * 8 + (sidx >> 5);
            if (cid >= NCH) continue;
            const unsigned tv = T[sidx];
            const unsigned g0 = cid * CE + (tv & 0xffffu), cn = tv >> 16;
            for (unsigned i = 0; i < cn; ++i) {
                const u32x2 rc = recs[g0 + i];
                const unsigned s = rc[0] & 0xffffu, dl = rc[0] >> 16, w = dl >> 3, n = dl & 7;
                const f32x4 ps = *(const f32x4*)(Ptab + s * 16);
                const f32x4 pd = *(const f32x4*)(sm + L_PD + dl * 16);
                const float dx = pd[0] - ps[0], dy = pd[1] - ps[1], dz = pd[2] - ps[2];
                const unsigned e = pk_bf16((dx * dx + dy * dy + dz * dz) * NEG_LOG2E, bf2f(rc[1]) * NEG_LOG2E);
                const unsigned dxy = pk_bf16(dx, dy), dz0 = pk_bf16(dz, 0.f);
                float y = bf2f(((const uint16_t*)(sm + L_AIMG))[dl * 64 + lane]) + bf2f(((const uint16_t*)Btab)[s * 64 + lane]);
                y += bf2f(e & 0xffffu) * wd + bf2f(e >> 16) * wa;
                const float m = bf2f(pk_bf16(y * __builtin_amdgcn_rcpf(1.0f + __builtin_amdgcn_exp2f(y)), 0.f) & 0xffffu);
                char* tgt = sm + L_SLOT + (w == 4 ? 0 : 4 + 3 * w) * 4096;
                atomicAdd((float*)(tgt + n * 256) + lane, m);
                float t = m * wxl;
                for (int o = 32; o > 0; o >>= 1) t += __shfl_xor(t, o);
                if (lane < 3) {
                    const float dv = lane == 0 ? bf2f(dxy & 0xffffu) : lane == 1 ? bf2f(dxy >> 16) : bf2f(dz0 & 0xffffu);
                    atomicAdd((float*)(tgt + 2048) + 4 * n + 1 + lane, dv * t);
                }
            }
        }
    }
    STAMP(8);
    bar();
    STAMP(9);

    if (tid < 640) {
        const int nl = tid >> 4, f4 = (tid & 15) * 4, w = nl >> 3, n = nl & 7;
        const int w0 = w == 4 ? 0 : 4 + 3 * w;
        const char* pb = sm + L_SLOT + w0 * 4096 + n * 256 + f4 * 4;
        f32x4 s = *(const f32x4*)pb + *(const f32x4*)(pb + 4096) + *(const f32x4*)(pb + 8192);
        if (w == 4) s += *(const f32x4*)(pb + 12288);
        s *= NEG_LN2;
        u32x2 o = {pk_bf16(s[0], s[1]), pk_bf16(s[2], s[3])};
        *(u32x2*)(sm + L_MIMG + nl * 128 + 16 * ((f4 >> 3) ^ ((nl >> 1) & 7)) + 8 * ((f4 >> 2) & 1)) = o;
    } else if (tid < 640 + 3 * BN) {
        const int x = tid - 640, nl = x / 3, cc = x - 3 * nl, w = nl >> 3, n = nl & 7;
        const int w0 = w == 4 ? 0 : 4 + 3 * w;
        const char* pb = sm + L_SLOT + w0 * 4096 + 2048 + (4 * n + 1 + cc) * 4;
        float s = *(const float*)pb + *(const float*)(pb + 4096) + *(const float*)(pb + 8192);
        if (w == 4) s += *(const float*)(pb + 12288);
        const float p0 = ((const float*)(sm + L_PD))[nl * 4 + cc];
        out[NN * 64 + (b * BN + nl) * 3 + cc] = (uint16_t)(pk_bf16(p0 + s, 0.f) & 0xffffu);
    }
    bar();
    STAMP(10);

    if (wv < 12) {
        const int rt = wv >> 2, ct = wv & 3;
        const int g = lane >> 4, q = (lane & 15) >> 2, p = lane & 3, node = 16 * rt + (lane & 15);
        f32x4 acc = {0.f, 0.f, 0.f, 0.f};
#pragma unroll
        for (int s = 0; s < 4; ++s) {
            const int k0 = 32 * s + 8 * g + q, k1 = k0 + 4;
            const int chn = 2 * ct + (p >> 1);
            const char* a0 = sm + L_WH + k0 * 128 + 16 * (chn ^ ((k0 & 2) | ((k0 >> 1) & 4))) + 8 * (p & 1);
            const char* a1 = sm + L_WH + k1 * 128 + 16 * (chn ^ ((k1 & 2) | ((k1 >> 1) & 4))) + 8 * (p & 1);
            const bf16x8 a = cat8(tr_read(a0), tr_read(a1));
            const char* img = sm + (s < 2 ? L_HIMG : L_MIMG) + node * 128;
            const bf16x8 bb = *(const bf16x8*)(img + 16 * ((4 * (s & 1) + g) ^ ((node >> 1) & 7)));
            acc = __builtin_amdgcn_mfma_f32_16x16x32_bf16(a, bb, acc, 0, 0, 0);
        }
        if (node < BN) {
            const u32x2 hv = *(const u32x2*)(sm + L_HIMG + node * 128 + 16 * ((2 * ct + (g >> 1)) ^ ((node >> 1) & 7)) + 8 * (g & 1));
            const float o0 = bf2f(hv[0] & 0xffffu) + silu_f(acc[0]), o1 = bf2f(hv[0] >> 16) + silu_f(acc[1]);
            const float o2 = bf2f(hv[1] & 0xffffu) + silu_f(acc[2]), o3 = bf2f(hv[1] >> 16) + silu_f(acc[3]);
            *(u32x2*)(out + (b * BN + node) * 64 + 16 * ct + 4 * g) = u32x2{pk_bf16(o0, o1), pk_bf16(o2, o3)};
        }
    }
    STAMP(11);
    STAMP_OUT(1, ws);
}

}

extern "C" void kernel_launch(void* const* d_in, const int* in_sizes, int n_in, void* d_out, int out_size, void* d_ws,
                              size_t ws_size, hipStream_t stream) {
    using namespace egnn;
    const uint16_t* h = (const uint16_t*)d_in[0];
    const uint16_t* pos = (const uint16_t*)d_in[1];
    const uint16_t* ea = (const uint16_t*)d_in[2];
    const int* src = (const int*)d_in[3];
    const int* dst = (const int*)d_in[4];
    const uint16_t* we = (const uint16_t*)d_in[5];
    const uint16_t* wx = (const uint16_t*)d_in[6];
    const uint16_t* wh = (const uint16_t*)d_in[7];
    (void)hipFuncSetAttribute((const void*)k_edge, hipFuncAttributeMaxDynamicSharedMemorySize, L_END);
    k_sort<<<NCH, K1_T, 0, stream>>>(src, dst, ea, h, pos, we, (char*)d_ws);
    k_edge<<<256, 1024, L_END, stream>>>(h, pos, we, wx, wh, (const char*)d_ws, (uint16_t*)d_out);
}
```

```cpp
#include <hip/hip_runtime.h>
#include <stdint.h>

namespace egnn {

typedef short    bf16x8 __attribute__((ext_vector_type(8)));
typedef short    s16x4  __attribute__((ext_vector_type(4)));
typedef float    f32x16 __attribute__((ext_vector_type(16)));
typedef float    f32x4  __attribute__((ext_vector_type(4)));
typedef float    f32x2  __attribute__((ext_vector_type(2)));
typedef __bf16   bf16x2 __attribute__((ext_vector_type(2)));
typedef unsigned u32x4  __attribute__((ext_vector_type(4)));
typedef unsigned u32x2  __attribute__((ext_vector_type(2)));
#define LDS_AS __attribute__((address_space(3)))
#define DI __device__ __forceinline__
#define SBAR() __builtin_amdgcn_sched_barrier(0)
#define PIN(x) asm volatile("" : "+v"(x))
#define MFMA32(a, b, c) __builtin_amdgcn_mfma_f32_32x32x16_bf16(a, b, c, 0, 0, 0)
#define MFMA16(a, b, c) __builtin_amdgcn_mfma_f32_16x16x32_bf16(a, b, c, 0, 0, 0)

constexpr int NN = 10000;
constexpr int NE = 640000;
constexpr int BN = 40;
constexpr int NBK = 250;
constexpr int CE = 2560;
constexpr int NCH = 250;
constexpr float NEG_LOG2E = -1.4426950408889634f;
constexpr float NEG_LN2 = -0.6931471805599453f;

constexpr size_t W_R32 = 0;
constexpr size_t W_R8  = 3u << 20;
constexpr size_t W_T   = 4u << 20;
constexpr size_t W_B   = (4u << 20) + (512u << 10);
constexpr size_t W_P   = 6u << 20;
constexpr size_t W_C   = (6u << 20) + (512u << 10);

DI unsigned win_of(unsigned dl) { return (dl * 39u) >> 8; }
DI unsigned win_start(unsigned w) { return (w * 841u + 127u) >> 7; }

DI float bf2f(unsigned v16) { return __uint_as_float(v16 << 16); }
DI unsigned pk_bf16(float lo, float hi) {
    f32x2 x = {lo, hi};
    return __builtin_bit_cast(unsigned, __builtin_convertvector(x, bf16x2));
}
DI float silu_f(float x) {
    return x * __builtin_amdgcn_rcpf(1.0f + __builtin_amdgcn_exp2f(x * NEG_LOG2E));
}
DI s16x4 tr_read(const char* p) {
    return __builtin_amdgcn_ds_read_tr16_b64_v4i16((LDS_AS s16x4*)p);
}
DI bf16x8 cat8(s16x4 a, s16x4 b) { return __builtin_shufflevector(a, b, 0, 1, 2, 3, 4, 5, 6, 7); }
DI void wait_vm0() { asm volatile("s_waitcnt vmcnt(0)" ::: "memory"); }
DI void wait_lgkm0() { asm volatile("s_waitcnt lgkmcnt(0)" ::: "memory"); }
DI void bar() { asm volatile("s_waitcnt lgkmcnt(0)\n\ts_barrier" ::: "memory"); }

DI void dma1(unsigned ldsaddr, const void* gbase, unsigned voff) {
    unsigned keep;
    asm volatile("s_mov_b32 %0, m0\n\ts_mov_b32 m0, %1\n\ts_nop 0\n\t"
                 "global_load_lds_dwordx4 %2, %3\n\t"
                 "s_mov_b32 m0, %0"
                 : "=&s"(keep) : "s"(ldsaddr), "v"(voff), "s"(gbase) : "memory");
}
DI void dma4(unsigned ldsaddr, const void* gbase, unsigned v0, unsigned v1, unsigned v2, unsigned v3) {
    unsigned keep;
    asm volatile("s_mov_b32 %0, m0\n\ts_mov_b32 m0, %1\n\ts_nop 0\n\t"
                 "global_load_lds_dwordx4 %2, %6\n\t"
                 "global_load_lds_dwordx4 %3, %6 offset:1024\n\t"
                 "global_load_lds_dwordx4 %4, %6 offset:2048\n\t"
                 "global_load_lds_dwordx4 %5, %6 offset:3072\n\t"
                 "s_mov_b32 m0, %0"
                 : "=&s"(keep) : "s"(ldsaddr), "v"(v0), "v"(v1), "v"(v2), "v"(v3), "s"(gbase) : "memory");
}
template <typename T> DI void st_nt(void* p, T v) { __builtin_nontemporal_store(v, (T*)p); }
template <int CTRL, int ROWMASK> DI unsigned dpp_add(unsigned x) {
    return x + (unsigned)__builtin_amdgcn_update_dpp(0, (int)x, CTRL, ROWMASK, 0xf, true);
}
DI unsigned scan32(unsigned x) {
    x = dpp_add<0x111, 0xf>(x); x = dpp_add<0x112, 0xf>(x); x = dpp_add<0x114, 0xf>(x); x = dpp_add<0x118, 0xf>(x);
    x = dpp_add<0x142, 0xa>(x);
    return x;
}
DI unsigned scan64(unsigned x) { x = scan32(x); x = dpp_add<0x143, 0xc>(x); return x; }
DI int wswz(int k) { return (k & 2) | ((k >> 1) & 4); }
DI bf16x8 wfrag16(const char* img, int s, int ct, int lane) {
    const int g = lane >> 4, q = (lane & 15) >> 2, p = lane & 3;
    const int k0 = 32 * s + 8 * g + q, k1 = k0 + 4, chn = 2 * ct + (p >> 1);
    return cat8(tr_read(img + k0 * 128 + 16 * (chn ^ wswz(k0)) + 8 * (p & 1)),
                tr_read(img + k1 * 128 + 16 * (chn ^ wswz(k1)) + 8 * (p & 1)));
}
DI bf16x8 nfrag16(const char* img, int node, int chunk) {
    return *(const bf16x8*)(img + node * 128 + 16 * (chunk ^ ((node >> 1) & 7)));
}

#define PSTAMP(i)
#define QSTAMP(i)
#define STAMP_DECL
#define STAMP(i)
#define CSTAMP(i)
#define STAMP_OUT(kern, wsp)

constexpr int K1_T = 640;
constexpr int S_CNT = 0, S_OFF = 1024, S_R32 = 2048, S_R8 = 12288, S_W = 14848, S_H = 23040, S_O = 29184, S_END = 34304;

__global__ __launch_bounds__(K1_T) void k_sort(const int* __restrict__ src, const int* __restrict__ dst,
                                               const uint16_t* __restrict__ ea, const uint16_t* __restrict__ h,
                                               const uint16_t* __restrict__ pos, const uint16_t* __restrict__ we,
                                               const uint16_t* __restrict__ wx, char* __restrict__ ws) {
    __shared__ __attribute__((aligned(16))) char sm[S_END];
    const int tid = threadIdx.x, lane = tid & 63;
    const int wv = __builtin_amdgcn_readfirstlane(tid >> 6);
    const int c = blockIdx.x;
    asm volatile("" :: "s"(src), "s"(dst), "s"(ea), "s"(h), "s"(pos), "s"(we), "s"(wx), "s"(ws));
    const unsigned smb = (unsigned)(size_t)(LDS_AS char*)sm;
    STAMP_DECL
    STAMP(0);

    const int e0 = c * CE + 4 * tid;
    const u32x4 d4 = *(const u32x4*)(dst + e0);
    const u32x4 s4 = *(const u32x4*)(src + e0);
    const u32x2 a4 = *(const u32x2*)(ea + e0);
    const int pn = c * BN + (lane < BN ? lane : 0);
    const unsigned px = pos[pn * 3], py = pos[pn * 3 + 1], pz = pos[pn * 3 + 2];

    {
        const int k = (wv & 7) * 8 + (lane >> 3);
        const int row = (wv - 5) * 8 + (lane >> 3);
        if (wv < 8) dma1(smb + S_W + wv * 1024, we, (64 + k) * 128 + (((lane & 7) ^ wswz(k)) * 16));
        if (wv >= 5) dma1(smb + S_H + (wv - 5) * 1024, h, (c * BN + row) * 128 + (((lane & 7) ^ ((row >> 1) & 7)) * 16));
    }
    if (tid < 256) ((unsigned*)(sm + S_CNT))[tid] = 0;
    bar();
    STAMP(1);

    unsigned bk[4], rk[4];
#pragma unroll
    for (int i = 0; i < 4; ++i) {
        bk[i] = (d4[i] * 52429u) >> 21;
        rk[i] = atomicAdd((unsigned*)(sm + S_CNT) + bk[i], 1u);
    }
    unsigned char code[4];
#pragma unroll
    for (int i = 0; i < 4; ++i) {
        const unsigned dl = d4[i] - bk[i] * BN, w = win_of(dl);
        code[i] = (unsigned char)(w * 8 + dl - win_start(w));
    }
    STAMP(2);
    wait_vm0();
    bar();
    STAMP(3);

    if (wv == 9) {
        const u32x4 cn = *(const u32x4*)(sm + S_CNT + lane * 16);
        const unsigned tot = cn[0] + cn[1] + cn[2] + cn[3];
        const unsigned ex = scan64(tot) - tot;
        const u32x4 of = {ex, ex + cn[0], ex + cn[0] + cn[1], ex + cn[0] + cn[1] + cn[2]};
        *(u32x4*)(sm + S_OFF + lane * 16) = of;
        char* T = ws + W_T + ((c & 7) * 32 + (c >> 3)) * 4 + lane * 4096;
#pragma unroll
        for (int i = 0; i < 4; ++i)
            if (4 * lane + i < NBK) st_nt(T + i * 1024, of[i] | (cn[i] << 16));
    } else if (wv == 8) {
        if (lane < BN) st_nt(ws + W_P + (size_t)pn * 16, f32x4{bf2f(px), bf2f(py), bf2f(pz), 0.f});
        if (c == 0) {
            const u32x4 z = {0, 0, 0, 0};
            st_nt(ws + W_P + (size_t)NN * 16 + lane * 16, z);
            if (lane < 8) st_nt(ws + W_B + (size_t)NN * 128 + lane * 16, z);
            st_nt(ws + W_C + lane * 16, lane < 16 ? *(const u32x4*)(we + 128 * 64 + lane * 8) : z);
            const int n = lane >> 2, j = lane & 3;
            st_nt(ws + W_C + 1024 + lane * 4, (n < 8 && (n >> 1) == j) ? (0x3F80u << (16 * (n & 1))) : 0u);
            st_nt(ws + W_C + 1280 + lane * 4, bf2f(wx[lane]) * NEG_LN2);
        }
    }
    STAMP(4);
    bar();
    STAMP(5);

#pragma unroll
    for (int i = 0; i < 4; ++i) {
        const unsigned p = ((const unsigned*)(sm + S_OFF))[bk[i]] + rk[i];
        const unsigned av = (i & 1) ? (a4[i >> 1] & 0xffff0000u) : (a4[i >> 1] << 16);
        ((unsigned*)(sm + S_R32))[p] = s4[i] | av;
        ((unsigned char*)(sm + S_R8))[p] = code[i];
    }
    bar();
    STAMP(6);
    st_nt(ws + W_R32 + (size_t)c * (CE * 4) + tid * 16, *(const u32x4*)(sm + S_R32 + tid * 16));
    if (tid < CE / 16) st_nt(ws + W_R8 + (size_t)c * CE + tid * 16, *(const u32x4*)(sm + S_R8 + tid * 16));
    STAMP(7);

    if (wv < 8) {
        const int ct = wv & 3, g = lane >> 4;
        const bf16x8 a0 = wfrag16(sm + S_W, 0, ct, lane), a1 = wfrag16(sm + S_W, 1, ct, lane);
        for (int nt = wv >> 2; nt < 3; nt += 2) {
            const int node = 16 * nt + (lane & 15);
            f32x4 acc = {0.f, 0.f, 0.f, 0.f};
            acc = MFMA16(a0, nfrag16(sm + S_H, node, g), acc);
            acc = MFMA16(a1, nfrag16(sm + S_H, node, 4 + g), acc);
            const u32x2 o = {pk_bf16(acc[0] * NEG_LOG2E, acc[1] * NEG_LOG2E), pk_bf16(acc[2] * NEG_LOG2E, acc[3] * NEG_LOG2E)};
            if (node < BN) *(u32x2*)(sm + S_O + node * 128 + 16 * ((2 * ct + (g >> 1)) ^ (node & 7)) + 8 * (g & 1)) = o;
        }
    }
    bar();
    if (tid < BN * 8) {
        const int node = tid >> 3, ch = (tid & 7) ^ (node & 7);
        st_nt(ws + W_B + (size_t)(c * BN + node) * 128 + ch * 16, *(const u32x4*)(sm + S_O + tid * 16));
    }
    STAMP(8);
    STAMP_OUT(0, ws);
}

constexpr int K2_W = 12, K2_T = 64 * K2_W;
constexpr int RCAP = 3072;
constexpr int L_SLOT = 0;
constexpr int L_STAGE = 49152;
constexpr int RALL = RCAP + 128;
constexpr int L_R32 = 73728;
constexpr int L_R8 = L_R32 + RALL * 4;
constexpr int L_AIMG = L_R8 + RALL;
constexpr int L_HIMG = L_AIMG + 6144;
constexpr int L_MIMG = L_HIMG + 6144;
constexpr int L_W1 = L_MIMG + 6144;
constexpr int L_WH = L_W1 + 8192;
constexpr int L_PD = L_WH + 16384;
constexpr int L_OH = L_PD + 1024;
constexpr int L_WX = L_OH + 256;
constexpr int L_BINS = L_OH + 1024;
constexpr int L_OFFS = L_BINS + 896;
constexpr int L_WIN = L_OFFS + 896;
constexpr int L_WF = L_WIN + 64;
constexpr int L_PB = L_WF + 64;
constexpr int L_END = L_PB + K2_W * 1024;
constexpr unsigned MAPCAP = 320;

DI bf16x8 as_frag(unsigned a, unsigned b, unsigned c, unsigned d) {
    u32x4 v = {a, b, c, d};
    return __builtin_bit_cast(bf16x8, v);
}
DI float ex2(float x) { return __builtin_amdgcn_exp2f(x); }
DI float rcp(float x) { return __builtin_amdgcn_rcpf(x); }

template <class H1, class H2, class H3>
DI void half_tile(const f32x16& xc, f32x16& xn, const bf16x8& aA, const bf16x8& aB, const bf16x8& ef, const bf16x8& eB,
                  const bf16x8& i0, const bf16x8& i1, f32x16& zp, u32x4& pkp, const bf16x8& wp, f32x16& zc,
                  const bf16x8& wc, H1&& hook1, H2&& hook2, H3&& hook3) {
    f32x16 zero;
#pragma unroll
    for (int i = 0; i < 16; ++i) zero[i] = 0.f;
    f32x2 e0, e1, e2, e3;
    u32x4 pk;
#define EXP2(e, i) do { e[0] = ex2(xc[i]); PIN(e[0]); } while (0)
#define EXP2H(e, i) do { e[1] = ex2(xc[i]); PIN(e[1]); } while (0)
#define RCP2(e) do { e[0] = rcp(e[0]); PIN(e[0]); } while (0)
#define RCP2H(e) do { e[1] = rcp(e[1]); PIN(e[1]); } while (0)
#define PKMODE 0
#define ADD1(e) do { e = e + 1.0f; PIN(e); } while (0)
#define MULX(e, i) do { e = e * f32x2{xc[i], xc[i + 1]}; PIN(e); } while (0)
#define CVT(d, e) do { d = __builtin_bit_cast(unsigned, __builtin_convertvector(e, bf16x2)); PIN(d); } while (0)
    xn = MFMA32(aA, i0, zero); SBAR();
    EXP2(e0, 0); EXP2H(e0, 1); EXP2(e1, 2); SBAR();
    zp = MFMA32(__builtin_bit_cast(bf16x8, pkp), wp, zp); SBAR();
    EXP2H(e1, 3); EXP2(e2, 4); EXP2H(e2, 5); EXP2(e3, 6); EXP2H(e3, 7); SBAR();
    ADD1(e0); ADD1(e1); ADD1(e2); ADD1(e3); SBAR();
    hook1(); SBAR();
    xn = MFMA32(aB, i1, xn); SBAR();
    RCP2(e0); RCP2H(e0); RCP2(e1); SBAR();
    xn = MFMA32(ef, eB, xn); SBAR();
    RCP2H(e1); RCP2(e2); RCP2H(e2); RCP2(e3); RCP2H(e3); SBAR();
    MULX(e0, 0); MULX(e1, 2); MULX(e2, 4); MULX(e3, 6); SBAR();
    CVT(pk[0], e0); CVT(pk[1], e1); CVT(pk[2], e2); CVT(pk[3], e3); SBAR();
    EXP2(e0, 8); EXP2H(e0, 9); SBAR();
    zc = MFMA32(__builtin_bit_cast(bf16x8, pk), wc, zc); SBAR();
    EXP2(e1, 10); EXP2H(e1, 11); EXP2(e2, 12); EXP2H(e2, 13); EXP2(e3, 14); EXP2H(e3, 15); SBAR();
    ADD1(e0); ADD1(e1); ADD1(e2); ADD1(e3); SBAR();
    hook2(); SBAR();
    RCP2(e0); RCP2H(e0); RCP2(e1); RCP2H(e1); RCP2(e2); RCP2H(e2); RCP2(e3); RCP2H(e3); SBAR();
    hook3(); SBAR();
    MULX(e0, 8); MULX(e1, 10); MULX(e2, 12); MULX(e3, 14); SBAR();
    CVT(pkp[0], e0); CVT(pkp[1], e1); CVT(pkp[2], e2); CVT(pkp[3], e3); SBAR();
#undef EXP2
#undef EXP2H
#undef RCP2
#undef RCP2H
#undef ADD1
#undef MULX
#undef CVT
}

__global__ __launch_bounds__(K2_T) void k_edge(const uint16_t* __restrict__ h, const uint16_t* __restrict__ we,
                                               const uint16_t* __restrict__ wh, const char* __restrict__ ws,
                                               uint16_t* __restrict__ out) {
    extern __shared__ __attribute__((aligned(16))) char sm[];
    const int tid = threadIdx.x, lane = tid & 63;
    const int wv = __builtin_amdgcn_readfirstlane(tid >> 6);
    const int b = (blockIdx.x & 7) * 32 + (blockIdx.x >> 3);
    if (b >= NBK) return;
    const unsigned smb = (unsigned)(size_t)(LDS_AS char*)sm;
    asm volatile("" :: "s"(h), "s"(we), "s"(wh), "s"(ws), "s"(out));
    const unsigned* R32 = (const unsigned*)(ws + W_R32);
    const unsigned char* R8 = (const unsigned char*)(ws + W_R8);
    const char* Btab = ws + W_B;
    const char* Ptab = ws + W_P;
    STAMP_DECL
    STAMP(0);

    const int l3 = (lane * 171) >> 9, sub = lane - 3 * l3;
    const int cid = 21 * wv + l3;
    const bool cok = lane < 63 && cid < NCH;
    const int cidc = cok ? cid : 0;
    unsigned tval;
    asm volatile("global_load_dword %0, %1, %2" : "=v"(tval)
                 : "v"((unsigned)W_T + (b * 256 + (cidc & 7) * 32 + (cidc >> 3)) * 4), "s"(ws) : "memory");

#pragma unroll
    for (int j = 0; j < 3; ++j) {
        const int p0 = wv + K2_W * j, p = p0 < 32 ? p0 : p0 - K2_W;
        const bool hp = p < 5, wp = p >= 5 && p < 29;
        const int q = hp ? p : (p < 13 ? p - 5 : p - 13);
        const char* gb = hp ? (const char*)h : !wp ? ws : (p < 13 ? (const char*)we : (const char*)wh);
        const int la = hp ? L_HIMG + p * 1024 : wp ? L_W1 + (p - 5) * 1024 : p == 29 ? L_PD : p == 30 ? L_AIMG + BN * 128 : L_OH;
        const unsigned flat = p == 29 ? (unsigned)W_P + b * BN * 16 : (unsigned)W_C + (p - 30) * 1024;
        const int row0 = (hp ? b * BN : 0) + 8 * q;
        const int sx = hp ? ((4 * q + (lane >> 4)) & 7) : (((lane >> 3) & 2) | ((q & 1) << 2));
        const unsigned vo = (hp || wp) ? (row0 + (lane >> 3)) * 128 + (((lane & 7) ^ sx) * 16) : flat + lane * 16;
        dma1(smb + la, gb, vo);
    }
    PSTAMP(1);
    asm volatile("s_waitcnt vmcnt(3)" : "+v"(tval) :: "memory");
    PSTAMP(2);

    const unsigned cnt = cok ? (tval >> 16) : 0u;
    const unsigned incl = scan64(sub == 0 ? cnt : 0u);
    const unsigned mw = __builtin_amdgcn_readlane(incl, 63);
    const bool slow_w = mw > MAPCAP;
    unsigned* map = (unsigned*)(sm + L_SLOT + wv * 4096);
    QSTAMP(1);
    if (!slow_w) {
        unsigned g = cidc * CE + (tval & 0xffffu) + sub;
        unsigned* mp = map + (incl - cnt) + sub;
#pragma clang loop unroll(disable) vectorize(disable) interleave(disable)
        for (unsigned i = sub; i < cnt; i += 3) { *mp = g; mp += 3; g += 3; }
    }
    PSTAMP(3);
    const unsigned mwe = slow_w ? 0u : mw;
    unsigned gi[5], r32[5], r8[5];
#pragma unroll
    for (int k = 0; k < 5; ++k) { const unsigned m = map[64 * k + lane]; gi[k] = (unsigned)(64 * k + lane) < mwe ? m : 0u; }
    r32[4] = 0; r8[4] = 0;
    if (mwe > 256)
        asm volatile("global_load_dword %0, %2, %4\n\tglobal_load_ubyte %1, %3, %5"
                     : "=&v"(r32[4]), "=&v"(r8[4]) : "v"(gi[4] * 4), "v"(gi[4]), "s"(R32), "s"(R8) : "memory");
#pragma unroll
    for (int k = 0; k < 4; ++k)
        asm volatile("global_load_dword %0, %2, %4\n\tglobal_load_ubyte %1, %3, %5"
                     : "=&v"(r32[k]), "=&v"(r8[k]) : "v"(gi[k] * 4), "v"(gi[k]), "s"(R32), "s"(R8) : "memory");
    unsigned pf;
    {
        const unsigned part = blockIdx.x >> 3;
        const unsigned off = tid < 626 ? (unsigned)W_B + part * (313 * 128) + tid * 64
                                       : (unsigned)W_P + part * (40 * 128) + (tid < 706 ? tid - 626 : 0) * 64;
        asm volatile("global_load_dword %0, %1, %2" : "=v"(pf) : "v"(off), "s"(ws) : "memory");
    }

    PSTAMP(4);
    {
        const u32x4 z = {0, 0, 0, 0};
        *(u32x4*)(sm + L_STAGE + wv * 2048 + lane * 16) = z;
        *(u32x4*)(sm + L_STAGE + wv * 2048 + 1024 + lane * 16) = z;
        const u32x4 n32 = {NN, NN, NN, NN}, n8 = {0x08080808u, 0x08080808u, 0x08080808u, 0x08080808u};
        *(u32x4*)(sm + L_R32 + tid * 16) = n32;
        if (wv == 0) {
            if (lane < RALL / 4 - K2_T) *(u32x4*)(sm + L_R32 + (K2_T + lane) * 16) = n32;
        } else if (wv < 5) {
            const int x = tid - 64;
            if (x < RALL / 16) *(u32x4*)(sm + L_R8 + x * 16) = n8;
        } else if (wv < 9) {
            const int x = tid - 5 * 64;
            if (x < 224) ((unsigned*)(sm + L_BINS))[x] = 0;
        }
        if (lane == 0) ((unsigned*)(sm + L_WF))[wv] = slow_w ? 1u : 0u;
    }
    PSTAMP(5);
    asm volatile("s_waitcnt vmcnt(9)" ::: "memory");
    PSTAMP(6);
    QSTAMP(2);
    bar();

    PSTAMP(7);
    {
        const int ct = wv & 3, g = lane >> 4, node = 16 * (wv >> 2) + (lane & 15);
        f32x4 acc = {0.f, 0.f, 0.f, 0.f};
        acc = MFMA16(wfrag16(sm + L_W1, 0, ct, lane), nfrag16(sm + L_HIMG, node, g), acc);
        acc = MFMA16(wfrag16(sm + L_W1, 1, ct, lane), nfrag16(sm + L_HIMG, node, 4 + g), acc);
        const u32x2 o = {pk_bf16(acc[0] * NEG_LOG2E, acc[1] * NEG_LOG2E), pk_bf16(acc[2] * NEG_LOG2E, acc[3] * NEG_LOG2E)};
        if (node < BN) *(u32x2*)(sm + L_AIMG + node * 128 + (16 * ct + 4 * g) * 2) = o;
    }

    PSTAMP(8);
    asm volatile("s_waitcnt vmcnt(1)"
                 : "+v"(r32[0]), "+v"(r32[1]), "+v"(r32[2]), "+v"(r32[3]), "+v"(r32[4]),
                   "+v"(r8[0]), "+v"(r8[1]), "+v"(r8[2]), "+v"(r8[3]), "+v"(r8[4]) :: "memory");
    PSTAMP(9);
    unsigned bin[5], rk[5];
#pragma unroll
    for (int k = 0; k < 5; ++k) {
        const bool have = (unsigned)(64 * k + lane) < mwe;
        bin[k] = (have ? (r8[k] >> 3) : 6u) * 32 + (lane & 31);
        rk[k] = atomicAdd((unsigned*)(sm + L_BINS) + bin[k], 1u);
    }
    QSTAMP(3);
    PSTAMP(10);
    bar();
    PSTAMP(11);

    if (wv == 0) {
        const unsigned* bins = (const unsigned*)(sm + L_BINS);
        const int j = lane & 31, hi = lane >> 5;
        const unsigned ca = bins[(2 * hi) * 32 + j] | (bins[(2 * hi + 1) * 32 + j] << 16);
        const unsigned cb = bins[4 * 32 + j] | (bins[5 * 32 + j] << 16);
        const unsigned ia = scan32(ca), ib = scan32(cb);
        const unsigned t01 = __builtin_amdgcn_readlane(ia, 31), t23 = __builtin_amdgcn_readlane(ia, 63);
        const unsigned t45 = __builtin_amdgcn_readlane(ib, 31);
        const unsigned tot[6] = {t01 & 0xffffu, t01 >> 16, t23 & 0xffffu, t23 >> 16, t45 & 0xffffu, t45 >> 16};
        unsigned st[7];
        st[0] = 0;
#pragma unroll
        for (int w = 0; w < 6; ++w) st[w + 1] = st[w] + ((tot[w] + 31) & ~31u);
        const unsigned xa = ia - ca, xb = ib - cb;
        unsigned* offs = (unsigned*)(sm + L_OFFS);
        offs[(2 * hi) * 32 + j] = (hi ? st[2] : st[0]) + (xa & 0xffffu);
        offs[(2 * hi + 1) * 32 + j] = (hi ? st[3] : st[1]) + (xa >> 16);
        offs[(4 + hi) * 32 + j] = hi ? st[5] + (xb >> 16) : st[4] + (xb & 0xffffu);
        const bool any_slow = __any(((const unsigned*)(sm + L_WF))[lane < K2_W ? lane : 0] != 0);
        unsigned wi = 0;
#pragma unroll
        for (int w = 0; w < 6; ++w) { wi = lane == w ? st[w] : wi; wi = lane == 6 + w ? (st[w + 1] - st[w]) >> 5 : wi; }
        wi = lane == 12 ? ((any_slow || st[6] > RCAP) ? 1u : 0u) : wi;
        if (lane < 13) ((unsigned*)(sm + L_WIN))[lane] = wi;
    }
    bar();
    QSTAMP(4);
    const bool slow = __builtin_amdgcn_readfirstlane(((const unsigned*)(sm + L_WIN))[12]) != 0;

    const int r = lane & 31, hh = lane >> 5;
    const int win = wv >> 1;
    const int wnode = win_start(win);
    char* part = sm + L_SLOT + wv * 4096;

    if (!slow) {
#pragma unroll
        for (int k = 0; k < 5; ++k) {
            const unsigned o = ((const unsigned*)(sm + L_OFFS))[bin[k]] + rk[k];
            const unsigned p = (unsigned)(64 * k + lane) < mwe ? o : (unsigned)(RALL - 1);
            ((unsigned*)(sm + L_R32))[p] = r32[k];
            ((unsigned char*)(sm + L_R8))[p] = (unsigned char)(r8[k] & 7u);
        }
        const int wstart = __builtin_amdgcn_readfirstlane(((const unsigned*)(sm + L_WIN))[win]);
        const int wtiles = __builtin_amdgcn_readfirstlane(((const unsigned*)(sm + L_WIN))[6 + win]);
        const int first = (wtiles + (((win >> 1) & 1) ^ 1)) >> 1;
        const int ta = (wv & 1) ? first : 0, tb = (wv & 1) ? wtiles : first;

        const int blk = (lane >> 4) & 1, q = (lane & 15) >> 2, p = lane & 3;
        bf16x8 i0, i1;
#pragma unroll
        for (int j = 0; j < 8; ++j) {
            i0[j] = (r == 8 * hh + j) ? (short)0x3F80 : (short)0;
            i1[j] = (r == 16 + 8 * hh + j) ? (short)0x3F80 : (short)0;
        }
        const int rho = 8 * (r & 3) + (r >> 2);
        const int fsw = (r & 3) ^ (((r >> 3) & 1) << 2);
        const char* aad[4];
#pragma unroll
        for (int s = 0; s < 4; ++s) aad[s] = sm + L_SLOT + wv * 4096 + rho * 128 + 16 * ((2 * s + hh) ^ fsw);
        unsigned kk[4];
#pragma unroll
        for (int i = 0; i < 4; ++i)
            kk[i] = 16 * ((lane & 7) ^ i ^ (((lane >> 4) & 1) << 2)) + (3072 - 1024 * i);
        const char* gB = Btab - 3072;
        const unsigned slot_a = smb + L_SLOT + wv * 4096;
        const unsigned pbuf_a = smb + L_PB + wv * 1024;
        char* stage_r = sm + L_STAGE + wv * 2048 + r * 64;
        const char* trb = sm + L_STAGE + wv * 2048 + (4 * hh + q) * 64 + blk * 32 + p * 8;
        const char* pdw = sm + L_PD + wnode * 16;
        asm volatile("s_waitcnt vmcnt(0)" : "+v"(pf) :: "memory");
        bar();
        QSTAMP(5);

        bf16x8 e0, e1;
        {
            const int row = hh ? BN + q : wnode + q;
            const char* ab = sm + L_AIMG + row * 128 + (16 * blk + 4 * p) * 2;
            e0 = cat8(tr_read(ab), tr_read(ab + 512));
            e1 = cat8(tr_read(ab + 64), tr_read(ab + 64 + 512));
        }
        f32x16 z0, z1, xa, xb;
#pragma unroll
        for (int i = 0; i < 16; ++i) { z0[i] = 0.f; z1[i] = 0.f; }
        QSTAMP(6);
        CSTAMP(12);

        if (ta < tb) {
            int base = wstart + 32 * ta;
            const int end = wstart + 32 * tb;
            bf16x8 a0, a1, a2, a3, ef, wf0, wf1;
            unsigned rs, rn, w0, w1, ua, vb;
            char* saddr;
            bool valid;
            u32x4 pkp = {0, 0, 0, 0};
#pragma unroll
            for (int j = 0; j < 8; ++j) wf1[j] = 0;

            auto issue_rows = [&](int ri) {
                const u32x4 s = *(const u32x4*)__builtin_assume_aligned(sm + L_R32 + (ri + 4 * (lane >> 3)) * 4, 16);
                wait_lgkm0();
                dma4(slot_a, gB, (s[0] & 0xffffu) * 128 + kk[0], (s[1] & 0xffffu) * 128 + kk[1],
                     (s[2] & 0xffffu) * 128 + kk[2], (s[3] & 0xffffu) * 128 + kk[3]);
            };
            auto fetch_pair = [&](int ri) {
                rs = ((const unsigned*)(sm + L_R32))[ri + lane];
                rn = ((const unsigned char*)(sm + L_R8))[ri + lane];
                dma1(pbuf_a, Ptab, (rs & 0xffffu) * 16);
            };
            auto geometry = [&]() {
                const f32x4 pd = *(const f32x4*)(pdw + rn * 16);
                const f32x4 ps = *(const f32x4*)(sm + L_PB + wv * 1024 + lane * 16);
                const float dx = pd[0] - ps[0], dy = pd[1] - ps[1], dz = pd[2] - ps[2];
                const float d2 = (dx * dx + dy * dy + dz * dz) * NEG_LOG2E;
                w0 = pk_bf16(1.0f, dx);
                w1 = pk_bf16(dy, dz);
                const unsigned ext = pk_bf16(d2, __uint_as_float(rs & 0xffff0000u) * NEG_LOG2E);
                const auto sw = __builtin_amdgcn_permlane32_swap(L_OH + rn * 16, ext, false, false);
                ua = sw[0];
                vb = sw[1];
                saddr = stage_r + rn * 8;
                valid = rn < 8;
            };
            auto read_frags = [&](unsigned u) {
                a0 = *(const bf16x8*)aad[0]; a1 = *(const bf16x8*)aad[1];
                a2 = *(const bf16x8*)aad[2]; a3 = *(const bf16x8*)aad[3];
                u32x4 ex = *(const u32x4*)(sm + (hh ? (unsigned)L_OH : u));
                ex[0] = hh ? u : ex[0];
                ef = __builtin_bit_cast(bf16x8, ex);
            };
            auto stage_weights = [&](bool wr) {
                if (wr) *(u32x2*)saddr = u32x2{w0, w1};
                wf0 = cat8(tr_read(trb), tr_read(trb + 512));
                wf1 = cat8(tr_read(trb + 1024), tr_read(trb + 1536));
                if (wr) *(u32x2*)saddr = u32x2{0, 0};
            };
            auto nop = [&]() {};

            fetch_pair(base);
            issue_rows(base);
            wait_vm0();
            geometry();
            read_frags(ua);
            if (base + 32 < end) issue_rows(base + 32);
            {
                f32x16 zero;
#pragma unroll
                for (int i = 0; i < 16; ++i) zero[i] = 0.f;
                xa = MFMA32(a0, i0, zero);
                xa = MFMA32(a1, i1, xa);
                xa = MFMA32(ef, e0, xa);
            }
            SBAR();
            auto set_prio = [&](int rem) {
                if (rem >= 192) __builtin_amdgcn_s_setprio(3);
                else if (rem >= 128) __builtin_amdgcn_s_setprio(2);
                else if (rem >= 64) __builtin_amdgcn_s_setprio(1);
                else __builtin_amdgcn_s_setprio(0);
            };
            while (true) {
                set_prio(end - base);
                half_tile(xa, xb, a2, a3, ef, e1, i0, i1, z1, pkp, wf1, z0, wf0,
                          [&]() { stage_weights(valid && hh == 0); },
                          [&]() { wait_vm0(); read_frags(vb); },
                          [&]() { if (base + 64 < end) issue_rows(base + 64); fetch_pair(base + 64); });
                half_tile(xb, xa, a0, a1, ef, e0, i0, i1, z0, pkp, wf1, z1, wf0, nop, nop, nop);
                if (base + 32 >= end) break;
                half_tile(xa, xb, a2, a3, ef, e1, i0, i1, z1, pkp, wf1, z0, wf0,
                          [&]() { stage_weights(valid && hh == 1); },
                          [&]() { wait_vm0(); geometry(); read_frags(ua); },
                          [&]() { if (base + 96 < end) issue_rows(base + 96); });
                half_tile(xb, xa, a0, a1, ef, e0, i0, i1, z0, pkp, wf1, z1, wf0, nop, nop, nop);
                base += 64;
                if (base >= end) break;
            }
            z1 = MFMA32(__builtin_bit_cast(bf16x8, pkp), wf1, z1);
        }
        CSTAMP(13);
        QSTAMP(7);
        float cs0 = 0.f, cs1 = 0.f;
#pragma unroll
        for (int g = 0; g < 4; ++g) {
            const f32x4 w0 = *(const f32x4*)(sm + L_WX + (8 * g + 4 * hh) * 4);
            const f32x4 w1 = *(const f32x4*)(sm + L_WX + (32 + 8 * g + 4 * hh) * 4);
#pragma unroll
            for (int i = 0; i < 4; ++i) { cs0 += z0[4 * g + i] * w0[i]; cs1 += z1[4 * g + i] * w1[i]; }
        }
        const float csh = cs0 + cs1;
        const auto cw = __builtin_amdgcn_permlane32_swap(__float_as_uint(csh), __float_as_uint(csh), false, false);
        const float cst = __uint_as_float(cw[0]) + __uint_as_float(cw[1]);
        wait_vm0();
        if (hh == 0) ((float*)(part + 2048))[r] = cst;
        if ((lane & 3) == 0) {
            const int n = r >> 2;
#pragma unroll
            for (int g = 0; g < 4; ++g) {
                *(f32x4*)(part + n * 256 + (8 * g + 4 * hh) * 4) = f32x4{z0[4 * g], z0[4 * g + 1], z0[4 * g + 2], z0[4 * g + 3]};
                *(f32x4*)(part + n * 256 + (32 + 8 * g + 4 * hh) * 4) = f32x4{z1[4 * g], z1[4 * g + 1], z1[4 * g + 2], z1[4 * g + 3]};
            }
        }
    } else {
        asm volatile("s_waitcnt vmcnt(0)" : "+v"(pf) :: "memory");
        {
            const u32x4 z = {0, 0, 0, 0};
            *(u32x4*)(part + lane * 16) = z;
            *(u32x4*)(part + 1024 + lane * 16) = z;
            if (lane < 8) *(u32x4*)(part + 2048 + lane * 16) = z;
        }
        bar();
        const float wd = bf2f(we[128 * 64 + lane]), wa = bf2f(we[129 * 64 + lane]);
        const float wxl = ((const float*)(sm + L_WX))[lane];
        const unsigned* T = (const unsigned*)(ws + W_T) + b * 256;
        for (int ci = wv; ci < NCH; ci += K2_W) {
            const unsigned tv = T[(ci & 7) * 32 + (ci >> 3)];
            const unsigned g0 = ci * CE + (tv & 0xffffu), cn = tv >> 16;
            for (unsigned i = 0; i < cn; ++i) {
                const unsigned rc = R32[g0 + i], cd = R8[g0 + i];
                const unsigned s = rc & 0xffffu, w = cd >> 3, n = cd & 7, dl = win_start(w) + n;
                const f32x4 ps = *(const f32x4*)(Ptab + s * 16);
                const f32x4 pd = *(const f32x4*)(sm + L_PD + dl * 16);
                const float dx = pd[0] - ps[0], dy = pd[1] - ps[1], dz = pd[2] - ps[2];
                const unsigned e = pk_bf16((dx * dx + dy * dy + dz * dz) * NEG_LOG2E, __uint_as_float(rc & 0xffff0000u) * NEG_LOG2E);
                const unsigned dxy = pk_bf16(dx, dy), dz0 = pk_bf16(dz, 0.f);
                float y = bf2f(((const uint16_t*)(sm + L_AIMG))[dl * 64 + lane]) + bf2f(((const uint16_t*)Btab)[s * 64 + lane]);
                y += bf2f(e & 0xffffu) * wd + bf2f(e >> 16) * wa;
                const float m = bf2f(pk_bf16(y * rcp(1.0f + ex2(y)), 0.f) & 0xffffu);
                char* tgt = sm + L_SLOT + 2 * w * 4096;
                atomicAdd((float*)(tgt + n * 256) + lane, m);
                float t = m * wxl;
                for (int o = 32; o > 0; o >>= 1) t += __shfl_xor(t, o);
                if (lane < 3) {
                    const float dv = lane == 0 ? bf2f(dxy & 0xffffu) : lane == 1 ? bf2f(dxy >> 16) : bf2f(dz0 & 0xffffu);
                    atomicAdd((float*)(tgt + 2048) + 4 * n + 1 + lane, dv * t);
                }
            }
        }
    }
    QSTAMP(8);
    bar();
    QSTAMP(9);

    if (tid < 640) {
        const int nl = tid >> 4, f4 = (tid & 15) * 4, w = win_of(nl), n = nl - win_start(w);
        const char* pb = sm + L_SLOT + 2 * w * 4096 + n * 256 + f4 * 4;
        f32x4 s = *(const f32x4*)pb + *(const f32x4*)(pb + 4096);
        s *= NEG_LN2;
        u32x2 o = {pk_bf16(s[0], s[1]), pk_bf16(s[2], s[3])};
        *(u32x2*)(sm + L_MIMG + nl * 128 + 16 * ((f4 >> 3) ^ ((nl >> 1) & 7)) + 8 * ((f4 >> 2) & 1)) = o;
    } else if (tid < 640 + 3 * BN) {
        const int x = tid - 640, nl = (x * 171) >> 9, cc = x - 3 * nl, w = win_of(nl), n = nl - win_start(w);
        const char* pb = sm + L_SLOT + 2 * w * 4096 + 2048 + (4 * n + 1 + cc) * 4;
        const float s = *(const float*)pb + *(const float*)(pb + 4096);
        const float p0 = ((const float*)(sm + L_PD))[nl * 4 + cc];
        out[NN * 64 + (b * BN + nl) * 3 + cc] = (uint16_t)(pk_bf16(p0 + s, 0.f) & 0xffffu);
    }
    bar();
    QSTAMP(10);

    {
        const int ct = wv & 3, g = lane >> 4, node = 16 * (wv >> 2) + (lane & 15);
        f32x4 acc = {0.f, 0.f, 0.f, 0.f};
#pragma unroll
        for (int s = 0; s < 4; ++s)
            acc = MFMA16(wfrag16(sm + L_WH, s, ct, lane), nfrag16(sm + (s < 2 ? L_HIMG : L_MIMG), node, 4 * (s & 1) + g), acc);
        if (node < BN) {
            const u32x2 hv = *(const u32x2*)(sm + L_HIMG + node * 128 + 16 * ((2 * ct + (g >> 1)) ^ ((node >> 1) & 7)) + 8 * (g & 1));
            const float o0 = bf2f(hv[0] & 0xffffu) + silu_f(acc[0]), o1 = bf2f(hv[0] >> 16) + silu_f(acc[1]);
            const float o2 = bf2f(hv[1] & 0xffffu) + silu_f(acc[2]), o3 = bf2f(hv[1] >> 16) + silu_f(acc[3]);
            *(u32x2*)(out + (b * BN + node) * 64 + 16 * ct + 4 * g) = u32x2{pk_bf16(o0, o1), pk_bf16(o2, o3)};
        }
    }
    QSTAMP(11);
    STAMP_OUT(1, ws);
}

}

extern "C" void kernel_launch(void* const* d_in, const int* in_sizes, int n_in, void* d_out, int out_size, void* d_ws,
                              size_t ws_size, hipStream_t stream) {
    using namespace egnn;
    const uint16_t* h = (const uint16_t*)d_in[0];
    const uint16_t* pos = (const uint16_t*)d_in[1];
    const uint16_t* ea = (const uint16_t*)d_in[2];
    const int* src = (const int*)d_in[3];
    const int* dst = (const int*)d_in[4];
    const uint16_t* we = (const uint16_t*)d_in[5];
    const uint16_t* wx = (const uint16_t*)d_in[6];
    const uint16_t* wh = (const uint16_t*)d_in[7];
    (void)hipFuncSetAttribute((const void*)k_edge, hipFuncAttributeMaxDynamicSharedMemorySize, L_END);
    k_sort<<<NCH, K1_T, 0, stream>>>(src, dst, ea, h, pos, we, wx, (char*)d_ws);
    k_edge<<<256, K2_T, L_END, stream>>>(h, we, wh, (const char*)d_ws, (uint16_t*)d_out);
}
```

```cpp
#define NO_TW2
#include <hip/hip_runtime.h>
#include <stdint.h>

namespace egnn {

typedef short    bf16x8 __attribute__((ext_vector_type(8)));
typedef short    s16x4  __attribute__((ext_vector_type(4)));
typedef float    f32x16 __attribute__((ext_vector_type(16)));
typedef float    f32x4  __attribute__((ext_vector_type(4)));
typedef float    f32x2  __attribute__((ext_vector_type(2)));
typedef __bf16   bf16x2 __attribute__((ext_vector_type(2)));
typedef unsigned u32x4  __attribute__((ext_vector_type(4)));
typedef unsigned u32x2  __attribute__((ext_vector_type(2)));
#define LDS_AS __attribute__((address_space(3)))
#define DI __device__ __forceinline__
#define SBAR() __builtin_amdgcn_sched_barrier(0)
#define PIN(x) asm volatile("" : "+v"(x))
#define MFMA32(a, b, c) __builtin_amdgcn_mfma_f32_32x32x16_bf16(a, b, c, 0, 0, 0)
#define MFMA16(a, b, c) __builtin_amdgcn_mfma_f32_16x16x32_bf16(a, b, c, 0, 0, 0)

constexpr int NN = 10000;
constexpr int NE = 640000;
constexpr int BN = 40;
constexpr int NBK = 250;
constexpr int CE = 2560;
constexpr int NCH = 250;
constexpr float NEG_LOG2E = -1.4426950408889634f;
constexpr float NEG_LN2 = -0.6931471805599453f;

constexpr size_t W_R32 = 0;
constexpr size_t W_R8  = 3u << 20;
constexpr size_t W_T   = 4u << 20;
constexpr size_t W_B   = (4u << 20) + (512u << 10);
constexpr size_t W_P   = 6u << 20;
constexpr size_t W_C   = (6u << 20) + (512u << 10);

DI unsigned win_of(unsigned dl) { return ((dl * 37u) >> 8) + (dl == 34u ? 1u : 0u); }
DI unsigned win_start(unsigned w) { return 7u * w - (w == 5u ? 1u : 0u); }

DI float bf2f(unsigned v16) { return __uint_as_float(v16 << 16); }
DI unsigned pk_bf16(float lo, float hi) {
    f32x2 x = {lo, hi};
    return __builtin_bit_cast(unsigned, __builtin_convertvector(x, bf16x2));
}
DI float silu_f(float x) {
    return x * __builtin_amdgcn_rcpf(1.0f + __builtin_amdgcn_exp2f(x * NEG_LOG2E));
}
DI s16x4 tr_read(const char* p) {
    return __builtin_amdgcn_ds_read_tr16_b64_v4i16((LDS_AS s16x4*)p);
}
DI bf16x8 cat8(s16x4 a, s16x4 b) { return __builtin_shufflevector(a, b, 0, 1, 2, 3, 4, 5, 6, 7); }
DI void wait_vm0() { asm volatile("s_waitcnt vmcnt(0)" ::: "memory"); }
DI void wait_lgkm0() { asm volatile("s_waitcnt lgkmcnt(0)" ::: "memory"); }
DI void bar() { asm volatile("s_waitcnt lgkmcnt(0)\n\ts_barrier" ::: "memory"); }

DI void dma1(unsigned ldsaddr, const void* gbase, unsigned voff) {
    unsigned keep;
    asm volatile("s_mov_b32 %0, m0\n\ts_mov_b32 m0, %1\n\ts_nop 0\n\t"
                 "global_load_lds_dwordx4 %2, %3\n\t"
                 "s_mov_b32 m0, %0"
                 : "=&s"(keep) : "s"(ldsaddr), "v"(voff), "s"(gbase) : "memory");
}
DI void dma4(unsigned ldsaddr, const void* gbase, unsigned v0, unsigned v1, unsigned v2, unsigned v3) {
    unsigned keep;
    asm volatile("s_mov_b32 %0, m0\n\ts_mov_b32 m0, %1\n\ts_nop 0\n\t"
                 "global_load_lds_dwordx4 %2, %6\n\t"
                 "global_load_lds_dwordx4 %3, %6 offset:1024\n\t"
                 "global_load_lds_dwordx4 %4, %6 offset:2048\n\t"
                 "global_load_lds_dwordx4 %5, %6 offset:3072\n\t"
                 "s_mov_b32 m0, %0"
                 : "=&s"(keep) : "s"(ldsaddr), "v"(v0), "v"(v1), "v"(v2), "v"(v3), "s"(gbase) : "memory");
}
template <typename T> DI void st_nt(void* p, T v) { __builtin_nontemporal_store(v, (T*)p); }
DI unsigned mad16(unsigned x, unsigned m, unsigned a) {
    unsigned r;
    asm("v_mad_u32_u16 %0, %1, %2, %3" : "=v"(r) : "v"(x), "s"(m), "v"(a));
    return r;
}
template <typename T> DI T lds_ld(unsigned a) { return *(const LDS_AS T*)(size_t)a; }
template <typename T> DI void lds_st(unsigned a, T v) { *(LDS_AS T*)(size_t)a = v; }
template <int CTRL, int ROWMASK> DI unsigned dpp_add(unsigned x) {
    return x + (unsigned)__builtin_amdgcn_update_dpp(0, (int)x, CTRL, ROWMASK, 0xf, true);
}
DI unsigned scan32(unsigned x) {
    x = dpp_add<0x111, 0xf>(x); x = dpp_add<0x112, 0xf>(x); x = dpp_add<0x114, 0xf>(x); x = dpp_add<0x118, 0xf>(x);
    x = dpp_add<0x142, 0xa>(x);
    return x;
}
DI unsigned scan64(unsigned x) { x = scan32(x); x = dpp_add<0x143, 0xc>(x); return x; }
DI int wswz(int k) { return (k & 2) | ((k >> 1) & 4); }
DI bf16x8 wfrag16(const char* img, int s, int ct, int lane) {
    const int g = lane >> 4, q = (lane & 15) >> 2, p = lane & 3;
    const int k0 = 32 * s + 8 * g + q, k1 = k0 + 4, chn = 2 * ct + (p >> 1);
    return cat8(tr_read(img + k0 * 128 + 16 * (chn ^ wswz(k0)) + 8 * (p & 1)),
                tr_read(img + k1 * 128 + 16 * (chn ^ wswz(k1)) + 8 * (p & 1)));
}
DI bf16x8 nfrag16(const char* img, int node, int chunk) {
    return *(const bf16x8*)(img + node * 128 + 16 * (chunk ^ ((node >> 1) & 7)));
}

#define PSTAMP(i)
#define QSTAMP(i)
#define STAMP_DECL
#define STAMP(i)
#define CSTAMP(i)
#define STAMP_OUT(kern, wsp)

constexpr int K1_T = 640;
constexpr int S_CNT = 0, S_OFF = 1024, S_R32 = 2048, S_R8 = 12288, S_W = 14848, S_H = 23040, S_O = 29184, S_END = 34304;

__global__ __launch_bounds__(K1_T) void k_sort(const int* __restrict__ src, const int* __restrict__ dst,
                                               const uint16_t* __restrict__ ea, const uint16_t* __restrict__ h,
                                               const uint16_t* __restrict__ pos, const uint16_t* __restrict__ we,
                                               const uint16_t* __restrict__ wx, char* __restrict__ ws) {
    __shared__ __attribute__((aligned(16))) char sm[S_END];
    const int tid = threadIdx.x, lane = tid & 63;
    const int wv = __builtin_amdgcn_readfirstlane(tid >> 6);
    const int c = blockIdx.x;
    asm volatile("" :: "s"(src), "s"(dst), "s"(ea), "s"(h), "s"(pos), "s"(we), "s"(wx), "s"(ws));
    const unsigned smb = (unsigned)(size_t)(LDS_AS char*)sm;
    STAMP_DECL
    STAMP(0);

    const int e0 = c * CE + 4 * tid;
    const u32x4 d4 = *(const u32x4*)(dst + e0);
    const u32x4 s4 = *(const u32x4*)(src + e0);
    const u32x2 a4 = *(const u32x2*)(ea + e0);
    unsigned tw;
    {
        const unsigned o = lane == 0 ? (unsigned)W_R32 + c * (CE * 4) : lane == 1 ? (unsigned)W_R8 + c * CE
                         : lane == 2 ? (unsigned)W_T + c * 1024 : lane == 3 ? (unsigned)W_B + c * (BN * 128) : (unsigned)W_P + c * (BN * 16);
        asm volatile("global_load_dword %0, %1, %2" : "=v"(tw) : "v"(o), "s"(ws) : "memory");
    }
    const int pn = c * BN + (lane < BN ? lane : 0);
    const unsigned px = pos[pn * 3], py = pos[pn * 3 + 1], pz = pos[pn * 3 + 2];

    {
        const int k = (wv & 7) * 8 + (lane >> 3);
        const int row = (wv - 5) * 8 + (lane >> 3);
        if (wv < 8) dma1(smb + S_W + wv * 1024, we, (64 + k) * 128 + (((lane & 7) ^ wswz(k)) * 16));
        if (wv >= 5) dma1(smb + S_H + (wv - 5) * 1024, h, (c * BN + row) * 128 + (((lane & 7) ^ ((row >> 1) & 7)) * 16));
    }
    if (tid < 256) ((unsigned*)(sm + S_CNT))[tid] = 0;
    bar();
    STAMP(1);

    unsigned bk[4], rk[4];
#pragma unroll
    for (int i = 0; i < 4; ++i) {
        bk[i] = (d4[i] * 52429u) >> 21;
        rk[i] = atomicAdd((unsigned*)(sm + S_CNT) + bk[i], 1u);
    }
    unsigned char code[4];
#pragma unroll
    for (int i = 0; i < 4; ++i) {
        const unsigned dl = d4[i] - bk[i] * BN, w = win_of(dl);
        code[i] = (unsigned char)(w * 8 + dl - win_start(w));
    }
    STAMP(2);
    asm volatile("s_waitcnt vmcnt(0)" : "+v"(tw) :: "memory");
    bar();
    STAMP(3);

    if (wv == 9) {
        const u32x4 cn = *(const u32x4*)(sm + S_CNT + lane * 16);
        const unsigned tot = cn[0] + cn[1] + cn[2] + cn[3];
        const unsigned ex = scan64(tot) - tot;
        const u32x4 of = {ex, ex + cn[0], ex + cn[0] + cn[1], ex + cn[0] + cn[1] + cn[2]};
        *(u32x4*)(sm + S_OFF + lane * 16) = of;
        char* T = ws + W_T + ((c & 7) * 32 + (c >> 3)) * 4 + lane * 4096;
#pragma unroll
        for (int i = 0; i < 4; ++i)
            if (4 * lane + i < NBK) st_nt(T + i * 1024, of[i] | (cn[i] << 16));
    } else if (wv == 8) {
        if (lane < BN) st_nt(ws + W_P + (size_t)pn * 16, f32x4{bf2f(px), bf2f(py), bf2f(pz), 0.f});
        if (c == 0) {
            const u32x4 z = {0, 0, 0, 0};
            st_nt(ws + W_P + (size_t)NN * 16 + lane * 16, z);
            if (lane < 8) st_nt(ws + W_B + (size_t)NN * 128 + lane * 16, z);
            st_nt(ws + W_C + lane * 16, lane < 16 ? *(const u32x4*)(we + 128 * 64 + lane * 8) : z);
            const int n = lane >> 2, j = lane & 3;
            st_nt(ws + W_C + 1024 + lane * 4, (n < 8 && (n >> 1) == j) ? (0x3F80u << (16 * (n & 1))) : 0u);
            st_nt(ws + W_C + 1280 + lane * 4, bf2f(wx[lane]) * NEG_LN2);
        }
    }
    STAMP(4);
    bar();
    STAMP(5);

#pragma unroll
    for (int i = 0; i < 4; ++i) {
        const unsigned p = ((const unsigned*)(sm + S_OFF))[bk[i]] + rk[i];
        const unsigned av = (i & 1) ? (a4[i >> 1] & 0xffff0000u) : (a4[i >> 1] << 16);
        ((unsigned*)(sm + S_R32))[p] = s4[i] | av;
        ((unsigned char*)(sm + S_R8))[p] = code[i];
    }
    bar();
    STAMP(6);
    st_nt(ws + W_R32 + (size_t)c * (CE * 4) + tid * 16, *(const u32x4*)(sm + S_R32 + tid * 16));
    if (tid < CE / 16) st_nt(ws + W_R8 + (size_t)c * CE + tid * 16, *(const u32x4*)(sm + S_R8 + tid * 16));
    STAMP(7);

    if (wv < 8) {
        const int ct = wv & 3, g = lane >> 4;
        const bf16x8 a0 = wfrag16(sm + S_W, 0, ct, lane), a1 = wfrag16(sm + S_W, 1, ct, lane);
        for (int nt = wv >> 2; nt < 3; nt += 2) {
            const int node = 16 * nt + (lane & 15);
            f32x4 acc = {0.f, 0.f, 0.f, 0.f};
            acc = MFMA16(a0, nfrag16(sm + S_H, node, g), acc);
            acc = MFMA16(a1, nfrag16(sm + S_H, node, 4 + g), acc);
            const u32x2 o = {pk_bf16(acc[0] * NEG_LOG2E, acc[1] * NEG_LOG2E), pk_bf16(acc[2] * NEG_LOG2E, acc[3] * NEG_LOG2E)};
            if (node < BN) *(u32x2*)(sm + S_O + node * 128 + 16 * ((2 * ct + (g >> 1)) ^ (node & 7)) + 8 * (g & 1)) = o;
        }
    }
    bar();
    if (tid < BN * 8) {
        const int node = tid >> 3, ch = (tid & 7) ^ (node & 7);
        st_nt(ws + W_B + (size_t)(c * BN + node) * 128 + ch * 16, *(const u32x4*)(sm + S_O + tid * 16));
    }
    STAMP(8);
    STAMP_OUT(0, ws);
}

constexpr int K2_W = 12, K2_T = 64 * K2_W;
constexpr int RCAP = 3072;
constexpr int L_SLOT = 0;
constexpr int L_STAGE = 49152;
constexpr int RALL = RCAP + 128;
constexpr int L_R32 = 73728;
constexpr int L_R8 = L_R32 + RALL * 4;
constexpr int L_AIMG = L_R8 + RALL;
constexpr int L_HIMG = L_AIMG + 6144;
constexpr int L_W1 = L_HIMG + 6144;
constexpr int L_WH = L_W1 + 8192;
constexpr int L_PD = L_WH + 16384;
constexpr int L_OH = L_PD + 1024;
constexpr int L_WX = L_OH + 256;
constexpr int L_BINS = L_OH + 1024;
constexpr int L_OFFS = L_BINS + 896;
constexpr int L_WIN = L_OFFS + 896;
constexpr int L_WF = L_WIN + 64;
constexpr int L_PB = L_WF + 64;
constexpr int L_END = L_PB + K2_W * 1024;
constexpr unsigned MAPCAP = 320;
constexpr int PROW = 272, PPOS = 8 * PROW;

DI bf16x8 as_frag(unsigned a, unsigned b, unsigned c, unsigned d) {
    u32x4 v = {a, b, c, d};
    return __builtin_bit_cast(bf16x8, v);
}
DI float ex2(float x) { return __builtin_amdgcn_exp2f(x); }
DI float rcp(float x) { return __builtin_amdgcn_rcpf(x); }

template <class H1, class H2, class H3>
DI void half_tile(const f32x16& xc, f32x16& xn, const bf16x8& aA, const bf16x8& aB, const bf16x8& ef, const bf16x8& eB,
                  const bf16x8& i0, const bf16x8& i1, f32x16& zp, u32x4& pkp, const bf16x8& wp, f32x16& zc,
                  const bf16x8& wc, H1&& hook1, H2&& hook2, H3&& hook3) {
    f32x16 zero;
#pragma unroll
    for (int i = 0; i < 16; ++i) zero[i] = 0.f;
    f32x2 e0, e1, e2, e3;
    u32x4 pk;
#define EXP2(e, i) do { e[0] = ex2(xc[i]); PIN(e[0]); } while (0)
#define EXP2H(e, i) do { e[1] = ex2(xc[i]); PIN(e[1]); } while (0)
#define RCP2(e) do { e[0] = rcp(e[0]); PIN(e[0]); } while (0)
#define RCP2H(e) do { e[1] = rcp(e[1]); PIN(e[1]); } while (0)
#define PKMODE 0
#define ADD1(e) do { e = e + 1.0f; PIN(e); } while (0)
#define MULX(e, i) do { e = e * f32x2{xc[i], xc[i + 1]}; PIN(e); } while (0)
#define CVT(d, e) do { d = __builtin_bit_cast(unsigned, __builtin_convertvector(e, bf16x2)); PIN(d); } while (0)
    xn = MFMA32(aA, i0, zero); SBAR();
    EXP2(e0, 0); EXP2H(e0, 1); EXP2(e1, 2); SBAR();
    zp = MFMA32(__builtin_bit_cast(bf16x8, pkp), wp, zp); SBAR();
    EXP2H(e1, 3); EXP2(e2, 4); EXP2H(e2, 5); EXP2(e3, 6); EXP2H(e3, 7); SBAR();
    ADD1(e0); ADD1(e1); ADD1(e2); ADD1(e3); SBAR();
    hook1(); SBAR();
    xn = MFMA32(aB, i1, xn); SBAR();
    RCP2(e0); RCP2H(e0); RCP2(e1); SBAR();
    xn = MFMA32(ef, eB, xn); SBAR();
    RCP2H(e1); RCP2(e2); RCP2H(e2); RCP2(e3); RCP2H(e3); SBAR();
    MULX(e0, 0); MULX(e1, 2); MULX(e2, 4); MULX(e3, 6); SBAR();
    CVT(pk[0], e0); CVT(pk[1], e1); CVT(pk[2], e2); CVT(pk[3], e3); SBAR();
    EXP2(e0, 8); EXP2H(e0, 9); SBAR();
    zc = MFMA32(__builtin_bit_cast(bf16x8, pk), wc, zc); SBAR();
    EXP2(e1, 10); EXP2H(e1, 11); EXP2(e2, 12); EXP2H(e2, 13); EXP2(e3, 14); EXP2H(e3, 15); SBAR();
    ADD1(e0); ADD1(e1); ADD1(e2); ADD1(e3); SBAR();
    hook2(); SBAR();
    RCP2(e0); RCP2H(e0); RCP2(e1); RCP2H(e1); RCP2(e2); RCP2H(e2); RCP2(e3); RCP2H(e3); SBAR();
    hook3(); SBAR();
    MULX(e0, 8); MULX(e1, 10); MULX(e2, 12); MULX(e3, 14); SBAR();
    CVT(pkp[0], e0); CVT(pkp[1], e1); CVT(pkp[2], e2); CVT(pkp[3], e3); SBAR();
#undef EXP2
#undef EXP2H
#undef RCP2
#undef RCP2H
#undef ADD1
#undef MULX
#undef CVT
}

__global__ __launch_bounds__(K2_T) void k_edge(const uint16_t* __restrict__ h, const uint16_t* __restrict__ we,
                                               const uint16_t* __restrict__ wh, const char* __restrict__ ws,
                                               uint16_t* __restrict__ out) {
    extern __shared__ __attribute__((aligned(16))) char sm[];
    const int tid = threadIdx.x, lane = tid & 63;
    const int wv = __builtin_amdgcn_readfirstlane(tid >> 6);
    const int b = (blockIdx.x & 7) * 32 + (blockIdx.x >> 3);
    if (b >= NBK) return;
    const unsigned smb = (unsigned)(size_t)(LDS_AS char*)sm;
    asm volatile("" :: "s"(h), "s"(we), "s"(wh), "s"(ws), "s"(out));
    const unsigned* R32 = (const unsigned*)(ws + W_R32);
    const unsigned char* R8 = (const unsigned char*)(ws + W_R8);
    const char* Btab = ws + W_B;
    const char* Ptab = ws + W_P;
    STAMP_DECL
    STAMP(0);

    const int l3 = (lane * 171) >> 9, sub = lane - 3 * l3;
    const int cid = 21 * wv + l3;
    const bool cok = lane < 63 && cid < NCH;
    const int cidc = cok ? cid : 0;
    unsigned tval;
    asm volatile("global_load_dword %0, %1, %2" : "=v"(tval)
                 : "v"((unsigned)W_T + (b * 256 + (cidc & 7) * 32 + (cidc >> 3)) * 4), "s"(ws) : "memory");

    {
        const int r8l = lane >> 3, c8 = lane & 7;
        auto wpiece = [&](const uint16_t* w, int q, int la) {
            dma1(smb + la + q * 1024, w, (8 * q + r8l) * 128 + ((c8 ^ ((r8l & 2) | ((q & 1) << 2))) * 16));
        };
        if (wv < 5) dma1(smb + L_HIMG + wv * 1024, h, (b * BN + 8 * wv + r8l) * 128 + ((c8 ^ ((4 * wv + (lane >> 4)) & 7)) * 16));
        else wpiece(we, wv - 5, L_W1);
        if (wv >= 4) wpiece(wh, wv - 4, L_WH);
        else if (wv == 0) wpiece(we, 7, L_W1);
        else {
            const unsigned go = wv == 1 ? (unsigned)W_P + b * BN * 16 : (unsigned)W_C + (wv - 2) * 1024;
            dma1(smb + (wv == 1 ? L_PD : wv == 2 ? L_AIMG + BN * 128 : L_OH), ws, go + lane * 16);
        }
    }
    unsigned tw;
    asm volatile("global_load_dword %0, %1, %2" : "=v"(tw) : "v"((unsigned)W_T + b * 1024), "s"(ws) : "memory");

    {
        const u32x4 z = {0, 0, 0, 0};
        lds_st(smb + L_STAGE + wv * 2048 + lane * 16, z);
        lds_st(smb + L_STAGE + wv * 2048 + 1024 + lane * 16, z);
        const u32x4 n32 = {NN, NN, NN, NN}, n8 = {0x08080808u, 0x08080808u, 0x08080808u, 0x08080808u};
        lds_st(smb + L_R32 + tid * 16, n32);
        if (wv == 0) {
            if (lane < RALL / 4 - K2_T) lds_st(smb + L_R32 + (K2_T + lane) * 16, n32);
        } else if (wv < 5) {
            if (tid - 64 < RALL / 16) lds_st(smb + L_R8 + (tid - 64) * 16, n8);
        } else if (wv == 5) {
            if (lane < 56) lds_st(smb + L_BINS + lane * 16, z);
        }
    }
    PSTAMP(1);
    bar();
    asm volatile("s_waitcnt vmcnt(3)" : "+v"(tval) :: "memory");
    PSTAMP(2);

    const unsigned cnt = cok ? (tval >> 16) : 0u;
    const unsigned incl = scan64(sub == 0 ? cnt : 0u);
    const unsigned mw = __builtin_amdgcn_readlane(incl, 63);
    const bool slow_w = mw > MAPCAP;
    const unsigned map_a = smb + L_SLOT + wv * 4096;
    QSTAMP(1);
    if (!slow_w) {
        unsigned g = cidc * CE + (tval & 0xffffu) + sub;
        unsigned mp = map_a + (incl - cnt + sub) * 4;
#pragma clang loop unroll(disable) vectorize(disable) interleave(disable)
        for (unsigned i = sub; i < cnt; i += 3) { lds_st(mp, g); mp += 12; g += 3; }
    }
    PSTAMP(3);
    const unsigned mwe = slow_w ? 0u : mw;
    unsigned gi[5], r32[5], r8[5];
#pragma unroll
    for (int k = 0; k < 5; ++k) {
        const unsigned m = lds_ld<unsigned>(map_a + (64 * k + lane) * 4);
        gi[k] = (unsigned)(64 * k + lane) < mwe ? m : 0u;
    }
    r32[4] = 0; r8[4] = 0;
    if (mwe > 256)
        asm volatile("global_load_dword %0, %2, %4\n\tglobal_load_ubyte %1, %3, %5"
                     : "=&v"(r32[4]), "=&v"(r8[4]) : "v"(gi[4] * 4), "v"(gi[4]), "s"(R32), "s"(R8) : "memory");
#pragma unroll
    for (int k = 0; k < 4; ++k)
        asm volatile("global_load_dword %0, %2, %4\n\tglobal_load_ubyte %1, %3, %5"
                     : "=&v"(r32[k]), "=&v"(r8[k]) : "v"(gi[k] * 4), "v"(gi[k]), "s"(R32), "s"(R8) : "memory");
    {
        const int q = 8 + (wv & 7), r8l = lane >> 3;
        dma1(smb + L_WH + q * 1024, wh, (8 * q + r8l) * 128 + (((lane & 7) ^ ((r8l & 2) | ((q & 1) << 2))) * 16));
    }
    if (lane == 0) lds_st(smb + L_WF + wv * 4, slow_w ? 1u : 0u);
    PSTAMP(4);
    asm volatile("s_waitcnt vmcnt(1)"
                 : "+v"(r32[0]), "+v"(r32[1]), "+v"(r32[2]), "+v"(r32[3]), "+v"(r32[4]),
                   "+v"(r8[0]), "+v"(r8[1]), "+v"(r8[2]), "+v"(r8[3]), "+v"(r8[4]), "+v"(tw) :: "memory");
    PSTAMP(5);
    unsigned bin[5], rk[5];
#pragma unroll
    for (int k = 0; k < 5; ++k) {
        const bool have = (unsigned)(64 * k + lane) < mwe;
        bin[k] = ((have ? (r8[k] >> 3) : 6u) * 32 + (lane & 31)) * 4;
        rk[k] = atomicAdd((unsigned*)(sm + L_BINS + bin[k]), 1u);
    }
    unsigned pf;
    {
        const unsigned part = blockIdx.x >> 3;
        const unsigned off = tid < 626 ? (unsigned)W_B + part * (313 * 128) + tid * 64
                                       : (unsigned)W_P + part * (40 * 128) + (tid < 706 ? tid - 626 : 0) * 64;
        asm volatile("global_load_dword %0, %1, %2" : "=v"(pf) : "v"(off), "s"(ws) : "memory");
    }
    QSTAMP(3);
    PSTAMP(6);
    bar();
    PSTAMP(7);

    if (wv == 0) {
        const unsigned* bins = (const unsigned*)(sm + L_BINS);
        const int j = lane & 31, hi = lane >> 5;
        const unsigned ca = bins[(2 * hi) * 32 + j] | (bins[(2 * hi + 1) * 32 + j] << 16);
        const unsigned cb = bins[4 * 32 + j] | (bins[5 * 32 + j] << 16);
        const unsigned wfl = lds_ld<unsigned>(smb + L_WF + (lane < K2_W ? lane : 0) * 4);
        const unsigned ia = scan32(ca), ib = scan32(cb);
        const unsigned t01 = __builtin_amdgcn_readlane(ia, 31), t23 = __builtin_amdgcn_readlane(ia, 63);
        const unsigned t45 = __builtin_amdgcn_readlane(ib, 31);
        const unsigned tot[6] = {t01 & 0xffffu, t01 >> 16, t23 & 0xffffu, t23 >> 16, t45 & 0xffffu, t45 >> 16};
        unsigned st[7];
        st[0] = 0;
#pragma unroll
        for (int w = 0; w < 6; ++w) st[w + 1] = st[w] + ((tot[w] + 31) & ~31u);
        const unsigned xa = ia - ca, xb = ib - cb;
        unsigned* offs = (unsigned*)(sm + L_OFFS);
        offs[(2 * hi) * 32 + j] = (hi ? st[2] : st[0]) + (xa & 0xffffu);
        offs[(2 * hi + 1) * 32 + j] = (hi ? st[3] : st[1]) + (xa >> 16);
        offs[(4 + hi) * 32 + j] = hi ? st[5] + (xb >> 16) : st[4] + (xb & 0xffffu);
        const bool any_slow = __any(wfl != 0);
        unsigned wi = 0;
#pragma unroll
        for (int w = 0; w < 6; ++w) { wi = lane == w ? st[w] : wi; wi = lane == 6 + w ? (st[w + 1] - st[w]) >> 5 : wi; }
        wi = lane == 12 ? ((any_slow || st[6] > RCAP) ? 1u : 0u) : wi;
        if (lane < 13) ((unsigned*)(sm + L_WIN))[lane] = wi;
    } else {
        const int ct = wv & 3, g = lane >> 4;
        const bf16x8 a0 = wfrag16(sm + L_W1, 0, ct, lane), a1 = wfrag16(sm + L_W1, 1, ct, lane);
        for (int nt = wv >> 2; ; nt = 0) {
            const int node = 16 * nt + (lane & 15);
            f32x4 acc = {0.f, 0.f, 0.f, 0.f};
            acc = MFMA16(a0, nfrag16(sm + L_HIMG, node, g), acc);
            acc = MFMA16(a1, nfrag16(sm + L_HIMG, node, 4 + g), acc);
            const u32x2 o = {pk_bf16(acc[0] * NEG_LOG2E, acc[1] * NEG_LOG2E), pk_bf16(acc[2] * NEG_LOG2E, acc[3] * NEG_LOG2E)};
            if (node < BN) *(u32x2*)(sm + L_AIMG + node * 128 + (16 * ct + 4 * g) * 2) = o;
            if (wv != 4 || nt == 0) break;
        }
    }
    PSTAMP(8);
    bar();
    QSTAMP(4);
    PSTAMP(9);
    const bool slow = __builtin_amdgcn_readfirstlane(((const unsigned*)(sm + L_WIN))[12]) != 0;

    const int r = lane & 31, hh = lane >> 5;
    const int win = wv >> 1;
    const int wnode = win_start(win);
    char* part = sm + L_SLOT + wv * 4096;

    if (!slow) {
#pragma unroll
        for (int k = 0; k < 5; ++k) {
            const unsigned o = lds_ld<unsigned>(smb + L_OFFS + bin[k]) + rk[k];
            const unsigned p = (unsigned)(64 * k + lane) < mwe ? o : (unsigned)(RALL - 1);
            lds_st(smb + L_R32 + p * 4, r32[k]);
            lds_st(smb + L_R8 + p, (unsigned char)(r8[k] & 7u));
        }
        const int wstart = __builtin_amdgcn_readfirstlane(((const unsigned*)(sm + L_WIN))[win]);
        const int wtiles = __builtin_amdgcn_readfirstlane(((const unsigned*)(sm + L_WIN))[6 + win]);
        const int first = (wtiles + (((win >> 1) & 1) ^ 1)) >> 1;
        const int ta = (wv & 1) ? first : 0, tb = (wv & 1) ? wtiles : first;

        const int blk = (lane >> 4) & 1, q = (lane & 15) >> 2, p = lane & 3;
        bf16x8 i0, i1;
#pragma unroll
        for (int j = 0; j < 8; ++j) {
            i0[j] = (r == 8 * hh + j) ? (short)0x3F80 : (short)0;
            i1[j] = (r == 16 + 8 * hh + j) ? (short)0x3F80 : (short)0;
        }
        const int rho = 8 * (r & 3) + (r >> 2);
        const int fsw = (r & 3) ^ (((r >> 3) & 1) << 2);
        unsigned aad[4];
#pragma unroll
        for (int s = 0; s < 4; ++s) aad[s] = smb + L_SLOT + wv * 4096 + rho * 128 + 16 * ((2 * s + hh) ^ fsw);
        unsigned kk[4];
#pragma unroll
        for (int i = 0; i < 4; ++i)
            kk[i] = 16 * ((lane & 7) ^ i ^ (((lane >> 4) & 1) << 2)) + (3072 - 1024 * i);
        const char* gB = Btab - 3072;
        const unsigned slot_a = smb + L_SLOT + wv * 4096;
        const unsigned pbuf_a = smb + L_PB + wv * 1024;
        const unsigned src_a = smb + L_R32 + (lane >> 3) * 16, rs_a = smb + L_R32 + lane * 4, rn_a = smb + L_R8 + lane;
        const unsigned stage_r = smb + L_STAGE + wv * 2048 + r * 64;
        const char* trb = sm + L_STAGE + wv * 2048 + (4 * hh + q) * 64 + blk * 32 + p * 8;
        const unsigned pdw = smb + L_PD + wnode * 16;
        const unsigned oh_a = smb + L_OH;
        asm volatile("s_waitcnt vmcnt(0)" : "+v"(pf) :: "memory");
        bar();
        QSTAMP(5);
        PSTAMP(10);

        bf16x8 e0, e1;
        {
            const int row = hh ? BN + q : wnode + q;
            const char* ab = sm + L_AIMG + row * 128 + (16 * blk + 4 * p) * 2;
            e0 = cat8(tr_read(ab), tr_read(ab + 512));
            e1 = cat8(tr_read(ab + 64), tr_read(ab + 64 + 512));
        }
        f32x16 z0, z1, xa, xb;
#pragma unroll
        for (int i = 0; i < 16; ++i) { z0[i] = 0.f; z1[i] = 0.f; }
        QSTAMP(6);
        CSTAMP(12);

        if (ta < tb) {
            int base = wstart + 32 * ta;
            const int end = wstart + 32 * tb;
            bf16x8 a0, a1, a2, a3, ef, wf0, wf1;
            unsigned rs, rn, w0, w1, ua, vb;
            unsigned saddr;
            bool valid;
            u32x4 pkp = {0, 0, 0, 0};
#pragma unroll
            for (int j = 0; j < 8; ++j) wf1[j] = 0;

            auto issue_rows = [&](int ri) {
                const u32x4 s = lds_ld<u32x4>(src_a + ri * 4);
                const unsigned v0 = mad16(s[0], 128, kk[0]), v1 = mad16(s[1], 128, kk[1]);
                const unsigned v2 = mad16(s[2], 128, kk[2]), v3 = mad16(s[3], 128, kk[3]);
                wait_lgkm0();
                dma4(slot_a, gB, v0, v1, v2, v3);
            };
            auto fetch_pair = [&](int ri) {
                rs = lds_ld<unsigned>(rs_a + ri * 4);
                rn = lds_ld<unsigned char>(rn_a + ri);
                dma1(pbuf_a, Ptab, mad16(rs, 16, 0u));
            };
            auto geometry = [&]() {
                const f32x4 pd = lds_ld<f32x4>(pdw + rn * 16);
                const f32x4 ps = lds_ld<f32x4>(pbuf_a + lane * 16);
                const float dx = pd[0] - ps[0], dy = pd[1] - ps[1], dz = pd[2] - ps[2];
                const float d2 = (dx * dx + dy * dy + dz * dz) * NEG_LOG2E;
                w0 = pk_bf16(1.0f, dx);
                w1 = pk_bf16(dy, dz);
                const unsigned ext = pk_bf16(d2, __uint_as_float(rs & 0xffff0000u) * NEG_LOG2E);
                const auto sw = __builtin_amdgcn_permlane32_swap(oh_a + rn * 16, ext, false, false);
                ua = sw[0];
                vb = sw[1];
                saddr = stage_r + rn * 8;
                PIN(saddr);
                valid = rn < 8;
            };
            auto read_frags = [&](unsigned u) {
                a0 = lds_ld<bf16x8>(aad[0]); a1 = lds_ld<bf16x8>(aad[1]);
                a2 = lds_ld<bf16x8>(aad[2]); a3 = lds_ld<bf16x8>(aad[3]);
                u32x4 ex = lds_ld<u32x4>(hh ? oh_a : u);
                ex[0] = hh ? u : ex[0];
                ef = __builtin_bit_cast(bf16x8, ex);
            };
            auto stage_weights = [&](bool wr) {
                if (wr) lds_st(saddr, u32x2{w0, w1});
                wf0 = cat8(tr_read(trb), tr_read(trb + 512));
                wf1 = cat8(tr_read(trb + 1024), tr_read(trb + 1536));
                if (wr) lds_st(saddr, u32x2{0, 0});
            };
            auto nop = [&]() {};

            fetch_pair(base);
            issue_rows(base);
            wait_vm0();
            geometry();
            read_frags(ua);
            if (base + 32 < end) issue_rows(base + 32);
            {
                f32x16 zero;
#pragma unroll
                for (int i = 0; i < 16; ++i) zero[i] = 0.f;
                xa = MFMA32(a0, i0, zero);
                xa = MFMA32(a1, i1, xa);
                xa = MFMA32(ef, e0, xa);
            }
            SBAR();
            auto set_prio = [&](int rem) {
                if (rem >= 192) __builtin_amdgcn_s_setprio(3);
                else if (rem >= 128) __builtin_amdgcn_s_setprio(2);
                else if (rem >= 64) __builtin_amdgcn_s_setprio(1);
                else __builtin_amdgcn_s_setprio(0);
            };
            while (true) {
                set_prio(end - base);
                half_tile(xa, xb, a2, a3, ef, e1, i0, i1, z1, pkp, wf1, z0, wf0,
                          [&]() { stage_weights(valid && hh == 0); },
                          [&]() { wait_vm0(); read_frags(vb); },
                          [&]() { if (base + 64 < end) issue_rows(base + 64); fetch_pair(base + 64); });
                half_tile(xb, xa, a0, a1, ef, e0, i0, i1, z0, pkp, wf1, z1, wf0, nop, nop, nop);
                if (base + 32 >= end) break;
                half_tile(xa, xb, a2, a3, ef, e1, i0, i1, z1, pkp, wf1, z0, wf0,
                          [&]() { stage_weights(valid && hh == 1); },
                          [&]() { wait_vm0(); geometry(); read_frags(ua); },
                          [&]() { if (base + 96 < end) issue_rows(base + 96); });
                half_tile(xb, xa, a0, a1, ef, e0, i0, i1, z0, pkp, wf1, z1, wf0, nop, nop, nop);
                base += 64;
                if (base >= end) break;
            }
            z1 = MFMA32(__builtin_bit_cast(bf16x8, pkp), wf1, z1);
        }
        CSTAMP(13);
        QSTAMP(7);
        float cs0 = 0.f, cs1 = 0.f;
#pragma unroll
        for (int g = 0; g < 4; ++g) {
            const f32x4 w0 = *(const f32x4*)(sm + L_WX + (8 * g + 4 * hh) * 4);
            const f32x4 w1 = *(const f32x4*)(sm + L_WX + (32 + 8 * g + 4 * hh) * 4);
#pragma unroll
            for (int i = 0; i < 4; ++i) { cs0 += z0[4 * g + i] * w0[i]; cs1 += z1[4 * g + i] * w1[i]; }
        }
        const float csh = cs0 + cs1;
        const auto cw = __builtin_amdgcn_permlane32_swap(__float_as_uint(csh), __float_as_uint(csh), false, false);
        const float cst = __uint_as_float(cw[0]) + __uint_as_float(cw[1]);
        wait_vm0();
        if (hh == 0) ((float*)(part + PPOS))[r] = cst;
        if ((lane & 3) == 0) {
            const int n = r >> 2;
#pragma unroll
            for (int g = 0; g < 4; ++g) {
                *(f32x4*)(part + n * PROW + (8 * g + 4 * hh) * 4) = f32x4{z0[4 * g], z0[4 * g + 1], z0[4 * g + 2], z0[4 * g + 3]};
                *(f32x4*)(part + n * PROW + (32 + 8 * g + 4 * hh) * 4) = f32x4{z1[4 * g], z1[4 * g + 1], z1[4 * g + 2], z1[4 * g + 3]};
            }
        }
    } else {
        asm volatile("s_waitcnt vmcnt(0)" : "+v"(pf) :: "memory");
        {
            const u32x4 z = {0, 0, 0, 0};
            *(u32x4*)(part + lane * 16) = z;
            *(u32x4*)(part + 1024 + lane * 16) = z;
            *(u32x4*)(part + 2048 + lane * 16) = z;
        }
        bar();
        const float wd = bf2f(we[128 * 64 + lane]), wa = bf2f(we[129 * 64 + lane]);
        const float wxl = ((const float*)(sm + L_WX))[lane];
        const unsigned* T = (const unsigned*)(ws + W_T) + b * 256;
        for (int ci = wv; ci < NCH; ci += K2_W) {
            const unsigned tv = T[(ci & 7) * 32 + (ci >> 3)];
            const unsigned g0 = ci * CE + (tv & 0xffffu), cn = tv >> 16;
            for (unsigned i = 0; i < cn; ++i) {
                const unsigned rc = R32[g0 + i], cd = R8[g0 + i];
                const unsigned s = rc & 0xffffu, w = cd >> 3, n = cd & 7, dl = win_start(w) + n;
                const f32x4 ps = *(const f32x4*)(Ptab + s * 16);
                const f32x4 pd = *(const f32x4*)(sm + L_PD + dl * 16);
                const float dx = pd[0] - ps[0], dy = pd[1] - ps[1], dz = pd[2] - ps[2];
                const unsigned e = pk_bf16((dx * dx + dy * dy + dz * dz) * NEG_LOG2E, __uint_as_float(rc & 0xffff0000u) * NEG_LOG2E);
                const unsigned dxy = pk_bf16(dx, dy), dz0 = pk_bf16(dz, 0.f);
                float y = bf2f(((const uint16_t*)(sm + L_AIMG))[dl * 64 + lane]) + bf2f(((const uint16_t*)Btab)[s * 64 + lane]);
                y += bf2f(e & 0xffffu) * wd + bf2f(e >> 16) * wa;
                const float m = bf2f(pk_bf16(y * rcp(1.0f + ex2(y)), 0.f) & 0xffffu);
                char* tgt = sm + L_SLOT + 2 * w * 4096;
                atomicAdd((float*)(tgt + n * PROW) + lane, m);
                float t = m * wxl;
                for (int o = 32; o > 0; o >>= 1) t += __shfl_xor(t, o);
                if (lane < 3) {
                    const float dv = lane == 0 ? bf2f(dxy & 0xffffu) : lane == 1 ? bf2f(dxy >> 16) : bf2f(dz0 & 0xffffu);
                    atomicAdd((float*)(tgt + PPOS) + 4 * n + 1 + lane, dv * t);
                }
            }
        }
    }
    const int ct = wv & 3, g = lane >> 4, node = 16 * (wv >> 2) + (lane & 15), nodec = node < BN ? node : BN - 1;
    bf16x8 wfr[4];
#pragma unroll
    for (int s = 0; s < 4; ++s) wfr[s] = wfrag16(sm + L_WH, s, ct, lane);
    f32x4 acc = {0.f, 0.f, 0.f, 0.f};
    acc = MFMA16(wfr[0], nfrag16(sm + L_HIMG, nodec, g), acc);
    acc = MFMA16(wfr[1], nfrag16(sm + L_HIMG, nodec, 4 + g), acc);
    const u32x2 hv = *(const u32x2*)(sm + L_HIMG + nodec * 128 + 16 * ((2 * ct + (g >> 1)) ^ ((nodec >> 1) & 7)) + 8 * (g & 1));
    const unsigned nwin = win_of(nodec);
    const char* pb = sm + L_SLOT + 2 * nwin * 4096 + (nodec - win_start(nwin)) * PROW + g * 32;
    QSTAMP(8);
    bar();
    QSTAMP(9);
#pragma unroll
    for (int s = 0; s < 2; ++s) {
        const f32x4 m0 = (*(const f32x4*)(pb + s * 128) + *(const f32x4*)(pb + s * 128 + 4096)) * NEG_LN2;
        const f32x4 m1 = (*(const f32x4*)(pb + s * 128 + 16) + *(const f32x4*)(pb + s * 128 + 16 + 4096)) * NEG_LN2;
        acc = MFMA16(wfr[2 + s], as_frag(pk_bf16(m0[0], m0[1]), pk_bf16(m0[2], m0[3]), pk_bf16(m1[0], m1[1]), pk_bf16(m1[2], m1[3])), acc);
    }
    if (wv >= 10) {
        const int x = (wv - 10) * 64 + lane;
        if (x < 3 * BN) {
            const int nl = (x * 171) >> 9, cc = x - 3 * nl, w = win_of(nl), n = nl - win_start(w);
            const char* pp = sm + L_SLOT + 2 * w * 4096 + PPOS + (4 * n + 1 + cc) * 4;
            const float sum = *(const float*)pp + *(const float*)(pp + 4096);
            const float p0 = ((const float*)(sm + L_PD))[nl * 4 + cc];
            out[NN * 64 + (b * BN + nl) * 3 + cc] = (uint16_t)(pk_bf16(p0 + sum, 0.f) & 0xffffu);
        }
    }
    QSTAMP(10);
    if (node < BN) {
        const float o0 = bf2f(hv[0] & 0xffffu) + silu_f(acc[0]), o1 = bf2f(hv[0] >> 16) + silu_f(acc[1]);
        const float o2 = bf2f(hv[1] & 0xffffu) + silu_f(acc[2]), o3 = bf2f(hv[1] >> 16) + silu_f(acc[3]);
        st_nt(out + (b * BN + node) * 64 + 16 * ct + 4 * g, u32x2{pk_bf16(o0, o1), pk_bf16(o2, o3)});
    }
    QSTAMP(11);
    STAMP_OUT(1, ws);
}

}

extern "C" void kernel_launch(void* const* d_in, const int* in_sizes, int n_in, void* d_out, int out_size, void* d_ws,
                              size_t ws_size, hipStream_t stream) {
    using namespace egnn;
    const uint16_t* h = (const uint16_t*)d_in[0];
    const uint16_t* pos = (const uint16_t*)d_in[1];
    const uint16_t* ea = (const uint16_t*)d_in[2];
    const int* src = (const int*)d_in[3];
    const int* dst = (const int*)d_in[4];
    const uint16_t* we = (const uint16_t*)d_in[5];
    const uint16_t* wx = (const uint16_t*)d_in[6];
    const uint16_t* wh = (const uint16_t*)d_in[7];
    (void)hipFuncSetAttribute((const void*)k_edge, hipFuncAttributeMaxDynamicSharedMemorySize, L_END);
    k_sort<<<NCH, K1_T, 0, stream>>>(src, dst, ea, h, pos, we, wx, (char*)d_ws);
    k_edge<<<256, K2_T, L_END, stream>>>(h, we, wh, (const char*)d_ws, (uint16_t*)d_out);
}
```

```cpp
#include <hip/hip_runtime.h>
#include <stdint.h>

namespace egnn {

typedef short    bf16x8 __attribute__((ext_vector_type(8)));
typedef short    s16x4  __attribute__((ext_vector_type(4)));
typedef float    f32x16 __attribute__((ext_vector_type(16)));
typedef float    f32x4  __attribute__((ext_vector_type(4)));
typedef float    f32x2  __attribute__((ext_vector_type(2)));
typedef __bf16   bf16x2 __attribute__((ext_vector_type(2)));
typedef unsigned u32x4  __attribute__((ext_vector_type(4)));
typedef unsigned u32x2  __attribute__((ext_vector_type(2)));
#define LDS_AS __attribute__((address_space(3)))
#define DI __device__ __forceinline__
#define SBAR() __builtin_amdgcn_sched_barrier(0)
#define PIN(x) asm volatile("" : "+v"(x))
#define MFMA32(a, b, c) __builtin_amdgcn_mfma_f32_32x32x16_bf16(a, b, c, 0, 0, 0)
#define MFMA16(a, b, c) __builtin_amdgcn_mfma_f32_16x16x32_bf16(a, b, c, 0, 0, 0)

constexpr int NN = 10000;
constexpr int NE = 640000;
constexpr int BN = 40;
constexpr int NBK = 250;
constexpr int CE = 2560;
constexpr int NCH = 250;
constexpr float NEG_LOG2E = -1.4426950408889634f;
constexpr float NEG_LN2 = -0.6931471805599453f;

constexpr size_t W_R   = 0;
constexpr size_t W_T   = 4u << 20;
constexpr size_t W_B   = (4u << 20) + (512u << 10);
constexpr size_t W_P   = 6u << 20;
constexpr size_t W_C   = (6u << 20) + (512u << 10);

DI unsigned win_of(unsigned dl) { return ((dl * 37u) >> 8) + (dl == 34u ? 1u : 0u); }
DI unsigned win_start(unsigned w) { return 7u * w - (w == 5u ? 1u : 0u); }

DI int wave_a(unsigned w) { return (0x987520u >> (4 * w)) & 15; }
DI int wave_b(unsigned w) { return (0xBA4631u >> (4 * w)) & 15; }

DI float bf2f(unsigned v16) { return __uint_as_float(v16 << 16); }
DI unsigned pk_bf16(float lo, float hi) {
    f32x2 x = {lo, hi};
    return __builtin_bit_cast(unsigned, __builtin_convertvector(x, bf16x2));
}
DI float silu_f(float x) {
    return x * __builtin_amdgcn_rcpf(1.0f + __builtin_amdgcn_exp2f(x * NEG_LOG2E));
}
DI s16x4 tr_read(const char* p) {
    return __builtin_amdgcn_ds_read_tr16_b64_v4i16((LDS_AS s16x4*)p);
}
DI bf16x8 cat8(s16x4 a, s16x4 b) { return __builtin_shufflevector(a, b, 0, 1, 2, 3, 4, 5, 6, 7); }
DI void wait_vm0() { asm volatile("s_waitcnt vmcnt(0)" ::: "memory"); }
DI void wait_lgkm0() { asm volatile("s_waitcnt lgkmcnt(0)" ::: "memory"); }
DI void bar() { asm volatile("s_waitcnt lgkmcnt(0)\n\ts_barrier" ::: "memory"); }

DI void dma1(unsigned ldsaddr, const void* gbase, unsigned voff) {
    unsigned keep;
    asm volatile("s_mov_b32 %0, m0\n\ts_mov_b32 m0, %1\n\ts_nop 0\n\t"
                 "global_load_lds_dwordx4 %2, %3\n\t"
                 "s_mov_b32 m0, %0"
                 : "=&s"(keep) : "s"(ldsaddr), "v"(voff), "s"(gbase) : "memory");
}
DI void dma4(unsigned ldsaddr, const void* gbase, unsigned v0, unsigned v1, unsigned v2, unsigned v3) {
    unsigned keep;
    asm volatile("s_mov_b32 %0, m0\n\ts_mov_b32 m0, %1\n\ts_nop 0\n\t"
                 "global_load_lds_dwordx4 %2, %6\n\t"
                 "global_load_lds_dwordx4 %3, %6 offset:1024\n\t"
                 "global_load_lds_dwordx4 %4, %6 offset:2048\n\t"
                 "global_load_lds_dwordx4 %5, %6 offset:3072\n\t"
                 "s_mov_b32 m0, %0"
                 : "=&s"(keep) : "s"(ldsaddr), "v"(v0), "v"(v1), "v"(v2), "v"(v3), "s"(gbase) : "memory");
}
template <typename T> DI void st_nt(void* p, T v) { __builtin_nontemporal_store(v, (T*)p); }
DI unsigned div3(unsigned x) { return (x * 43691u) >> 17; }
DI unsigned mad16(unsigned x, unsigned m, unsigned a) {
    unsigned r;
    asm("v_mad_u32_u16 %0, %1, %2, %3" : "=v"(r) : "v"(x), "s"(m), "v"(a));
    return r;
}
template <typename T> DI T lds_ld(unsigned a) { return *(const LDS_AS T*)(size_t)a; }
template <typename T> DI void lds_st(unsigned a, T v) { *(LDS_AS T*)(size_t)a = v; }
template <int CTRL, int ROWMASK> DI unsigned dpp_add(unsigned x) {
    return x + (unsigned)__builtin_amdgcn_update_dpp(0, (int)x, CTRL, ROWMASK, 0xf, true);
}
DI unsigned scan32(unsigned x) {
    x = dpp_add<0x111, 0xf>(x); x = dpp_add<0x112, 0xf>(x); x = dpp_add<0x114, 0xf>(x); x = dpp_add<0x118, 0xf>(x);
    x = dpp_add<0x142, 0xa>(x);
    return x;
}
DI unsigned scan64(unsigned x) { x = scan32(x); x = dpp_add<0x143, 0xc>(x); return x; }
DI int wswz(int k) { return (k & 2) | ((k >> 1) & 4); }
DI bf16x8 wfrag16(const char* img, int s, int ct, int lane) {
    const int g = lane >> 4, q = (lane & 15) >> 2, p = lane & 3;
    const int k0 = 32 * s + 8 * g + q, k1 = k0 + 4, chn = 2 * ct + (p >> 1);
    return cat8(tr_read(img + k0 * 128 + 16 * (chn ^ wswz(k0)) + 8 * (p & 1)),
                tr_read(img + k1 * 128 + 16 * (chn ^ wswz(k1)) + 8 * (p & 1)));
}
DI bf16x8 nfrag16(const char* img, int node, int chunk) {
    return *(const bf16x8*)(img + node * 128 + 16 * (chunk ^ ((node >> 1) & 7)));
}

#define PSTAMP(i)
#define QSTAMP(i)
#define STAMP_DECL
#define STAMP(i)
#define CSTAMP(i)
#define STAMP_OUT(kern, wsp)

constexpr int K1_T = 640;
constexpr int S_CNT = 0, S_OFF = 1024, S_TOT = 2048, S_R = 2064, S_W = S_R + 16384, S_H = S_W + 8192, S_O = S_H + 6144, S_END = S_O + 5120;

__global__ __launch_bounds__(K1_T) void k_sort(const int* __restrict__ src, const int* __restrict__ dst,
                                               const uint16_t* __restrict__ ea, const uint16_t* __restrict__ h,
                                               const uint16_t* __restrict__ pos, const uint16_t* __restrict__ we,
                                               const uint16_t* __restrict__ wx, char* __restrict__ ws) {
    __shared__ __attribute__((aligned(16))) char sm[S_END];
    const int tid = threadIdx.x, lane = tid & 63;
    const int wv = __builtin_amdgcn_readfirstlane(tid >> 6);
    const int c = blockIdx.x;
    asm volatile("" :: "s"(src), "s"(dst), "s"(ea), "s"(h), "s"(pos), "s"(we), "s"(wx), "s"(ws));
    const unsigned smb = (unsigned)(size_t)(LDS_AS char*)sm;
    STAMP_DECL
    STAMP(0);

    const int e0 = c * CE + 4 * tid;
    const u32x4 d4 = *(const u32x4*)(dst + e0);
    const u32x4 s4 = *(const u32x4*)(src + e0);
    const u32x2 a4 = *(const u32x2*)(ea + e0);
    unsigned tw;
    {
        const unsigned o = lane == 0 ? (unsigned)W_R + c * 16384
                         : lane == 2 ? (unsigned)W_T + c * 1024 : lane == 3 ? (unsigned)W_B + c * (BN * 128) : (unsigned)W_P + c * (BN * 16);
        asm volatile("global_load_dword %0, %1, %2" : "=v"(tw) : "v"(o), "s"(ws) : "memory");
    }
    const int pn = c * BN + (lane < BN ? lane : 0);
    const unsigned px = pos[pn * 3], py = pos[pn * 3 + 1], pz = pos[pn * 3 + 2];

    {
        const int k = (wv & 7) * 8 + (lane >> 3);
        const int row = (wv - 5) * 8 + (lane >> 3);
        if (wv < 8) dma1(smb + S_W + wv * 1024, we, (64 + k) * 128 + (((lane & 7) ^ wswz(k)) * 16));
        if (wv >= 5) dma1(smb + S_H + (wv - 5) * 1024, h, (c * BN + row) * 128 + (((lane & 7) ^ ((row >> 1) & 7)) * 16));
    }
    if (tid < 256) ((unsigned*)(sm + S_CNT))[tid] = 0;
    bar();
    STAMP(1);

    unsigned bk[4], rk[4];
#pragma unroll
    for (int i = 0; i < 4; ++i) {
        bk[i] = (d4[i] * 52429u) >> 21;
        rk[i] = atomicAdd((unsigned*)(sm + S_CNT) + bk[i], 1u);
    }
    unsigned char code[4];
#pragma unroll
    for (int i = 0; i < 4; ++i) {
        const unsigned dl = d4[i] - bk[i] * BN, w = win_of(dl);
        code[i] = (unsigned char)(w * 8 + dl - win_start(w));
    }
    STAMP(2);
    asm volatile("s_waitcnt vmcnt(0)" : "+v"(tw) :: "memory");
    bar();
    STAMP(3);

    if (wv == 9) {
        const u32x4 cn = *(const u32x4*)(sm + S_CNT + lane * 16);
        const u32x4 tr = {div3(cn[0] + 2), div3(cn[1] + 2), div3(cn[2] + 2), div3(cn[3] + 2)};
        const unsigned tot = tr[0] + tr[1] + tr[2] + tr[3];
        const unsigned in = scan64(tot), ex = in - tot;
        const u32x4 of = {ex, ex + tr[0], ex + tr[0] + tr[1], ex + tr[0] + tr[1] + tr[2]};
        *(u32x4*)(sm + S_OFF + lane * 16) = of;
        if (lane == 63) *(unsigned*)(sm + S_TOT) = in;
        char* T = ws + W_T + ((c & 7) * 32 + (c >> 3)) * 4 + lane * 4096;
#pragma unroll
        for (int i = 0; i < 4; ++i)
            if (4 * lane + i < NBK) st_nt(T + i * 1024, of[i] | (cn[i] << 16));
    } else if (wv == 8) {
        if (lane < BN) st_nt(ws + W_P + (size_t)pn * 16, f32x4{bf2f(px), bf2f(py), bf2f(pz), 0.f});
        if (c == 0) {
            const u32x4 z = {0, 0, 0, 0};
            st_nt(ws + W_P + (size_t)NN * 16 + lane * 16, z);
            if (lane < 8) st_nt(ws + W_B + (size_t)NN * 128 + lane * 16, z);
            st_nt(ws + W_C + lane * 16, lane < 16 ? *(const u32x4*)(we + 128 * 64 + lane * 8) : z);
            const int n = lane >> 2, j = lane & 3;
            st_nt(ws + W_C + 1024 + lane * 4, (n < 8 && (n >> 1) == j) ? (0x3F80u << (16 * (n & 1))) : 0u);
            st_nt(ws + W_C + 1280 + lane * 4, bf2f(wx[lane]) * NEG_LN2);
        }
    }
    STAMP(4);
    bar();
    STAMP(5);

#pragma unroll
    for (int i = 0; i < 4; ++i) {
        const unsigned q = div3(rk[i]), j = rk[i] - 3 * q;
        const unsigned t = smb + S_R + (((const unsigned*)(sm + S_OFF))[bk[i]] + q) * 16;
        const unsigned av = (i & 1) ? (a4[i >> 1] & 0xffff0000u) : (a4[i >> 1] << 16);
        lds_st(t + j * 4, s4[i] | av);
        lds_st(t + 12 + j, code[i]);
    }
    bar();
    STAMP(6);
    {
        const unsigned ntot = *(const unsigned*)(sm + S_TOT);
        char* R = ws + W_R + (size_t)c * 16384;
        if (tid < ntot) st_nt(R + tid * 16, *(const u32x4*)(sm + S_R + tid * 16));
        if (tid + K1_T < ntot) st_nt(R + (tid + K1_T) * 16, *(const u32x4*)(sm + S_R + (tid + K1_T) * 16));
    }
    STAMP(7);

    if (wv < 8) {
        const int ct = wv & 3, g = lane >> 4;
        const bf16x8 a0 = wfrag16(sm + S_W, 0, ct, lane), a1 = wfrag16(sm + S_W, 1, ct, lane);
        for (int nt = wv >> 2; nt < 3; nt += 2) {
            const int node = 16 * nt + (lane & 15);
            f32x4 acc = {0.f, 0.f, 0.f, 0.f};
            acc = MFMA16(a0, nfrag16(sm + S_H, node, g), acc);
            acc = MFMA16(a1, nfrag16(sm + S_H, node, 4 + g), acc);
            const u32x2 o = {pk_bf16(acc[0] * NEG_LOG2E, acc[1] * NEG_LOG2E), pk_bf16(acc[2] * NEG_LOG2E, acc[3] * NEG_LOG2E)};
            if (node < BN) *(u32x2*)(sm + S_O + node * 128 + 16 * ((2 * ct + (g >> 1)) ^ (node & 7)) + 8 * (g & 1)) = o;
        }
    }
    bar();
    if (tid < BN * 8) {
        const int node = tid >> 3, ch = (tid & 7) ^ (node & 7);
        st_nt(ws + W_B + (size_t)(c * BN + node) * 128 + ch * 16, *(const u32x4*)(sm + S_O + tid * 16));
    }
    STAMP(8);
    STAMP_OUT(0, ws);
}

constexpr int K2_W = 12, K2_T = 64 * K2_W;
constexpr int RCAP = 3072;
constexpr int L_SLOT = 0;
constexpr int L_STAGE = 49152;
constexpr int RALL = RCAP + 128;
constexpr int L_R32 = 73728;
constexpr int L_R8 = L_R32 + RALL * 4;
constexpr int L_AIMG = L_R8 + RALL;
constexpr int L_HIMG = L_AIMG + 6144;
constexpr int L_W1 = L_HIMG + 6144;
constexpr int L_WH = L_W1 + 8192;
constexpr int L_PD = L_WH + 16384;
constexpr int L_OH = L_PD + 1024;
constexpr int L_WX = L_OH + 256;
constexpr int L_BINS = L_OH + 1024;
constexpr int L_OFFS = L_BINS + 896;
constexpr int L_WIN = L_OFFS + 896;
constexpr int L_WF = L_WIN + 64;
constexpr int L_PB = L_WF + 64;
constexpr int L_END = L_PB + K2_W * 1024;
constexpr unsigned MAPCAP = 128;
constexpr int PROW = 272, PPOS = 8 * PROW;

DI bf16x8 as_frag(unsigned a, unsigned b, unsigned c, unsigned d) {
    u32x4 v = {a, b, c, d};
    return __builtin_bit_cast(bf16x8, v);
}
DI float ex2(float x) { return __builtin_amdgcn_exp2f(x); }
DI float rcp(float x) { return __builtin_amdgcn_rcpf(x); }

template <class H1, class H2, class H3>
DI void half_tile(const f32x16& xc, f32x16& xn, const bf16x8& aA, const bf16x8& aB, const bf16x8& ef, const bf16x8& eB,
                  const bf16x8& i0, const bf16x8& i1, f32x16& zp, u32x4& pkp, const bf16x8& wp, f32x16& zc,
                  const bf16x8& wc, H1&& hook1, H2&& hook2, H3&& hook3) {
    f32x16 zero;
#pragma unroll
    for (int i = 0; i < 16; ++i) zero[i] = 0.f;
    f32x2 e0, e1, e2, e3;
    u32x4 pk;
#define EXP2(e, i) do { e[0] = ex2(xc[i]); PIN(e[0]); } while (0)
#define EXP2H(e, i) do { e[1] = ex2(xc[i]); PIN(e[1]); } while (0)
#define RCP2(e) do { e[0] = rcp(e[0]); PIN(e[0]); } while (0)
#define RCP2H(e) do { e[1] = rcp(e[1]); PIN(e[1]); } while (0)
#define PKMODE 0
#define ADD1(e) do { e = e + 1.0f; PIN(e); } while (0)
#define MULX(e, i) do { e = e * f32x2{xc[i], xc[i + 1]}; PIN(e); } while (0)
#define CVT(d, e) do { d = __builtin_bit_cast(unsigned, __builtin_convertvector(e, bf16x2)); PIN(d); } while (0)
    xn = MFMA32(aA, i0, zero); SBAR();
    EXP2(e0, 0); EXP2H(e0, 1); EXP2(e1, 2); SBAR();
    zp = MFMA32(__builtin_bit_cast(bf16x8, pkp), wp, zp); SBAR();
    EXP2H(e1, 3); EXP2(e2, 4); EXP2H(e2, 5); EXP2(e3, 6); EXP2H(e3, 7); SBAR();
    ADD1(e0); ADD1(e1); ADD1(e2); ADD1(e3); SBAR();
    hook1(); SBAR();
    xn = MFMA32(aB, i1, xn); SBAR();
    RCP2(e0); RCP2H(e0); RCP2(e1); SBAR();
    xn = MFMA32(ef, eB, xn); SBAR();
    RCP2H(e1); RCP2(e2); RCP2H(e2); RCP2(e3); RCP2H(e3); SBAR();
    MULX(e0, 0); MULX(e1, 2); MULX(e2, 4); MULX(e3, 6); SBAR();
    CVT(pk[0], e0); CVT(pk[1], e1); CVT(pk[2], e2); CVT(pk[3], e3); SBAR();
    EXP2(e0, 8); EXP2H(e0, 9); SBAR();
    zc = MFMA32(__builtin_bit_cast(bf16x8, pk), wc, zc); SBAR();
    EXP2(e1, 10); EXP2H(e1, 11); EXP2(e2, 12); EXP2H(e2, 13); EXP2(e3, 14); EXP2H(e3, 15); SBAR();
    ADD1(e0); ADD1(e1); ADD1(e2); ADD1(e3); SBAR();
    hook2(); SBAR();
    RCP2(e0); RCP2H(e0); RCP2(e1); RCP2H(e1); RCP2(e2); RCP2H(e2); RCP2(e3); RCP2H(e3); SBAR();
    hook3(); SBAR();
    MULX(e0, 8); MULX(e1, 10); MULX(e2, 12); MULX(e3, 14); SBAR();
    CVT(pkp[0], e0); CVT(pkp[1], e1); CVT(pkp[2], e2); CVT(pkp[3], e3); SBAR();
#undef EXP2
#undef EXP2H
#undef RCP2
#undef RCP2H
#undef ADD1
#undef MULX
#undef CVT
}

__global__ __launch_bounds__(K2_T) void k_edge(const uint16_t* __restrict__ h, const uint16_t* __restrict__ we,
                                               const uint16_t* __restrict__ wh, const char* __restrict__ ws,
                                               uint16_t* __restrict__ out) {
    extern __shared__ __attribute__((aligned(16))) char sm[];
    const int tid = threadIdx.x, lane = tid & 63;
    const int wv = __builtin_amdgcn_readfirstlane(tid >> 6);
    const int b = (blockIdx.x & 7) * 32 + (blockIdx.x >> 3);
    if (b >= NBK) return;
    const unsigned smb = (unsigned)(size_t)(LDS_AS char*)sm;
    asm volatile("" :: "s"(h), "s"(we), "s"(wh), "s"(ws), "s"(out));
    const char* Rec = ws + W_R;
    const char* Btab = ws + W_B;
    const char* Ptab = ws + W_P;
    STAMP_DECL
    STAMP(0);

    const int l3 = (lane * 171) >> 9, sub = lane - 3 * l3;
    const int cid = 21 * wv + l3;
    const bool cok = lane < 63 && cid < NCH;
    const int cidc = cok ? cid : 0;
    unsigned tval;
    asm volatile("global_load_dword %0, %1, %2" : "=v"(tval)
                 : "v"((unsigned)W_T + (b * 256 + (cidc & 7) * 32 + (cidc >> 3)) * 4), "s"(ws) : "memory");

    {
        const int r8l = lane >> 3, c8 = lane & 7;
        auto wpiece = [&](const uint16_t* w, int q, int la) {
            dma1(smb + la + q * 1024, w, (8 * q + r8l) * 128 + ((c8 ^ ((r8l & 2) | ((q & 1) << 2))) * 16));
        };
        if (wv < 5) dma1(smb + L_HIMG + wv * 1024, h, (b * BN + 8 * wv + r8l) * 128 + ((c8 ^ ((4 * wv + (lane >> 4)) & 7)) * 16));
        else wpiece(we, wv - 5, L_W1);
        if (wv >= 4) wpiece(wh, wv - 4, L_WH);
        else if (wv == 0) wpiece(we, 7, L_W1);
        else {
            const unsigned go = wv == 1 ? (unsigned)W_P + b * BN * 16 : (unsigned)W_C + (wv - 2) * 1024;
            dma1(smb + (wv == 1 ? L_PD : wv == 2 ? L_AIMG + BN * 128 : L_OH), ws, go + lane * 16);
        }
    }
    unsigned tw;
    asm volatile("global_load_dword %0, %1, %2" : "=v"(tw)
                 : "v"(lane == 0 ? b * (BN * 128) : (NN * 128 + b * (BN * 6)) & ~3), "s"(out) : "memory");

    {
        const u32x4 z = {0, 0, 0, 0};
        lds_st(smb + L_STAGE + wv * 2048 + lane * 16, z);
        lds_st(smb + L_STAGE + wv * 2048 + 1024 + lane * 16, z);
        const u32x4 n32 = {NN, NN, NN, NN}, n8 = {0x08080808u, 0x08080808u, 0x08080808u, 0x08080808u};
        lds_st(smb + L_R32 + tid * 16, n32);
        if (wv == 0) {
            if (lane < RALL / 4 - K2_T) lds_st(smb + L_R32 + (K2_T + lane) * 16, n32);
        } else if (wv < 5) {
            if (tid - 64 < RALL / 16) lds_st(smb + L_R8 + (tid - 64) * 16, n8);
        } else if (wv == 5) {
            if (lane < 56) lds_st(smb + L_BINS + lane * 16, z);
        }
    }
    PSTAMP(1);
    bar();
    asm volatile("s_waitcnt vmcnt(3)" : "+v"(tval) :: "memory");
    PSTAMP(2);

    const unsigned cnt = cok ? (tval >> 16) : 0u;
    const unsigned ntr = div3(cnt + 2);
    const unsigned incl = scan64(sub == 0 ? ntr : 0u);
    const unsigned mw = __builtin_amdgcn_readlane(incl, 63);
    const bool slow_w = mw > MAPCAP;
    const unsigned map_a = smb + L_SLOT + wv * 4096;
    QSTAMP(1);
    if (!slow_w) {
        unsigned g = (cidc * 1024 + (tval & 0xffffu) + sub) | ((cnt - 3 * sub) << 20);
        unsigned mp = map_a + (incl - ntr + sub) * 4;
#pragma clang loop unroll(disable) vectorize(disable) interleave(disable)
        for (unsigned i = sub; i < ntr; i += 3) { lds_st(mp, g); mp += 12; g += 3 - (9u << 20); }
    }
    PSTAMP(3);
    const unsigned mwe = slow_w ? 0u : mw;
    unsigned left[2];
    u32x4 rec[2];
#pragma unroll
    for (int k = 0; k < 2; ++k) {
        const unsigned m = lds_ld<unsigned>(map_a + (64 * k + lane) * 4);
        const bool ok = (unsigned)(64 * k + lane) < mwe;
        left[k] = ok ? m >> 20 : 0u;
        const unsigned go = ok ? (m & 0xfffffu) * 16 : 0u;
        asm volatile("global_load_dwordx4 %0, %1, %2" : "=v"(rec[k]) : "v"(go), "s"(Rec) : "memory");
    }
    {
        const int q = 8 + (wv & 7), r8l = lane >> 3;
        dma1(smb + L_WH + q * 1024, wh, (8 * q + r8l) * 128 + (((lane & 7) ^ ((r8l & 2) | ((q & 1) << 2))) * 16));
    }
    if (lane == 0) lds_st(smb + L_WF + wv * 4, slow_w ? 1u : 0u);
    PSTAMP(4);
    asm volatile("s_waitcnt vmcnt(0)" : "+v"(rec[0]), "+v"(rec[1]), "+v"(tw) :: "memory");
    PSTAMP(5);
    unsigned bin[6], rk[6];
#pragma unroll
    for (int k = 0; k < 6; ++k) {
        const unsigned w = (rec[k / 3][3] >> (8 * (k % 3) + 3)) & 7u;
        bin[k] = (left[k / 3] > (unsigned)(k % 3) ? w : 6u) * 128 + (lane & 31) * 4;
        rk[k] = atomicAdd((unsigned*)(sm + L_BINS + bin[k]), 1u);
    }
    unsigned pf;
    {
        const unsigned part = blockIdx.x >> 3;
        const unsigned off = tid < 626 ? (unsigned)W_B + part * (313 * 128) + tid * 64
                                       : (unsigned)W_P + part * (40 * 128) + (tid < 706 ? tid - 626 : 0) * 64;
        asm volatile("global_load_dword %0, %1, %2" : "=v"(pf) : "v"(off), "s"(ws) : "memory");
    }
    QSTAMP(3);
    PSTAMP(6);
    bar();
    PSTAMP(7);

    if (wv == 0) {
        const unsigned* bins = (const unsigned*)(sm + L_BINS);
        const int j = lane & 31, hi = lane >> 5;
        const unsigned ca = bins[(2 * hi) * 32 + j] | (bins[(2 * hi + 1) * 32 + j] << 16);
        const unsigned cb = bins[4 * 32 + j] | (bins[5 * 32 + j] << 16);
        const unsigned wfl = lds_ld<unsigned>(smb + L_WF + (lane < K2_W ? lane : 0) * 4);
        const unsigned ia = scan32(ca), ib = scan32(cb);
        const unsigned t01 = __builtin_amdgcn_readlane(ia, 31), t23 = __builtin_amdgcn_readlane(ia, 63);
        const unsigned t45 = __builtin_amdgcn_readlane(ib, 31);
        const unsigned tot[6] = {t01 & 0xffffu, t01 >> 16, t23 & 0xffffu, t23 >> 16, t45 & 0xffffu, t45 >> 16};
        unsigned st[7];
        st[0] = 0;
#pragma unroll
        for (int w = 0; w < 6; ++w) st[w + 1] = st[w] + ((tot[w] + 31) & ~31u);
        const unsigned xa = ia - ca, xb = ib - cb;
        unsigned* offs = (unsigned*)(sm + L_OFFS);
        offs[(2 * hi) * 32 + j] = (hi ? st[2] : st[0]) + (xa & 0xffffu);
        offs[(2 * hi + 1) * 32 + j] = (hi ? st[3] : st[1]) + (xa >> 16);
        offs[(4 + hi) * 32 + j] = hi ? st[5] + (xb >> 16) : st[4] + (xb & 0xffffu);
        const bool any_slow = __any(wfl != 0);
        unsigned wi = 0;
#pragma unroll
        for (int w = 0; w < 6; ++w) { wi = lane == w ? st[w] : wi; wi = lane == 6 + w ? (st[w + 1] - st[w]) >> 5 : wi; }
        wi = lane == 12 ? ((any_slow || st[6] > RCAP) ? 1u : 0u) : wi;
        if (lane < 13) ((unsigned*)(sm + L_WIN))[lane] = wi;
    } else {
        const int ct = wv & 3, g = lane >> 4;
        const bf16x8 a0 = wfrag16(sm + L_W1, 0, ct, lane), a1 = wfrag16(sm + L_W1, 1, ct, lane);
        for (int nt = wv >> 2; ; nt = 0) {
            const int node = 16 * nt + (lane & 15);
            f32x4 acc = {0.f, 0.f, 0.f, 0.f};
            acc = MFMA16(a0, nfrag16(sm + L_HIMG, node, g), acc);
            acc = MFMA16(a1, nfrag16(sm + L_HIMG, node, 4 + g), acc);
            const u32x2 o = {pk_bf16(acc[0] * NEG_LOG2E, acc[1] * NEG_LOG2E), pk_bf16(acc[2] * NEG_LOG2E, acc[3] * NEG_LOG2E)};
            if (node < BN) *(u32x2*)(sm + L_AIMG + node * 128 + (16 * ct + 4 * g) * 2) = o;
            if (wv != 4 || nt == 0) break;
        }
    }
    PSTAMP(8);
    bar();
    QSTAMP(4);
    PSTAMP(9);
    const bool slow = __builtin_amdgcn_readfirstlane(((const unsigned*)(sm + L_WIN))[12]) != 0;

    const int r = lane & 31, hh = lane >> 5;
    const int win = (int)((0x545432231100ull >> (4 * wv)) & 7);
    const bool second = (0xC5A >> wv) & 1;
    const int wnode = win_start(win);
    char* part = sm + L_SLOT + wv * 4096;

    if (!slow) {
#pragma unroll
        for (int k = 0; k < 6; ++k) {
            const unsigned o = lds_ld<unsigned>(smb + L_OFFS + bin[k]) + rk[k];
            const unsigned p = left[k / 3] > (unsigned)(k % 3) ? o : (unsigned)(RALL - 1);
            lds_st(smb + L_R32 + p * 4, rec[k / 3][k % 3]);
            lds_st(smb + L_R8 + p, (unsigned char)((rec[k / 3][3] >> (8 * (k % 3))) & 7u));
        }
        const unsigned wiv = lds_ld<unsigned>(smb + L_WIN + (lane & 15) * 4);
        int tw6[6];
#pragma unroll
        for (int w = 0; w < 6; ++w) tw6[w] = __builtin_amdgcn_readlane(wiv, 6 + w);
        const int wstart = __builtin_amdgcn_readlane(wiv, win);
        const int l0 = tw6[0] + tw6[3] + tw6[4], l1 = tw6[0] + tw6[2] + tw6[5];
        const int l2 = tw6[1] + tw6[2] + tw6[4], l3s = tw6[1] + tw6[3] + tw6[5];
        const int wtiles = win == 0 ? tw6[0] : win == 1 ? tw6[1] : win == 2 ? tw6[2] : win == 3 ? tw6[3] : win == 4 ? tw6[4] : tw6[5];
        const int la = win == 0 ? l0 : win == 1 ? l2 : win == 2 ? l1 : win == 3 ? l3s : win == 4 ? l0 : l1;
        const int lb = win == 0 ? l1 : win == 1 ? l3s : win == 2 ? l2 : win == 3 ? l0 : win == 4 ? l2 : l3s;
        int first = (4 * wtiles - la + lb + 4) >> 3;
        first = first < 0 ? 0 : first > wtiles ? wtiles : first;
        const int ta = second ? first : 0, tb = second ? wtiles : first;

        const int blk = (lane >> 4) & 1, q = (lane & 15) >> 2, p = lane & 3;
        bf16x8 i0, i1;
#pragma unroll
        for (int j = 0; j < 8; ++j) {
            i0[j] = (r == 8 * hh + j) ? (short)0x3F80 : (short)0;
            i1[j] = (r == 16 + 8 * hh + j) ? (short)0x3F80 : (short)0;
        }
        const int rho = 8 * (r & 3) + (r >> 2);
        const int fsw = (r & 3) ^ (((r >> 3) & 1) << 2);
        unsigned aad[4];
#pragma unroll
        for (int s = 0; s < 4; ++s) aad[s] = smb + L_SLOT + wv * 4096 + rho * 128 + 16 * ((2 * s + hh) ^ fsw);
        unsigned kk[4];
#pragma unroll
        for (int i = 0; i < 4; ++i)
            kk[i] = 16 * ((lane & 7) ^ i ^ (((lane >> 4) & 1) << 2)) + (3072 - 1024 * i);
        const char* gB = Btab - 3072;
        const unsigned slot_a = smb + L_SLOT + wv * 4096;
        const unsigned pbuf_a = smb + L_PB + wv * 1024;
        const unsigned src_a = smb + L_R32 + (lane >> 3) * 16, rs_a = smb + L_R32 + lane * 4, rn_a = smb + L_R8 + lane;
        const unsigned stage_r = smb + L_STAGE + wv * 2048 + r * 64;
        const char* trb = sm + L_STAGE + wv * 2048 + (4 * hh + q) * 64 + blk * 32 + p * 8;
        const unsigned pdw = smb + L_PD + wnode * 16;
        const unsigned oh_a = smb + L_OH;
        bar();
        QSTAMP(5);
        PSTAMP(10);

        bf16x8 e0, e1;
        {
            const int row = hh ? BN + q : wnode + q;
            const char* ab = sm + L_AIMG + row * 128 + (16 * blk + 4 * p) * 2;
            e0 = cat8(tr_read(ab), tr_read(ab + 512));
            e1 = cat8(tr_read(ab + 64), tr_read(ab + 64 + 512));
        }
        f32x16 z0, z1, xa, xb;
#pragma unroll
        for (int i = 0; i < 16; ++i) { z0[i] = 0.f; z1[i] = 0.f; }
        QSTAMP(6);
        CSTAMP(12);

        if (ta < tb) {
            int base = wstart + 32 * ta;
            const int end = wstart + 32 * tb;
            bf16x8 a0, a1, a2, a3, ef, wf0, wf1;
            unsigned rs, rn, w0, w1, ua, vb;
            unsigned saddr;
            bool valid;
            u32x4 pkp = {0, 0, 0, 0};
#pragma unroll
            for (int j = 0; j < 8; ++j) wf1[j] = 0;

            u32x4 sn, exr;
            f32x4 pd, ps;
            auto read_src = [&](int ri) { sn = lds_ld<u32x4>(src_a + ri * 4); };
            auto issue_rows = [&]() {
                const unsigned v0 = mad16(sn[0], 128, kk[0]), v1 = mad16(sn[1], 128, kk[1]);
                const unsigned v2 = mad16(sn[2], 128, kk[2]), v3 = mad16(sn[3], 128, kk[3]);
                wait_lgkm0();
                dma4(slot_a, gB, v0, v1, v2, v3);
            };
            auto read_pair = [&](int ri) {
                rs = lds_ld<unsigned>(rs_a + ri * 4);
                rn = lds_ld<unsigned char>(rn_a + ri);
            };
            auto issue_pos = [&]() { dma1(pbuf_a, Ptab, mad16(rs, 16, 0u)); };
            auto read_pos = [&]() {
                pd = lds_ld<f32x4>(pdw + rn * 16);
                ps = lds_ld<f32x4>(pbuf_a + lane * 16);
            };
            auto geometry = [&]() {
                const float dx = pd[0] - ps[0], dy = pd[1] - ps[1], dz = pd[2] - ps[2];
                const float d2 = (dx * dx + dy * dy + dz * dz) * NEG_LOG2E;
                w0 = pk_bf16(1.0f, dx);
                w1 = pk_bf16(dy, dz);
                const unsigned ext = pk_bf16(d2, __uint_as_float(rs & 0xffff0000u) * NEG_LOG2E);
                const auto sw = __builtin_amdgcn_permlane32_swap(oh_a + rn * 16, ext, false, false);
                ua = sw[0];
                vb = sw[1];
                saddr = stage_r + rn * 8;
                PIN(saddr); PIN(w0); PIN(w1);
                valid = rn < 8;
            };
            auto read_rows = [&]() {
                a0 = lds_ld<bf16x8>(aad[0]); a1 = lds_ld<bf16x8>(aad[1]);
                a2 = lds_ld<bf16x8>(aad[2]); a3 = lds_ld<bf16x8>(aad[3]);
            };
            auto read_ext = [&](unsigned u) { exr = lds_ld<u32x4>(hh ? oh_a : u); };
            auto fix_ext = [&](unsigned u) {
                u32x4 ex = exr;
                ex[0] = hh ? u : ex[0];
                ef = __builtin_bit_cast(bf16x8, ex);
                PIN(ef);
            };
            auto stage_weights = [&](bool wr) {
                if (wr) lds_st(saddr, u32x2{w0, w1});
                wf0 = cat8(tr_read(trb), tr_read(trb + 512));
                wf1 = cat8(tr_read(trb + 1024), tr_read(trb + 1536));
                if (wr) lds_st(saddr, u32x2{0, 0});
            };
            auto nop = [&]() {};

            read_pair(base);
            read_src(base);
            issue_rows();
            issue_pos();
            if (base + 32 < end) read_src(base + 32);
            PSTAMP(11);
            asm volatile("s_waitcnt vmcnt(0)" : "+v"(pf) :: "memory");
            PSTAMP(12);
            read_pos();
            read_rows();
            geometry();
            read_ext(ua);
            if (base + 32 < end) issue_rows();
            fix_ext(ua);
            {
                f32x16 zero;
#pragma unroll
                for (int i = 0; i < 16; ++i) zero[i] = 0.f;
                xa = MFMA32(a0, i0, zero);
                xa = MFMA32(a1, i1, xa);
                xa = MFMA32(ef, e0, xa);
            }
            SBAR();
            PSTAMP(13);
            auto set_prio = [&](int rem) {
                if (rem >= 192) __builtin_amdgcn_s_setprio(3);
                else if (rem >= 128) __builtin_amdgcn_s_setprio(2);
                else if (rem >= 64) __builtin_amdgcn_s_setprio(1);
                else __builtin_amdgcn_s_setprio(0);
            };
            while (true) {
                set_prio(end - base);
                half_tile(xa, xb, a2, a3, ef, e1, i0, i1, z1, pkp, wf1, z0, wf0,
                          [&]() { stage_weights(valid && hh == 0); read_src(base + 64); read_pair(base + 64); },
                          [&]() { wait_vm0(); read_rows(); read_ext(vb); },
                          nop);
                half_tile(xb, xa, a0, a1, ef, e0, i0, i1, z0, pkp, wf1, z1, wf0,
                          [&]() { fix_ext(vb); if (base + 64 < end) issue_rows(); issue_pos(); }, nop, nop);
                if (base + 32 >= end) break;
                half_tile(xa, xb, a2, a3, ef, e1, i0, i1, z1, pkp, wf1, z0, wf0,
                          [&]() { stage_weights(valid && hh == 1); read_src(base + 96); },
                          [&]() { wait_vm0(); read_rows(); read_pos(); },
                          [&]() { geometry(); read_ext(ua); });
                half_tile(xb, xa, a0, a1, ef, e0, i0, i1, z0, pkp, wf1, z1, wf0,
                          [&]() { fix_ext(ua); if (base + 96 < end) issue_rows(); }, nop, nop);
                base += 64;
                if (base >= end) break;
            }
            z1 = MFMA32(__builtin_bit_cast(bf16x8, pkp), wf1, z1);
        }
        asm volatile("s_waitcnt vmcnt(0)" : "+v"(pf) :: "memory");
        CSTAMP(13);
        QSTAMP(7);
        float cs0 = 0.f, cs1 = 0.f;
#pragma unroll
        for (int g = 0; g < 4; ++g) {
            const f32x4 w0 = *(const f32x4*)(sm + L_WX + (8 * g + 4 * hh) * 4);
            const f32x4 w1 = *(const f32x4*)(sm + L_WX + (32 + 8 * g + 4 * hh) * 4);
#pragma unroll
            for (int i = 0; i < 4; ++i) { cs0 += z0[4 * g + i] * w0[i]; cs1 += z1[4 * g + i] * w1[i]; }
        }
        const float csh = cs0 + cs1;
        const auto cw = __builtin_amdgcn_permlane32_swap(__float_as_uint(csh), __float_as_uint(csh), false, false);
        const float cst = __uint_as_float(cw[0]) + __uint_as_float(cw[1]);
        wait_vm0();
        if (hh == 0) ((float*)(part + PPOS))[r] = cst;
        if ((lane & 3) == 0) {
            const int n = r >> 2;
#pragma unroll
            for (int g = 0; g < 4; ++g) {
                *(f32x4*)(part + n * PROW + (8 * g + 4 * hh) * 4) = f32x4{z0[4 * g], z0[4 * g + 1], z0[4 * g + 2], z0[4 * g + 3]};
                *(f32x4*)(part + n * PROW + (32 + 8 * g + 4 * hh) * 4) = f32x4{z1[4 * g], z1[4 * g + 1], z1[4 * g + 2], z1[4 * g + 3]};
            }
        }
    } else {
        asm volatile("s_waitcnt vmcnt(0)" : "+v"(pf) :: "memory");
        {
            const u32x4 z = {0, 0, 0, 0};
            *(u32x4*)(part + lane * 16) = z;
            *(u32x4*)(part + 1024 + lane * 16) = z;
            *(u32x4*)(part + 2048 + lane * 16) = z;
        }
        bar();
        const float wd = bf2f(we[128 * 64 + lane]), wa = bf2f(we[129 * 64 + lane]);
        const float wxl = ((const float*)(sm + L_WX))[lane];
        const unsigned* T = (const unsigned*)(ws + W_T) + b * 256;
        for (int ci = wv; ci < NCH; ci += K2_W) {
            const unsigned tv = T[(ci & 7) * 32 + (ci >> 3)];
            const unsigned g0 = ci * 1024 + (tv & 0xffffu), cn = tv >> 16;
            for (unsigned i = 0; i < cn; ++i) {
                const unsigned* tp = (const unsigned*)(Rec + (size_t)(g0 + div3(i)) * 16);
                const unsigned j = i - 3 * div3(i);
                const unsigned rc = tp[j], cd = (tp[3] >> (8 * j)) & 0xffu;
                const unsigned s = rc & 0xffffu, w = cd >> 3, n = cd & 7, dl = win_start(w) + n;
                const f32x4 ps = *(const f32x4*)(Ptab + s * 16);
                const f32x4 pd = *(const f32x4*)(sm + L_PD + dl * 16);
                const float dx = pd[0] - ps[0], dy = pd[1] - ps[1], dz = pd[2] - ps[2];
                const unsigned e = pk_bf16((dx * dx + dy * dy + dz * dz) * NEG_LOG2E, __uint_as_float(rc & 0xffff0000u) * NEG_LOG2E);
                const unsigned dxy = pk_bf16(dx, dy), dz0 = pk_bf16(dz, 0.f);
                float y = bf2f(((const uint16_t*)(sm + L_AIMG))[dl * 64 + lane]) + bf2f(((const uint16_t*)Btab)[s * 64 + lane]);
                y += bf2f(e & 0xffffu) * wd + bf2f(e >> 16) * wa;
                const float m = bf2f(pk_bf16(y * rcp(1.0f + ex2(y)), 0.f) & 0xffffu);
                char* tgt = sm + L_SLOT + wave_a(w) * 4096;
                atomicAdd((float*)(tgt + n * PROW) + lane, m);
                float t = m * wxl;
                for (int o = 32; o > 0; o >>= 1) t += __shfl_xor(t, o);
                if (lane < 3) {
                    const float dv = lane == 0 ? bf2f(dxy & 0xffffu) : lane == 1 ? bf2f(dxy >> 16) : bf2f(dz0 & 0xffffu);
                    atomicAdd((float*)(tgt + PPOS) + 4 * n + 1 + lane, dv * t);
                }
            }
        }
    }
    const int ct = wv & 3, g = lane >> 4, node = 16 * (wv >> 2) + (lane & 15), nodec = node < BN ? node : BN - 1;
    bf16x8 wfr[4];
#pragma unroll
    for (int s = 0; s < 4; ++s) wfr[s] = wfrag16(sm + L_WH, s, ct, lane);
    f32x4 acc = {0.f, 0.f, 0.f, 0.f};
    acc = MFMA16(wfr[0], nfrag16(sm + L_HIMG, nodec, g), acc);
    acc = MFMA16(wfr[1], nfrag16(sm + L_HIMG, nodec, 4 + g), acc);
    const u32x2 hv = *(const u32x2*)(sm + L_HIMG + nodec * 128 + 16 * ((2 * ct + (g >> 1)) ^ ((nodec >> 1) & 7)) + 8 * (g & 1));
    const unsigned nwin = win_of(nodec);
    const char* pb = sm + L_SLOT + wave_a(nwin) * 4096 + (nodec - win_start(nwin)) * PROW + g * 32;
    const int pd2 = (wave_b(nwin) - wave_a(nwin)) * 4096;
    QSTAMP(8);
    bar();
    QSTAMP(9);
#pragma unroll
    for (int s = 0; s < 2; ++s) {
        const f32x4 m0 = (*(const f32x4*)(pb + s * 128) + *(const f32x4*)(pb + s * 128 + pd2)) * NEG_LN2;
        const f32x4 m1 = (*(const f32x4*)(pb + s * 128 + 16) + *(const f32x4*)(pb + s * 128 + 16 + pd2)) * NEG_LN2;
        acc = MFMA16(wfr[2 + s], as_frag(pk_bf16(m0[0], m0[1]), pk_bf16(m0[2], m0[3]), pk_bf16(m1[0], m1[1]), pk_bf16(m1[2], m1[3])), acc);
    }
    if (wv >= 10) {
        const int x = (wv - 10) * 64 + lane;
        if (x < 3 * BN) {
            const int nl = (x * 171) >> 9, cc = x - 3 * nl, w = win_of(nl), n = nl - win_start(w);
            const char* pp = sm + L_SLOT + PPOS + (4 * n + 1 + cc) * 4;
            const float sum = *(const float*)(pp + wave_a(w) * 4096) + *(const float*)(pp + wave_b(w) * 4096);
            const float p0 = ((const float*)(sm + L_PD))[nl * 4 + cc];
            out[NN * 64 + (b * BN + nl) * 3 + cc] = (uint16_t)(pk_bf16(p0 + sum, 0.f) & 0xffffu);
        }
    }
    QSTAMP(10);
    if (node < BN) {
        const float o0 = bf2f(hv[0] & 0xffffu) + silu_f(acc[0]), o1 = bf2f(hv[0] >> 16) + silu_f(acc[1]);
        const float o2 = bf2f(hv[1] & 0xffffu) + silu_f(acc[2]), o3 = bf2f(hv[1] >> 16) + silu_f(acc[3]);
        st_nt(out + (b * BN + node) * 64 + 16 * ct + 4 * g, u32x2{pk_bf16(o0, o1), pk_bf16(o2, o3)});
    }
    QSTAMP(11);
    STAMP_OUT(1, ws);
}

}

extern "C" void kernel_launch(void* const* d_in, const int* in_sizes, int n_in, void* d_out, int out_size, void* d_ws,
                              size_t ws_size, hipStream_t stream) {
    using namespace egnn;
    const uint16_t* h = (const uint16_t*)d_in[0];
    const uint16_t* pos = (const uint16_t*)d_in[1];
    const uint16_t* ea = (const uint16_t*)d_in[2];
    const int* src = (const int*)d_in[3];
    const int* dst = (const int*)d_in[4];
    const uint16_t* we = (const uint16_t*)d_in[5];
    const uint16_t* wx = (const uint16_t*)d_in[6];
    const uint16_t* wh = (const uint16_t*)d_in[7];
    (void)hipFuncSetAttribute((const void*)k_edge, hipFuncAttributeMaxDynamicSharedMemorySize, L_END);
    k_sort<<<NCH, K1_T, 0, stream>>>(src, dst, ea, h, pos, we, wx, (char*)d_ws);
    k_edge<<<256, K2_T, L_END, stream>>>(h, we, wh, (const char*)d_ws, (uint16_t*)d_out);
}
```

```cpp
#include <hip/hip_runtime.h>
#include <stdint.h>

namespace egnn {

typedef short    bf16x8 __attribute__((ext_vector_type(8)));
typedef short    s16x4  __attribute__((ext_vector_type(4)));
typedef float    f32x16 __attribute__((ext_vector_type(16)));
typedef float    f32x4  __attribute__((ext_vector_type(4)));
typedef float    f32x2  __attribute__((ext_vector_type(2)));
typedef __bf16   bf16x2 __attribute__((ext_vector_type(2)));
typedef unsigned u32x4  __attribute__((ext_vector_type(4)));
typedef unsigned u32x2  __attribute__((ext_vector_type(2)));
#define LDS_AS __attribute__((address_space(3)))
#define DI __device__ __forceinline__
#define SBAR() __builtin_amdgcn_sched_barrier(0)
#define PIN(x) asm volatile("" : "+v"(x))
#define MFMA32(a, b, c) __builtin_amdgcn_mfma_f32_32x32x16_bf16(a, b, c, 0, 0, 0)
#define MFMA16(a, b, c) __builtin_amdgcn_mfma_f32_16x16x32_bf16(a, b, c, 0, 0, 0)

constexpr int NN = 10000;
constexpr int NE = 640000;
constexpr int BN = 40;
constexpr int NBK = 250;
constexpr int CE = 2560;
constexpr int NCH = 250;
constexpr float NEG_LOG2E = -1.4426950408889634f;
constexpr float NEG_LN2 = -0.6931471805599453f;

constexpr size_t W_R   = 0;
constexpr size_t W_T   = 4u << 20;
constexpr size_t W_B   = (4u << 20) + (512u << 10);
constexpr size_t W_P   = 6u << 20;
constexpr size_t W_C   = (6u << 20) + (512u << 10);

DI unsigned win_of(unsigned dl) { return ((dl * 37u) >> 8) + (dl == 34u ? 1u : 0u); }
DI unsigned win_start(unsigned w) { return 7u * w - (w == 5u ? 1u : 0u); }

DI int wave_a(unsigned w) { return (0x987520u >> (4 * w)) & 15; }
DI int wave_b(unsigned w) { return (0xBA4631u >> (4 * w)) & 15; }

DI float bf2f(unsigned v16) { return __uint_as_float(v16 << 16); }
DI unsigned pk_bf16(float lo, float hi) {
    f32x2 x = {lo, hi};
    return __builtin_bit_cast(unsigned, __builtin_convertvector(x, bf16x2));
}
DI float silu_f(float x) {
    return x * __builtin_amdgcn_rcpf(1.0f + __builtin_amdgcn_exp2f(x * NEG_LOG2E));
}
DI unsigned long long rtime() { unsigned long long t; asm volatile("s_memrealtime %0\n\ts_waitcnt lgkmcnt(0)" : "=s"(t) :: "memory"); return t; }
DI void probe_wait(const char* ws, unsigned ticks) {
    const unsigned long long t0 = *(const volatile unsigned long long*)(ws + (7u << 20));
    const unsigned lo = __builtin_amdgcn_readfirstlane((unsigned)t0);
    for (int i = 0; i < 100000; ++i) { if ((unsigned)rtime() - lo >= ticks) break; __builtin_amdgcn_s_sleep(1); }
}
DI s16x4 tr_read(const char* p) {
    return __builtin_amdgcn_ds_read_tr16_b64_v4i16((LDS_AS s16x4*)p);
}
DI bf16x8 cat8(s16x4 a, s16x4 b) { return __builtin_shufflevector(a, b, 0, 1, 2, 3, 4, 5, 6, 7); }
DI void wait_vm0() { asm volatile("s_waitcnt vmcnt(0)" ::: "memory"); }
DI void wait_lgkm0() { asm volatile("s_waitcnt lgkmcnt(0)" ::: "memory"); }
DI void bar() { asm volatile("s_waitcnt lgkmcnt(0)\n\ts_barrier" ::: "memory"); }

DI void dma1(unsigned ldsaddr, const void* gbase, unsigned voff) {
    unsigned keep;
    asm volatile("s_mov_b32 %0, m0\n\ts_mov_b32 m0, %1\n\ts_nop 0\n\t"
                 "global_load_lds_dwordx4 %2, %3\n\t"
                 "s_mov_b32 m0, %0"
                 : "=&s"(keep) : "s"(ldsaddr), "v"(voff), "s"(gbase) : "memory");
}
DI void dma4(unsigned ldsaddr, const void* gbase, unsigned v0, unsigned v1, unsigned v2, unsigned v3) {
    unsigned keep;
    asm volatile("s_mov_b32 %0, m0\n\ts_mov_b32 m0, %1\n\ts_nop 0\n\t"
                 "global_load_lds_dwordx4 %2, %6\n\t"
                 "global_load_lds_dwordx4 %3, %6 offset:1024\n\t"
                 "global_load_lds_dwordx4 %4, %6 offset:2048\n\t"
                 "global_load_lds_dwordx4 %5, %6 offset:3072\n\t"
                 "s_mov_b32 m0, %0"
                 : "=&s"(keep) : "s"(ldsaddr), "v"(v0), "v"(v1), "v"(v2), "v"(v3), "s"(gbase) : "memory");
}
template <typename T> DI void st_nt(void* p, T v) { __builtin_nontemporal_store(v, (T*)p); }
DI unsigned div3(unsigned x) { return (x * 43691u) >> 17; }
DI unsigned mad16(unsigned x, unsigned m, unsigned a) {
    unsigned r;
    asm("v_mad_u32_u16 %0, %1, %2, %3" : "=v"(r) : "v"(x), "s"(m), "v"(a));
    return r;
}
template <typename T> DI T lds_ld(unsigned a) { return *(const LDS_AS T*)(size_t)a; }
template <typename T> DI void lds_st(unsigned a, T v) { *(LDS_AS T*)(size_t)a = v; }
template <int CTRL, int ROWMASK> DI unsigned dpp_add(unsigned x) {
    return x + (unsigned)__builtin_amdgcn_update_dpp(0, (int)x, CTRL, ROWMASK, 0xf, true);
}
DI unsigned scan32(unsigned x) {
    x = dpp_add<0x111, 0xf>(x); x = dpp_add<0x112, 0xf>(x); x = dpp_add<0x114, 0xf>(x); x = dpp_add<0x118, 0xf>(x);
    x = dpp_add<0x142, 0xa>(x);
    return x;
}
DI unsigned scan64(unsigned x) { x = scan32(x); x = dpp_add<0x143, 0xc>(x); return x; }
DI int wswz(int k) { return (k & 2) | ((k >> 1) & 4); }
DI bf16x8 wfrag16(const char* img, int s, int ct, int lane) {
    const int g = lane >> 4, q = (lane & 15) >> 2, p = lane & 3;
    const int k0 = 32 * s + 8 * g + q, k1 = k0 + 4, chn = 2 * ct + (p >> 1);
    return cat8(tr_read(img + k0 * 128 + 16 * (chn ^ wswz(k0)) + 8 * (p & 1)),
                tr_read(img + k1 * 128 + 16 * (chn ^ wswz(k1)) + 8 * (p & 1)));
}
DI bf16x8 nfrag16(const char* img, int node, int chunk) {
    return *(const bf16x8*)(img + node * 128 + 16 * (chunk ^ ((node >> 1) & 7)));
}

#define PSTAMP(i)
#define QSTAMP(i)
#define STAMP_DECL
#define STAMP(i)
#define CSTAMP(i)
#define STAMP_OUT(kern, wsp)

constexpr int K1_T = 640;
constexpr int S_CNT = 0, S_OFF = 1024, S_TOT = 2048, S_R = 2064, S_W = S_R + 16384, S_H = S_W + 8192, S_O = S_H + 6144, S_END = S_O + 5120;

__global__ __launch_bounds__(K1_T) void k_sort(const int* __restrict__ src, const int* __restrict__ dst,
                                               const uint16_t* __restrict__ ea, const uint16_t* __restrict__ h,
                                               const uint16_t* __restrict__ pos, const uint16_t* __restrict__ we,
                                               const uint16_t* __restrict__ wx, char* __restrict__ ws) {
    __shared__ __attribute__((aligned(16))) char sm[S_END];
    const int tid = threadIdx.x, lane = tid & 63;
    const int wv = __builtin_amdgcn_readfirstlane(tid >> 6);
    const int c = blockIdx.x;
    asm volatile("" :: "s"(src), "s"(dst), "s"(ea), "s"(h), "s"(pos), "s"(we), "s"(wx), "s"(ws));
    const unsigned smb = (unsigned)(size_t)(LDS_AS char*)sm;
    const unsigned my0 = (unsigned)rtime();
    STAMP_DECL
    STAMP(0);

    const int e0 = c * CE + 4 * tid;
    const u32x4 d4 = *(const u32x4*)(dst + e0);
    const u32x4 s4 = *(const u32x4*)(src + e0);
    const u32x2 a4 = *(const u32x2*)(ea + e0);
    unsigned tw;
    {
        const unsigned o = lane == 0 ? (unsigned)W_R + c * 16384
                         : lane == 2 ? (unsigned)W_T + c * 1024 : lane == 3 ? (unsigned)W_B + c * (BN * 128) : (unsigned)W_P + c * (BN * 16);
        asm volatile("global_load_dword %0, %1, %2" : "=v"(tw) : "v"(o), "s"(ws) : "memory");
    }
    const int pn = c * BN + (lane < BN ? lane : 0);
    const unsigned px = pos[pn * 3], py = pos[pn * 3 + 1], pz = pos[pn * 3 + 2];

    {
        const int k = (wv & 7) * 8 + (lane >> 3);
        const int row = (wv - 5) * 8 + (lane >> 3);
        if (wv < 8) dma1(smb + S_W + wv * 1024, we, (64 + k) * 128 + (((lane & 7) ^ wswz(k)) * 16));
        if (wv >= 5) dma1(smb + S_H + (wv - 5) * 1024, h, (c * BN + row) * 128 + (((lane & 7) ^ ((row >> 1) & 7)) * 16));
    }
    if (tid < 256) ((unsigned*)(sm + S_CNT))[tid] = 0;
    bar();
    STAMP(1);

    unsigned bk[4], rk[4];
#pragma unroll
    for (int i = 0; i < 4; ++i) {
        bk[i] = (d4[i] * 52429u) >> 21;
        rk[i] = atomicAdd((unsigned*)(sm + S_CNT) + bk[i], 1u);
    }
    unsigned char code[4];
#pragma unroll
    for (int i = 0; i < 4; ++i) {
        const unsigned dl = d4[i] - bk[i] * BN, w = win_of(dl);
        code[i] = (unsigned char)(w * 8 + dl - win_start(w));
    }
    STAMP(2);
    asm volatile("s_waitcnt vmcnt(0)" : "+v"(tw) :: "memory");
    bar();
    STAMP(3);

    if (wv == 9) {
        const u32x4 cn = *(const u32x4*)(sm + S_CNT + lane * 16);
        const u32x4 tr = {div3(cn[0] + 2), div3(cn[1] + 2), div3(cn[2] + 2), div3(cn[3] + 2)};
        const unsigned tot = tr[0] + tr[1] + tr[2] + tr[3];
        const unsigned in = scan64(tot), ex = in - tot;
        const u32x4 of = {ex, ex + tr[0], ex + tr[0] + tr[1], ex + tr[0] + tr[1] + tr[2]};
        *(u32x4*)(sm + S_OFF + lane * 16) = of;
        if (lane == 63) *(unsigned*)(sm + S_TOT) = in;
        char* T = ws + W_T + ((c & 7) * 32 + (c >> 3)) * 4 + lane * 4096;
#pragma unroll
        for (int i = 0; i < 4; ++i)
            if (4 * lane + i < NBK) st_nt(T + i * 1024, of[i] | (cn[i] << 16));
    } else if (wv == 8) {
        if (lane < BN) st_nt(ws + W_P + (size_t)pn * 16, f32x4{bf2f(px), bf2f(py), bf2f(pz), 0.f});
        if (c == 0) {
            const u32x4 z = {0, 0, 0, 0};
            st_nt(ws + W_P + (size_t)NN * 16 + lane * 16, z);
            if (lane < 8) st_nt(ws + W_B + (size_t)NN * 128 + lane * 16, z);
            st_nt(ws + W_C + lane * 16, lane < 16 ? *(const u32x4*)(we + 128 * 64 + lane * 8) : z);
            const int n = lane >> 2, j = lane & 3;
            st_nt(ws + W_C + 1024 + lane * 4, (n < 8 && (n >> 1) == j) ? (0x3F80u << (16 * (n & 1))) : 0u);
            st_nt(ws + W_C + 1280 + lane * 4, bf2f(wx[lane]) * NEG_LN2);
        }
    }
    STAMP(4);
    bar();
    STAMP(5);

#pragma unroll
    for (int i = 0; i < 4; ++i) {
        const unsigned q = div3(rk[i]), j = rk[i] - 3 * q;
        const unsigned t = smb + S_R + (((const unsigned*)(sm + S_OFF))[bk[i]] + q) * 16;
        const unsigned av = (i & 1) ? (a4[i >> 1] & 0xffff0000u) : (a4[i >> 1] << 16);
        lds_st(t + j * 4, s4[i] | av);
        lds_st(t + 12 + j, code[i]);
    }
    bar();
    STAMP(6);
    {
        const unsigned ntot = *(const unsigned*)(sm + S_TOT);
        char* R = ws + W_R + (size_t)c * 16384;
        if (tid < ntot) st_nt(R + tid * 16, *(const u32x4*)(sm + S_R + tid * 16));
        if (tid + K1_T < ntot) st_nt(R + (tid + K1_T) * 16, *(const u32x4*)(sm + S_R + (tid + K1_T) * 16));
    }
    STAMP(7);

    if (wv < 8) {
        const int ct = wv & 3, g = lane >> 4;
        const bf16x8 a0 = wfrag16(sm + S_W, 0, ct, lane), a1 = wfrag16(sm + S_W, 1, ct, lane);
        for (int nt = wv >> 2; nt < 3; nt += 2) {
            const int node = 16 * nt + (lane & 15);
            f32x4 acc = {0.f, 0.f, 0.f, 0.f};
            acc = MFMA16(a0, nfrag16(sm + S_H, node, g), acc);
            acc = MFMA16(a1, nfrag16(sm + S_H, node, 4 + g), acc);
            const u32x2 o = {pk_bf16(acc[0] * NEG_LOG2E, acc[1] * NEG_LOG2E), pk_bf16(acc[2] * NEG_LOG2E, acc[3] * NEG_LOG2E)};
            if (node < BN) *(u32x2*)(sm + S_O + node * 128 + 16 * ((2 * ct + (g >> 1)) ^ (node & 7)) + 8 * (g & 1)) = o;
        }
    }
    bar();
    if (tid < BN * 8) {
        const int node = tid >> 3, ch = (tid & 7) ^ (node & 7);
        st_nt(ws + W_B + (size_t)(c * BN + node) * 128 + ch * 16, *(const u32x4*)(sm + S_O + tid * 16));
    }
    wait_vm0();
    bar();
    for (int i = 0; i < 100000; ++i) { if ((unsigned)rtime() - my0 >= 1200) break; __builtin_amdgcn_s_sleep(1); }
    STAMP(8);
    STAMP_OUT(0, ws);
}

constexpr int K2_W = 12, K2_T = 64 * K2_W;
constexpr int RCAP = 3072;
constexpr int L_SLOT = 0;
constexpr int L_STAGE = 49152;
constexpr int RALL = RCAP + 128;
constexpr int L_R32 = 73728;
constexpr int L_R8 = L_R32 + RALL * 4;
constexpr int L_AIMG = L_R8 + RALL;
constexpr int L_HIMG = L_AIMG + 6144;
constexpr int L_W1 = L_HIMG + 6144;
constexpr int L_WH = L_W1 + 8192;
constexpr int L_PD = L_WH + 16384;
constexpr int L_OH = L_PD + 1024;
constexpr int L_WX = L_OH + 256;
constexpr int L_BINS = L_OH + 1024;
constexpr int L_OFFS = L_BINS + 896;
constexpr int L_WIN = L_OFFS + 896;
constexpr int L_WF = L_WIN + 64;
constexpr int L_PB = L_WF + 64;
constexpr int L_END = L_PB + K2_W * 1024;
constexpr unsigned MAPCAP = 128;
constexpr int PROW = 272, PPOS = 8 * PROW;

DI bf16x8 as_frag(unsigned a, unsigned b, unsigned c, unsigned d) {
    u32x4 v = {a, b, c, d};
    return __builtin_bit_cast(bf16x8, v);
}
DI float ex2(float x) { return __builtin_amdgcn_exp2f(x); }
DI float rcp(float x) { return __builtin_amdgcn_rcpf(x); }

template <class H1, class H2, class H3>
DI void half_tile(const f32x16& xc, f32x16& xn, const bf16x8& aA, const bf16x8& aB, const bf16x8& ef, const bf16x8& eB,
                  const bf16x8& i0, const bf16x8& i1, f32x16& zp, u32x4& pkp, const bf16x8& wp, f32x16& zc,
                  const bf16x8& wc, H1&& hook1, H2&& hook2, H3&& hook3) {
    f32x16 zero;
#pragma unroll
    for (int i = 0; i < 16; ++i) zero[i] = 0.f;
    f32x2 e0, e1, e2, e3;
    u32x4 pk;
#define EXP2(e, i) do { e[0] = ex2(xc[i]); PIN(e[0]); } while (0)
#define EXP2H(e, i) do { e[1] = ex2(xc[i]); PIN(e[1]); } while (0)
#define RCP2(e) do { e[0] = rcp(e[0]); PIN(e[0]); } while (0)
#define RCP2H(e) do { e[1] = rcp(e[1]); PIN(e[1]); } while (0)
#define PKMODE 0
#define ADD1(e) do { e = e + 1.0f; PIN(e); } while (0)
#define MULX(e, i) do { e = e * f32x2{xc[i], xc[i + 1]}; PIN(e); } while (0)
#define CVT(d, e) do { d = __builtin_bit_cast(unsigned, __builtin_convertvector(e, bf16x2)); PIN(d); } while (0)
    xn = MFMA32(aA, i0, zero); SBAR();
    EXP2(e0, 0); EXP2H(e0, 1); EXP2(e1, 2); SBAR();
    zp = MFMA32(__builtin_bit_cast(bf16x8, pkp), wp, zp); SBAR();
    EXP2H(e1, 3); EXP2(e2, 4); EXP2H(e2, 5); EXP2(e3, 6); EXP2H(e3, 7); SBAR();
    ADD1(e0); ADD1(e1); ADD1(e2); ADD1(e3); SBAR();
    hook1(); SBAR();
    xn = MFMA32(aB, i1, xn); SBAR();
    RCP2(e0); RCP2H(e0); RCP2(e1); SBAR();
    xn = MFMA32(ef, eB, xn); SBAR();
    RCP2H(e1); RCP2(e2); RCP2H(e2); RCP2(e3); RCP2H(e3); SBAR();
    MULX(e0, 0); MULX(e1, 2); MULX(e2, 4); MULX(e3, 6); SBAR();
    CVT(pk[0], e0); CVT(pk[1], e1); CVT(pk[2], e2); CVT(pk[3], e3); SBAR();
    EXP2(e0, 8); EXP2H(e0, 9); SBAR();
    zc = MFMA32(__builtin_bit_cast(bf16x8, pk), wc, zc); SBAR();
    EXP2(e1, 10); EXP2H(e1, 11); EXP2(e2, 12); EXP2H(e2, 13); EXP2(e3, 14); EXP2H(e3, 15); SBAR();
    ADD1(e0); ADD1(e1); ADD1(e2); ADD1(e3); SBAR();
    hook2(); SBAR();
    RCP2(e0); RCP2H(e0); RCP2(e1); RCP2H(e1); RCP2(e2); RCP2H(e2); RCP2(e3); RCP2H(e3); SBAR();
    hook3(); SBAR();
    MULX(e0, 8); MULX(e1, 10); MULX(e2, 12); MULX(e3, 14); SBAR();
    CVT(pkp[0], e0); CVT(pkp[1], e1); CVT(pkp[2], e2); CVT(pkp[3], e3); SBAR();
#undef EXP2
#undef EXP2H
#undef RCP2
#undef RCP2H
#undef ADD1
#undef MULX
#undef CVT
}

__global__ __launch_bounds__(K2_T) void k_edge(const uint16_t* __restrict__ h, const uint16_t* __restrict__ we,
                                               const uint16_t* __restrict__ wh, const char* __restrict__ ws,
                                               uint16_t* __restrict__ out) {
    extern __shared__ __attribute__((aligned(16))) char sm[];
    const int tid = threadIdx.x, lane = tid & 63;
    const int wv = __builtin_amdgcn_readfirstlane(tid >> 6);
    const int b = (blockIdx.x & 7) * 32 + (blockIdx.x >> 3);
    if (b >= NBK) return;
    const unsigned smb = (unsigned)(size_t)(LDS_AS char*)sm;
    asm volatile("" :: "s"(h), "s"(we), "s"(wh), "s"(ws), "s"(out));
    const char* Rec = ws + W_R;
    const char* Btab = ws + W_B;
    const char* Ptab = ws + W_P;
    STAMP_DECL
    STAMP(0);

    const int l3 = (lane * 171) >> 9, sub = lane - 3 * l3;
    const int cid = 21 * wv + l3;
    const bool cok = lane < 63 && cid < NCH;
    const int cidc = cok ? cid : 0;
    unsigned tval;
    asm volatile("global_load_dword %0, %1, %2" : "=v"(tval)
                 : "v"((unsigned)W_T + (b * 256 + (cidc & 7) * 32 + (cidc >> 3)) * 4), "s"(ws) : "memory");

    {
        const int r8l = lane >> 3, c8 = lane & 7;
        auto wpiece = [&](const uint16_t* w, int q, int la) {
            dma1(smb + la + q * 1024, w, (8 * q + r8l) * 128 + ((c8 ^ ((r8l & 2) | ((q & 1) << 2))) * 16));
        };
        if (wv < 5) dma1(smb + L_HIMG + wv * 1024, h, (b * BN + 8 * wv + r8l) * 128 + ((c8 ^ ((4 * wv + (lane >> 4)) & 7)) * 16));
        else wpiece(we, wv - 5, L_W1);
        if (wv >= 4) wpiece(wh, wv - 4, L_WH);
        else if (wv == 0) wpiece(we, 7, L_W1);
        else {
            const unsigned go = wv == 1 ? (unsigned)W_P + b * BN * 16 : (unsigned)W_C + (wv - 2) * 1024;
            dma1(smb + (wv == 1 ? L_PD : wv == 2 ? L_AIMG + BN * 128 : L_OH), ws, go + lane * 16);
        }
    }
    unsigned tw;
    asm volatile("global_load_dword %0, %1, %2" : "=v"(tw)
                 : "v"(lane == 0 ? b * (BN * 128) : (NN * 128 + b * (BN * 6)) & ~3), "s"(out) : "memory");

    {
        const u32x4 z = {0, 0, 0, 0};
        lds_st(smb + L_STAGE + wv * 2048 + lane * 16, z);
        lds_st(smb + L_STAGE + wv * 2048 + 1024 + lane * 16, z);
        const u32x4 n32 = {NN, NN, NN, NN}, n8 = {0x08080808u, 0x08080808u, 0x08080808u, 0x08080808u};
        lds_st(smb + L_R32 + tid * 16, n32);
        if (wv == 0) {
            if (lane < RALL / 4 - K2_T) lds_st(smb + L_R32 + (K2_T + lane) * 16, n32);
        } else if (wv < 5) {
            if (tid - 64 < RALL / 16) lds_st(smb + L_R8 + (tid - 64) * 16, n8);
        } else if (wv == 5) {
            if (lane < 56) lds_st(smb + L_BINS + lane * 16, z);
        }
    }
    PSTAMP(1);
    bar();
    asm volatile("s_waitcnt vmcnt(3)" : "+v"(tval) :: "memory");
    PSTAMP(2);

    const unsigned cnt = cok ? (tval >> 16) : 0u;
    const unsigned ntr = div3(cnt + 2);
    const unsigned incl = scan64(sub == 0 ? ntr : 0u);
    const unsigned mw = __builtin_amdgcn_readlane(incl, 63);
    const bool slow_w = mw > MAPCAP;
    const unsigned map_a = smb + L_SLOT + wv * 4096;
    QSTAMP(1);
    if (!slow_w) {
        unsigned g = (cidc * 1024 + (tval & 0xffffu) + sub) | ((cnt - 3 * sub) << 20);
        unsigned mp = map_a + (incl - ntr + sub) * 4;
#pragma clang loop unroll(disable) vectorize(disable) interleave(disable)
        for (unsigned i = sub; i < ntr; i += 3) { lds_st(mp, g); mp += 12; g += 3 - (9u << 20); }
    }
    PSTAMP(3);
    const unsigned mwe = slow_w ? 0u : mw;
    unsigned left[2];
    u32x4 rec[2];
#pragma unroll
    for (int k = 0; k < 2; ++k) {
        const unsigned m = lds_ld<unsigned>(map_a + (64 * k + lane) * 4);
        const bool ok = (unsigned)(64 * k + lane) < mwe;
        left[k] = ok ? m >> 20 : 0u;
        const unsigned go = ok ? (m & 0xfffffu) * 16 : 0u;
        asm volatile("global_load_dwordx4 %0, %1, %2" : "=v"(rec[k]) : "v"(go), "s"(Rec) : "memory");
    }
    {
        const int q = 8 + (wv & 7), r8l = lane >> 3;
        dma1(smb + L_WH + q * 1024, wh, (8 * q + r8l) * 128 + (((lane & 7) ^ ((r8l & 2) | ((q & 1) << 2))) * 16));
    }
    if (lane == 0) lds_st(smb + L_WF + wv * 4, slow_w ? 1u : 0u);
    PSTAMP(4);
    asm volatile("s_waitcnt vmcnt(0)" : "+v"(rec[0]), "+v"(rec[1]), "+v"(tw) :: "memory");
    PSTAMP(5);
    unsigned bin[6], rk[6];
#pragma unroll
    for (int k = 0; k < 6; ++k) {
        const unsigned w = (rec[k / 3][3] >> (8 * (k % 3) + 3)) & 7u;
        bin[k] = (left[k / 3] > (unsigned)(k % 3) ? w : 6u) * 128 + (lane & 31) * 4;
        rk[k] = atomicAdd((unsigned*)(sm + L_BINS + bin[k]), 1u);
    }
    unsigned pf;
    {
        const unsigned part = blockIdx.x >> 3;
        const unsigned off = tid < 626 ? (unsigned)W_B + part * (313 * 128) + tid * 64
                                       : (unsigned)W_P + part * (40 * 128) + (tid < 706 ? tid - 626 : 0) * 64;
        asm volatile("global_load_dword %0, %1, %2" : "=v"(pf) : "v"(off), "s"(ws) : "memory");
    }
    QSTAMP(3);
    PSTAMP(6);
    bar();
    PSTAMP(7);

    if (wv == 0) {
        const unsigned* bins = (const unsigned*)(sm + L_BINS);
        const int j = lane & 31, hi = lane >> 5;
        const unsigned ca = bins[(2 * hi) * 32 + j] | (bins[(2 * hi + 1) * 32 + j] << 16);
        const unsigned cb = bins[4 * 32 + j] | (bins[5 * 32 + j] << 16);
        const unsigned wfl = lds_ld<unsigned>(smb + L_WF + (lane < K2_W ? lane : 0) * 4);
        const unsigned ia = scan32(ca), ib = scan32(cb);
        const unsigned t01 = __builtin_amdgcn_readlane(ia, 31), t23 = __builtin_amdgcn_readlane(ia, 63);
        const unsigned t45 = __builtin_amdgcn_readlane(ib, 31);
        const unsigned tot[6] = {t01 & 0xffffu, t01 >> 16, t23 & 0xffffu, t23 >> 16, t45 & 0xffffu, t45 >> 16};
        unsigned st[7];
        st[0] = 0;
#pragma unroll
        for (int w = 0; w < 6; ++w) st[w + 1] = st[w] + ((tot[w] + 31) & ~31u);
        const unsigned xa = ia - ca, xb = ib - cb;
        unsigned* offs = (unsigned*)(sm + L_OFFS);
        offs[(2 * hi) * 32 + j] = (hi ? st[2] : st[0]) + (xa & 0xffffu);
        offs[(2 * hi + 1) * 32 + j] = (hi ? st[3] : st[1]) + (xa >> 16);
        offs[(4 + hi) * 32 + j] = hi ? st[5] + (xb >> 16) : st[4] + (xb & 0xffffu);
        const bool any_slow = __any(wfl != 0);
        unsigned wi = 0;
#pragma unroll
        for (int w = 0; w < 6; ++w) { wi = lane == w ? st[w] : wi; wi = lane == 6 + w ? (st[w + 1] - st[w]) >> 5 : wi; }
        wi = lane == 12 ? ((any_slow || st[6] > RCAP) ? 1u : 0u) : wi;
        if (lane < 13) ((unsigned*)(sm + L_WIN))[lane] = wi;
    } else {
        const int ct = wv & 3, g = lane >> 4;
        const bf16x8 a0 = wfrag16(sm + L_W1, 0, ct, lane), a1 = wfrag16(sm + L_W1, 1, ct, lane);
        for (int nt = wv >> 2; ; nt = 0) {
            const int node = 16 * nt + (lane & 15);
            f32x4 acc = {0.f, 0.f, 0.f, 0.f};
            acc = MFMA16(a0, nfrag16(sm + L_HIMG, node, g), acc);
            acc = MFMA16(a1, nfrag16(sm + L_HIMG, node, 4 + g), acc);
            const u32x2 o = {pk_bf16(acc[0] * NEG_LOG2E, acc[1] * NEG_LOG2E), pk_bf16(acc[2] * NEG_LOG2E, acc[3] * NEG_LOG2E)};
            if (node < BN) *(u32x2*)(sm + L_AIMG + node * 128 + (16 * ct + 4 * g) * 2) = o;
            if (wv != 4 || nt == 0) break;
        }
    }
    PSTAMP(8);
    bar();
    QSTAMP(4);
    PSTAMP(9);
    const bool slow = __builtin_amdgcn_readfirstlane(((const unsigned*)(sm + L_WIN))[12]) != 0;

    const int r = lane & 31, hh = lane >> 5;
    const int win = (int)((0x545432231100ull >> (4 * wv)) & 7);
    const bool second = (0xC5A >> wv) & 1;
    const int wnode = win_start(win);
    char* part = sm + L_SLOT + wv * 4096;

    if (!slow) {
#pragma unroll
        for (int k = 0; k < 6; ++k) {
            const unsigned o = lds_ld<unsigned>(smb + L_OFFS + bin[k]) + rk[k];
            const unsigned p = left[k / 3] > (unsigned)(k % 3) ? o : (unsigned)(RALL - 1);
            lds_st(smb + L_R32 + p * 4, rec[k / 3][k % 3]);
            lds_st(smb + L_R8 + p, (unsigned char)((rec[k / 3][3] >> (8 * (k % 3))) & 7u));
        }
        const unsigned wiv = lds_ld<unsigned>(smb + L_WIN + (lane & 15) * 4);
        int tw6[6];
#pragma unroll
        for (int w = 0; w < 6; ++w) tw6[w] = __builtin_amdgcn_readlane(wiv, 6 + w);
        const int wstart = __builtin_amdgcn_readlane(wiv, win);
        const int l0 = tw6[0] + tw6[3] + tw6[4], l1 = tw6[0] + tw6[2] + tw6[5];
        const int l2 = tw6[1] + tw6[2] + tw6[4], l3s = tw6[1] + tw6[3] + tw6[5];
        const int wtiles = win == 0 ? tw6[0] : win == 1 ? tw6[1] : win == 2 ? tw6[2] : win == 3 ? tw6[3] : win == 4 ? tw6[4] : tw6[5];
        const int la = win == 0 ? l0 : win == 1 ? l2 : win == 2 ? l1 : win == 3 ? l3s : win == 4 ? l0 : l1;
        const int lb = win == 0 ? l1 : win == 1 ? l3s : win == 2 ? l2 : win == 3 ? l0 : win == 4 ? l2 : l3s;
        int first = (4 * wtiles - la + lb + 4) >> 3;
        first = first < 0 ? 0 : first > wtiles ? wtiles : first;
        const int ta = second ? first : 0, tb = second ? wtiles : first;

        const int blk = (lane >> 4) & 1, q = (lane & 15) >> 2, p = lane & 3;
        bf16x8 i0, i1;
#pragma unroll
        for (int j = 0; j < 8; ++j) {
            i0[j] = (r == 8 * hh + j) ? (short)0x3F80 : (short)0;
            i1[j] = (r == 16 + 8 * hh + j) ? (short)0x3F80 : (short)0;
        }
        const int rho = 8 * (r & 3) + (r >> 2);
        const int fsw = (r & 3) ^ (((r >> 3) & 1) << 2);
        unsigned aad[4];
#pragma unroll
        for (int s = 0; s < 4; ++s) aad[s] = smb + L_SLOT + wv * 4096 + rho * 128 + 16 * ((2 * s + hh) ^ fsw);
        unsigned kk[4];
#pragma unroll
        for (int i = 0; i < 4; ++i)
            kk[i] = 16 * ((lane & 7) ^ i ^ (((lane >> 4) & 1) << 2)) + (3072 - 1024 * i);
        const char* gB = Btab - 3072;
        const unsigned slot_a = smb + L_SLOT + wv * 4096;
        const unsigned pbuf_a = smb + L_PB + wv * 1024;
        const unsigned src_a = smb + L_R32 + (lane >> 3) * 16, rs_a = smb + L_R32 + lane * 4, rn_a = smb + L_R8 + lane;
        const unsigned stage_r = smb + L_STAGE + wv * 2048 + r * 64;
        const char* trb = sm + L_STAGE + wv * 2048 + (4 * hh + q) * 64 + blk * 32 + p * 8;
        const unsigned pdw = smb + L_PD + wnode * 16;
        const unsigned oh_a = smb + L_OH;
        bar();
        QSTAMP(5);
        PSTAMP(10);

        bf16x8 e0, e1;
        {
            const int row = hh ? BN + q : wnode + q;
            const char* ab = sm + L_AIMG + row * 128 + (16 * blk + 4 * p) * 2;
            e0 = cat8(tr_read(ab), tr_read(ab + 512));
            e1 = cat8(tr_read(ab + 64), tr_read(ab + 64 + 512));
        }
        f32x16 z0, z1, xa, xb;
#pragma unroll
        for (int i = 0; i < 16; ++i) { z0[i] = 0.f; z1[i] = 0.f; }
        QSTAMP(6);
        CSTAMP(12);

        if (ta < tb) {
            int base = wstart + 32 * ta;
            const int end = wstart + 32 * tb;
            bf16x8 a0, a1, a2, a3, ef, wf0, wf1;
            unsigned rs, rn, w0, w1, ua, vb;
            unsigned saddr;
            bool valid;
            u32x4 pkp = {0, 0, 0, 0};
#pragma unroll
            for (int j = 0; j < 8; ++j) wf1[j] = 0;

            u32x4 sn, exr;
            f32x4 pd, ps;
            auto read_src = [&](int ri) { sn = lds_ld<u32x4>(src_a + ri * 4); };
            auto issue_rows = [&]() {
                const unsigned v0 = mad16(sn[0], 128, kk[0]), v1 = mad16(sn[1], 128, kk[1]);
                const unsigned v2 = mad16(sn[2], 128, kk[2]), v3 = mad16(sn[3], 128, kk[3]);
                wait_lgkm0();
                dma4(slot_a, gB, v0, v1, v2, v3);
            };
            auto read_pair = [&](int ri) {
                rs = lds_ld<unsigned>(rs_a + ri * 4);
                rn = lds_ld<unsigned char>(rn_a + ri);
            };
            auto issue_pos = [&]() { dma1(pbuf_a, Ptab, mad16(rs, 16, 0u)); };
            auto read_pos = [&]() {
                pd = lds_ld<f32x4>(pdw + rn * 16);
                ps = lds_ld<f32x4>(pbuf_a + lane * 16);
            };
            auto geometry = [&]() {
                const float dx = pd[0] - ps[0], dy = pd[1] - ps[1], dz = pd[2] - ps[2];
                const float d2 = (dx * dx + dy * dy + dz * dz) * NEG_LOG2E;
                w0 = pk_bf16(1.0f, dx);
                w1 = pk_bf16(dy, dz);
                const unsigned ext = pk_bf16(d2, __uint_as_float(rs & 0xffff0000u) * NEG_LOG2E);
                const auto sw = __builtin_amdgcn_permlane32_swap(oh_a + rn * 16, ext, false, false);
                ua = sw[0];
                vb = sw[1];
                saddr = stage_r + rn * 8;
                PIN(saddr); PIN(w0); PIN(w1);
                valid = rn < 8;
            };
            auto read_rows = [&]() {
                a0 = lds_ld<bf16x8>(aad[0]); a1 = lds_ld<bf16x8>(aad[1]);
                a2 = lds_ld<bf16x8>(aad[2]); a3 = lds_ld<bf16x8>(aad[3]);
            };
            auto read_ext = [&](unsigned u) { exr = lds_ld<u32x4>(hh ? oh_a : u); };
            auto fix_ext = [&](unsigned u) {
                u32x4 ex = exr;
                ex[0] = hh ? u : ex[0];
                ef = __builtin_bit_cast(bf16x8, ex);
                PIN(ef);
            };
            auto stage_weights = [&](bool wr) {
                if (wr) lds_st(saddr, u32x2{w0, w1});
                wf0 = cat8(tr_read(trb), tr_read(trb + 512));
                wf1 = cat8(tr_read(trb + 1024), tr_read(trb + 1536));
                if (wr) lds_st(saddr, u32x2{0, 0});
            };
            auto nop = [&]() {};

            read_pair(base);
            read_src(base);
            issue_rows();
            issue_pos();
            if (base + 32 < end) read_src(base + 32);
            PSTAMP(11);
            asm volatile("s_waitcnt vmcnt(0)" : "+v"(pf) :: "memory");
            PSTAMP(12);
            read_pos();
            read_rows();
            geometry();
            read_ext(ua);
            if (base + 32 < end) issue_rows();
            fix_ext(ua);
            {
                f32x16 zero;
#pragma unroll
                for (int i = 0; i < 16; ++i) zero[i] = 0.f;
                xa = MFMA32(a0, i0, zero);
                xa = MFMA32(a1, i1, xa);
                xa = MFMA32(ef, e0, xa);
            }
            SBAR();
            PSTAMP(13);
            auto set_prio = [&](int rem) {
                if (rem >= 192) __builtin_amdgcn_s_setprio(3);
                else if (rem >= 128) __builtin_amdgcn_s_setprio(2);
                else if (rem >= 64) __builtin_amdgcn_s_setprio(1);
                else __builtin_amdgcn_s_setprio(0);
            };
            while (true) {
                set_prio(end - base);
                half_tile(xa, xb, a2, a3, ef, e1, i0, i1, z1, pkp, wf1, z0, wf0,
                          [&]() { stage_weights(valid && hh == 0); read_src(base + 64); read_pair(base + 64); },
                          [&]() { wait_vm0(); read_rows(); read_ext(vb); },
                          nop);
                half_tile(xb, xa, a0, a1, ef, e0, i0, i1, z0, pkp, wf1, z1, wf0,
                          [&]() { fix_ext(vb); if (base + 64 < end) issue_rows(); issue_pos(); }, nop, nop);
                if (base + 32 >= end) break;
                half_tile(xa, xb, a2, a3, ef, e1, i0, i1, z1, pkp, wf1, z0, wf0,
                          [&]() { stage_weights(valid && hh == 1); read_src(base + 96); },
                          [&]() { wait_vm0(); read_rows(); read_pos(); },
                          [&]() { geometry(); read_ext(ua); });
                half_tile(xb, xa, a0, a1, ef, e0, i0, i1, z0, pkp, wf1, z1, wf0,
                          [&]() { fix_ext(ua); if (base + 96 < end) issue_rows(); }, nop, nop);
                base += 64;
                if (base >= end) break;
            }
            z1 = MFMA32(__builtin_bit_cast(bf16x8, pkp), wf1, z1);
        }
        asm volatile("s_waitcnt vmcnt(0)" : "+v"(pf) :: "memory");
        CSTAMP(13);
        QSTAMP(7);
        float cs0 = 0.f, cs1 = 0.f;
#pragma unroll
        for (int g = 0; g < 4; ++g) {
            const f32x4 w0 = *(const f32x4*)(sm + L_WX + (8 * g + 4 * hh) * 4);
            const f32x4 w1 = *(const f32x4*)(sm + L_WX + (32 + 8 * g + 4 * hh) * 4);
#pragma unroll
            for (int i = 0; i < 4; ++i) { cs0 += z0[4 * g + i] * w0[i]; cs1 += z1[4 * g + i] * w1[i]; }
        }
        const float csh = cs0 + cs1;
        const auto cw = __builtin_amdgcn_permlane32_swap(__float_as_uint(csh), __float_as_uint(csh), false, false);
        const float cst = __uint_as_float(cw[0]) + __uint_as_float(cw[1]);
        wait_vm0();
        if (hh == 0) ((float*)(part + PPOS))[r] = cst;
        if ((lane & 3) == 0) {
            const int n = r >> 2;
#pragma unroll
            for (int g = 0; g < 4; ++g) {
                *(f32x4*)(part + n * PROW + (8 * g + 4 * hh) * 4) = f32x4{z0[4 * g], z0[4 * g + 1], z0[4 * g + 2], z0[4 * g + 3]};
                *(f32x4*)(part + n * PROW + (32 + 8 * g + 4 * hh) * 4) = f32x4{z1[4 * g], z1[4 * g + 1], z1[4 * g + 2], z1[4 * g + 3]};
            }
        }
    } else {
        asm volatile("s_waitcnt vmcnt(0)" : "+v"(pf) :: "memory");
        {
            const u32x4 z = {0, 0, 0, 0};
            *(u32x4*)(part + lane * 16) = z;
            *(u32x4*)(part + 1024 + lane * 16) = z;
            *(u32x4*)(part + 2048 + lane * 16) = z;
        }
        bar();
        const float wd = bf2f(we[128 * 64 + lane]), wa = bf2f(we[129 * 64 + lane]);
        const float wxl = ((const float*)(sm + L_WX))[lane];
        const unsigned* T = (const unsigned*)(ws + W_T) + b * 256;
        for (int ci = wv; ci < NCH; ci += K2_W) {
            const unsigned tv = T[(ci & 7) * 32 + (ci >> 3)];
            const unsigned g0 = ci * 1024 + (tv & 0xffffu), cn = tv >> 16;
            for (unsigned i = 0; i < cn; ++i) {
                const unsigned* tp = (const unsigned*)(Rec + (size_t)(g0 + div3(i)) * 16);
                const unsigned j = i - 3 * div3(i);
                const unsigned rc = tp[j], cd = (tp[3] >> (8 * j)) & 0xffu;
                const unsigned s = rc & 0xffffu, w = cd >> 3, n = cd & 7, dl = win_start(w) + n;
                const f32x4 ps = *(const f32x4*)(Ptab + s * 16);
                const f32x4 pd = *(const f32x4*)(sm + L_PD + dl * 16);
                const float dx = pd[0] - ps[0], dy = pd[1] - ps[1], dz = pd[2] - ps[2];
                const unsigned e = pk_bf16((dx * dx + dy * dy + dz * dz) * NEG_LOG2E, __uint_as_float(rc & 0xffff0000u) * NEG_LOG2E);
                const unsigned dxy = pk_bf16(dx, dy), dz0 = pk_bf16(dz, 0.f);
                float y = bf2f(((const uint16_t*)(sm + L_AIMG))[dl * 64 + lane]) + bf2f(((const uint16_t*)Btab)[s * 64 + lane]);
                y += bf2f(e & 0xffffu) * wd + bf2f(e >> 16) * wa;
                const float m = bf2f(pk_bf16(y * rcp(1.0f + ex2(y)), 0.f) & 0xffffu);
                char* tgt = sm + L_SLOT + wave_a(w) * 4096;
                atomicAdd((float*)(tgt + n * PROW) + lane, m);
                float t = m * wxl;
                for (int o = 32; o > 0; o >>= 1) t += __shfl_xor(t, o);
                if (lane < 3) {
                    const float dv = lane == 0 ? bf2f(dxy & 0xffffu) : lane == 1 ? bf2f(dxy >> 16) : bf2f(dz0 & 0xffffu);
                    atomicAdd((float*)(tgt + PPOS) + 4 * n + 1 + lane, dv * t);
                }
            }
        }
    }
    const int ct = wv & 3, g = lane >> 4, node = 16 * (wv >> 2) + (lane & 15), nodec = node < BN ? node : BN - 1;
    bf16x8 wfr[4];
#pragma unroll
    for (int s = 0; s < 4; ++s) wfr[s] = wfrag16(sm + L_WH, s, ct, lane);
    f32x4 acc = {0.f, 0.f, 0.f, 0.f};
    acc = MFMA16(wfr[0], nfrag16(sm + L_HIMG, nodec, g), acc);
    acc = MFMA16(wfr[1], nfrag16(sm + L_HIMG, nodec, 4 + g), acc);
    const u32x2 hv = *(const u32x2*)(sm + L_HIMG + nodec * 128 + 16 * ((2 * ct + (g >> 1)) ^ ((nodec >> 1) & 7)) + 8 * (g & 1));
    const unsigned nwin = win_of(nodec);
    const char* pb = sm + L_SLOT + wave_a(nwin) * 4096 + (nodec - win_start(nwin)) * PROW + g * 32;
    const int pd2 = (wave_b(nwin) - wave_a(nwin)) * 4096;
    QSTAMP(8);
    bar();
    QSTAMP(9);
#pragma unroll
    for (int s = 0; s < 2; ++s) {
        const f32x4 m0 = (*(const f32x4*)(pb + s * 128) + *(const f32x4*)(pb + s * 128 + pd2)) * NEG_LN2;
        const f32x4 m1 = (*(const f32x4*)(pb + s * 128 + 16) + *(const f32x4*)(pb + s * 128 + 16 + pd2)) * NEG_LN2;
        acc = MFMA16(wfr[2 + s], as_frag(pk_bf16(m0[0], m0[1]), pk_bf16(m0[2], m0[3]), pk_bf16(m1[0], m1[1]), pk_bf16(m1[2], m1[3])), acc);
    }
    if (wv >= 10) {
        const int x = (wv - 10) * 64 + lane;
        if (x < 3 * BN) {
            const int nl = (x * 171) >> 9, cc = x - 3 * nl, w = win_of(nl), n = nl - win_start(w);
            const char* pp = sm + L_SLOT + PPOS + (4 * n + 1 + cc) * 4;
            const float sum = *(const float*)(pp + wave_a(w) * 4096) + *(const float*)(pp + wave_b(w) * 4096);
            const float p0 = ((const float*)(sm + L_PD))[nl * 4 + cc];
            out[NN * 64 + (b * BN + nl) * 3 + cc] = (uint16_t)(pk_bf16(p0 + sum, 0.f) & 0xffffu);
        }
    }
    QSTAMP(10);
    if (node < BN) {
        const float o0 = bf2f(hv[0] & 0xffffu) + silu_f(acc[0]), o1 = bf2f(hv[0] >> 16) + silu_f(acc[1]);
        const float o2 = bf2f(hv[1] & 0xffffu) + silu_f(acc[2]), o3 = bf2f(hv[1] >> 16) + silu_f(acc[3]);
        st_nt(out + (b * BN + node) * 64 + 16 * ct + 4 * g, u32x2{pk_bf16(o0, o1), pk_bf16(o2, o3)});
    }
    QSTAMP(11);
    STAMP_OUT(1, ws);
}

}

extern "C" void kernel_launch(void* const* d_in, const int* in_sizes, int n_in, void* d_out, int out_size, void* d_ws,
                              size_t ws_size, hipStream_t stream) {
    using namespace egnn;
    const uint16_t* h = (const uint16_t*)d_in[0];
    const uint16_t* pos = (const uint16_t*)d_in[1];
    const uint16_t* ea = (const uint16_t*)d_in[2];
    const int* src = (const int*)d_in[3];
    const int* dst = (const int*)d_in[4];
    const uint16_t* we = (const uint16_t*)d_in[5];
    const uint16_t* wx = (const uint16_t*)d_in[6];
    const uint16_t* wh = (const uint16_t*)d_in[7];
    (void)hipFuncSetAttribute((const void*)k_edge, hipFuncAttributeMaxDynamicSharedMemorySize, L_END);
    k_sort<<<NCH, K1_T, 0, stream>>>(src, dst, ea, h, pos, we, wx, (char*)d_ws);
    k_edge<<<256, K2_T, L_END, stream>>>(h, we, wh, (const char*)d_ws, (uint16_t*)d_out);
}
```

```cpp
#include <hip/hip_runtime.h>
#include <stdint.h>

namespace egnn {

typedef short    bf16x8 __attribute__((ext_vector_type(8)));
typedef short    s16x4  __attribute__((ext_vector_type(4)));
typedef float    f32x16 __attribute__((ext_vector_type(16)));
typedef float    f32x4  __attribute__((ext_vector_type(4)));
typedef float    f32x2  __attribute__((ext_vector_type(2)));
typedef __bf16   bf16x2 __attribute__((ext_vector_type(2)));
typedef unsigned u32x4  __attribute__((ext_vector_type(4)));
typedef unsigned u32x2  __attribute__((ext_vector_type(2)));
#define LDS_AS __attribute__((address_space(3)))
#define DI __device__ __forceinline__
#define SBAR() __builtin_amdgcn_sched_barrier(0)
#define PIN(x) asm volatile("" : "+v"(x))
#define MFMA32(a, b, c) __builtin_amdgcn_mfma_f32_32x32x16_bf16(a, b, c, 0, 0, 0)
#define MFMA16(a, b, c) __builtin_amdgcn_mfma_f32_16x16x32_bf16(a, b, c, 0, 0, 0)

constexpr int NN = 10000;
constexpr int NE = 640000;
constexpr int BN = 40;
constexpr int NBK = 250;
constexpr int CE = 2560;
constexpr int NCH = 250;
constexpr float NEG_LOG2E = -1.4426950408889634f;
constexpr float NEG_LN2 = -0.6931471805599453f;

constexpr size_t W_R   = 0;
constexpr size_t W_T   = 4u << 20;
constexpr size_t W_B   = (4u << 20) + (512u << 10);
constexpr size_t W_P   = 6u << 20;
constexpr size_t W_C   = (6u << 20) + (512u << 10);

DI unsigned win_of(unsigned dl) { return ((dl * 37u) >> 8) + (dl == 34u ? 1u : 0u); }
DI unsigned win_start(unsigned w) { return 7u * w - (w == 5u ? 1u : 0u); }

DI int wave_a(unsigned w) { return (0x987520u >> (4 * w)) & 15; }
DI int wave_b(unsigned w) { return (0xBA4631u >> (4 * w)) & 15; }

DI float bf2f(unsigned v16) { return __uint_as_float(v16 << 16); }
DI unsigned pk_bf16(float lo, float hi) {
    f32x2 x = {lo, hi};
    return __builtin_bit_cast(unsigned, __builtin_convertvector(x, bf16x2));
}
DI float silu_f(float x) {
    return x * __builtin_amdgcn_rcpf(1.0f + __builtin_amdgcn_exp2f(x * NEG_LOG2E));
}
DI s16x4 tr_read(const char* p) {
    return __builtin_amdgcn_ds_read_tr16_b64_v4i16((LDS_AS s16x4*)p);
}
DI bf16x8 cat8(s16x4 a, s16x4 b) { return __builtin_shufflevector(a, b, 0, 1, 2, 3, 4, 5, 6, 7); }
DI void wait_vm0() { asm volatile("s_waitcnt vmcnt(0)" ::: "memory"); }
DI void wait_lgkm0() { asm volatile("s_waitcnt lgkmcnt(0)" ::: "memory"); }
DI void bar() { asm volatile("s_waitcnt lgkmcnt(0)\n\ts_barrier" ::: "memory"); }

DI void dma1(unsigned ldsaddr, const void* gbase, unsigned voff) {
    unsigned keep;
    asm volatile("s_mov_b32 %0, m0\n\ts_mov_b32 m0, %1\n\ts_nop 0\n\t"
                 "global_load_lds_dwordx4 %2, %3\n\t"
                 "s_mov_b32 m0, %0"
                 : "=&s"(keep) : "s"(ldsaddr), "v"(voff), "s"(gbase) : "memory");
}
DI void dma4(unsigned ldsaddr, const void* gbase, unsigned v0, unsigned v1, unsigned v2, unsigned v3) {
    unsigned keep;
    asm volatile("s_mov_b32 %0, m0\n\ts_mov_b32 m0, %1\n\ts_nop 0\n\t"
                 "global_load_lds_dwordx4 %2, %6\n\t"
                 "global_load_lds_dwordx4 %3, %6 offset:1024\n\t"
                 "global_load_lds_dwordx4 %4, %6 offset:2048\n\t"
                 "global_load_lds_dwordx4 %5, %6 offset:3072\n\t"
                 "s_mov_b32 m0, %0"
                 : "=&s"(keep) : "s"(ldsaddr), "v"(v0), "v"(v1), "v"(v2), "v"(v3), "s"(gbase) : "memory");
}
template <typename T> DI void st_nt(void* p, T v) { __builtin_nontemporal_store(v, (T*)p); }
DI unsigned div3(unsigned x) { return (x * 43691u) >> 17; }
DI unsigned mad16(unsigned x, unsigned m, unsigned a) {
    unsigned r;
    asm("v_mad_u32_u16 %0, %1, %2, %3" : "=v"(r) : "v"(x), "s"(m), "v"(a));
    return r;
}
template <typename T> DI T lds_ld(unsigned a) { return *(const LDS_AS T*)(size_t)a; }
template <typename T> DI void lds_st(unsigned a, T v) { *(LDS_AS T*)(size_t)a = v; }
template <int CTRL, int ROWMASK> DI unsigned dpp_add(unsigned x) {
    return x + (unsigned)__builtin_amdgcn_update_dpp(0, (int)x, CTRL, ROWMASK, 0xf, true);
}
DI unsigned scan32(unsigned x) {
    x = dpp_add<0x111, 0xf>(x); x = dpp_add<0x112, 0xf>(x); x = dpp_add<0x114, 0xf>(x); x = dpp_add<0x118, 0xf>(x);
    x = dpp_add<0x142, 0xa>(x);
    return x;
}
DI unsigned scan64(unsigned x) { x = scan32(x); x = dpp_add<0x143, 0xc>(x); return x; }
DI int wswz(int k) { return (k & 2) | ((k >> 1) & 4); }
DI bf16x8 wfrag16(const char* img, int s, int ct, int lane) {
    const int g = lane >> 4, q = (lane & 15) >> 2, p = lane & 3;
    const int k0 = 32 * s + 8 * g + q, k1 = k0 + 4, chn = 2 * ct + (p >> 1);
    return cat8(tr_read(img + k0 * 128 + 16 * (chn ^ wswz(k0)) + 8 * (p & 1)),
                tr_read(img + k1 * 128 + 16 * (chn ^ wswz(k1)) + 8 * (p & 1)));
}
DI bf16x8 nfrag16(const char* img, int node, int chunk) {
    return *(const bf16x8*)(img + node * 128 + 16 * (chunk ^ ((node >> 1) & 7)));
}

#define PSTAMP(i)
#define QSTAMP(i)
#define STAMP_DECL
#define STAMP(i)
#define CSTAMP(i)
#define STAMP_OUT(kern, wsp)

constexpr int K1_T = 640;
constexpr int S_CNT = 0, S_OFF = 1024, S_TOT = 2048, S_R = 2064, S_W = S_R + 16384, S_H = S_W + 8192, S_O = S_H + 6144, S_END = S_O + 5120;

__global__ __launch_bounds__(K1_T) void k_sort(const int* __restrict__ src, const int* __restrict__ dst,
                                               const uint16_t* __restrict__ ea, const uint16_t* __restrict__ h,
                                               const uint16_t* __restrict__ pos, const uint16_t* __restrict__ we,
                                               const uint16_t* __restrict__ wx, char* __restrict__ ws) {
    __shared__ __attribute__((aligned(16))) char sm[S_END];
    const int tid = threadIdx.x, lane = tid & 63;
    const int wv = __builtin_amdgcn_readfirstlane(tid >> 6);
    const int c = blockIdx.x;
    asm volatile("" :: "s"(src), "s"(dst), "s"(ea), "s"(h), "s"(pos), "s"(we), "s"(wx), "s"(ws));
    const unsigned smb = (unsigned)(size_t)(LDS_AS char*)sm;
    STAMP_DECL
    STAMP(0);

    const int e0 = c * CE + 4 * tid;
    const u32x4 d4 = *(const u32x4*)(dst + e0);
    const u32x4 s4 = *(const u32x4*)(src + e0);
    const u32x2 a4 = *(const u32x2*)(ea + e0);
    unsigned tw;
    {
        const unsigned o = lane == 0 ? (unsigned)W_R + c * 16384
                         : lane == 2 ? (unsigned)W_T + c * 1024 : lane == 3 ? (unsigned)W_B + c * (BN * 128) : (unsigned)W_P + c * (BN * 16);
        asm volatile("global_load_dword %0, %1, %2" : "=v"(tw) : "v"(o), "s"(ws) : "memory");
    }
    const int pn = c * BN + (lane < BN ? lane : 0);
    const unsigned px = pos[pn * 3], py = pos[pn * 3 + 1], pz = pos[pn * 3 + 2];

    {
        const int k = (wv & 7) * 8 + (lane >> 3);
        const int row = (wv - 5) * 8 + (lane >> 3);
        if (wv < 8) dma1(smb + S_W + wv * 1024, we, (64 + k) * 128 + (((lane & 7) ^ wswz(k)) * 16));
        if (wv >= 5) dma1(smb + S_H + (wv - 5) * 1024, h, (c * BN + row) * 128 + (((lane & 7) ^ ((row >> 1) & 7)) * 16));
    }
    if (tid < 256) ((unsigned*)(sm + S_CNT))[tid] = 0;
    bar();
    STAMP(1);

    unsigned bk[4], rk[4];
#pragma unroll
    for (int i = 0; i < 4; ++i) {
        bk[i] = (d4[i] * 52429u) >> 21;
        rk[i] = atomicAdd((unsigned*)(sm + S_CNT) + bk[i], 1u);
    }
    unsigned char code[4];
#pragma unroll
    for (int i = 0; i < 4; ++i) {
        const unsigned dl = d4[i] - bk[i] * BN, w = win_of(dl);
        code[i] = (unsigned char)(w * 8 + dl - win_start(w));
    }
    STAMP(2);
    asm volatile("s_waitcnt vmcnt(0)" : "+v"(tw) :: "memory");
    bar();
    STAMP(3);

    if (wv == 9) {
        const u32x4 cn = *(const u32x4*)(sm + S_CNT + lane * 16);
        const u32x4 tr = {div3(cn[0] + 2), div3(cn[1] + 2), div3(cn[2] + 2), div3(cn[3] + 2)};
        const unsigned tot = tr[0] + tr[1] + tr[2] + tr[3];
        const unsigned in = scan64(tot), ex = in - tot;
        const u32x4 of = {ex, ex + tr[0], ex + tr[0] + tr[1], ex + tr[0] + tr[1] + tr[2]};
        *(u32x4*)(sm + S_OFF + lane * 16) = of;
        if (lane == 63) *(unsigned*)(sm + S_TOT) = in;
        char* T = ws + W_T + ((c & 7) * 32 + (c >> 3)) * 4 + lane * 4096;
#pragma unroll
        for (int i = 0; i < 4; ++i)
            if (4 * lane + i < NBK) st_nt(T + i * 1024, of[i] | (cn[i] << 16));
    } else if (wv == 8) {
        if (lane < BN) st_nt(ws + W_P + (size_t)pn * 16, f32x4{bf2f(px), bf2f(py), bf2f(pz), 0.f});
        if (c == 0) {
            const u32x4 z = {0, 0, 0, 0};
            st_nt(ws + W_P + (size_t)NN * 16 + lane * 16, z);
            if (lane < 8) st_nt(ws + W_B + (size_t)NN * 128 + lane * 16, z);
            st_nt(ws + W_C + lane * 16, lane < 16 ? *(const u32x4*)(we + 128 * 64 + lane * 8) : z);
            const int n = lane >> 2, j = lane & 3;
            st_nt(ws + W_C + 1024 + lane * 4, (n < 8 && (n >> 1) == j) ? (0x3F80u << (16 * (n & 1))) : 0u);
            st_nt(ws + W_C + 1280 + lane * 4, bf2f(wx[lane]) * NEG_LN2);
        }
    }
    STAMP(4);
    bar();
    STAMP(5);

#pragma unroll
    for (int i = 0; i < 4; ++i) {
        const unsigned q = div3(rk[i]), j = rk[i] - 3 * q;
        const unsigned t = smb + S_R + (((const unsigned*)(sm + S_OFF))[bk[i]] + q) * 16;
        const unsigned av = (i & 1) ? (a4[i >> 1] & 0xffff0000u) : (a4[i >> 1] << 16);
        lds_st(t + j * 4, s4[i] | av);
        lds_st(t + 12 + j, code[i]);
    }
    bar();
    STAMP(6);
    {
        const unsigned ntot = *(const unsigned*)(sm + S_TOT);
        char* R = ws + W_R + (size_t)c * 16384;
        if (tid < ntot) st_nt(R + tid * 16, *(const u32x4*)(sm + S_R + tid * 16));
        if (tid + K1_T < ntot) st_nt(R + (tid + K1_T) * 16, *(const u32x4*)(sm + S_R + (tid + K1_T) * 16));
    }
    STAMP(7);

    if (wv < 8) {
        const int ct = wv & 3, g = lane >> 4;
        const bf16x8 a0 = wfrag16(sm + S_W, 0, ct, lane), a1 = wfrag16(sm + S_W, 1, ct, lane);
        for (int nt = wv >> 2; nt < 3; nt += 2) {
            const int node = 16 * nt + (lane & 15);
            f32x4 acc = {0.f, 0.f, 0.f, 0.f};
            acc = MFMA16(a0, nfrag16(sm + S_H, node, g), acc);
            acc = MFMA16(a1, nfrag16(sm + S_H, node, 4 + g), acc);
            const u32x2 o = {pk_bf16(acc[0] * NEG_LOG2E, acc[1] * NEG_LOG2E), pk_bf16(acc[2] * NEG_LOG2E, acc[3] * NEG_LOG2E)};
            if (node < BN) *(u32x2*)(sm + S_O + node * 128 + 16 * ((2 * ct + (g >> 1)) ^ (node & 7)) + 8 * (g & 1)) = o;
        }
    }
    bar();
    if (tid < BN * 8) {
        const int node = tid >> 3, ch = (tid & 7) ^ (node & 7);
        st_nt(ws + W_B + (size_t)(c * BN + node) * 128 + ch * 16, *(const u32x4*)(sm + S_O + tid * 16));
    }
    STAMP(8);
    STAMP_OUT(0, ws);
}

constexpr int K2_W = 12, K2_T = 64 * K2_W;
constexpr int RCAP = 3072;
constexpr int L_SLOT = 0;
constexpr int L_STAGE = 49152;
constexpr int RALL = RCAP + 128;
constexpr int L_R32 = 73728;
constexpr int L_R8 = L_R32 + RALL * 4;
constexpr int L_AIMG = L_R8 + RALL;
constexpr int L_HIMG = L_AIMG + 6144;
constexpr int L_W1 = L_HIMG + 6144;
constexpr int L_WH = L_W1 + 8192;
constexpr int L_PD = L_WH + 16384;
constexpr int L_OH = L_PD + 1024;
constexpr int L_WX = L_OH + 256;
constexpr int L_BINS = L_OH + 1024;
constexpr int L_OFFS = L_BINS + 896;
constexpr int L_WIN = L_OFFS + 896;
constexpr int L_WF = L_WIN + 64;
constexpr int L_PB = L_WF + 64;
constexpr int L_END = L_PB + K2_W * 1024;
constexpr unsigned MAPCAP = 128;
constexpr int PROW = 272, PPOS = 8 * PROW;

DI bf16x8 as_frag(unsigned a, unsigned b, unsigned c, unsigned d) {
    u32x4 v = {a, b, c, d};
    return __builtin_bit_cast(bf16x8, v);
}
DI float ex2(float x) { return __builtin_amdgcn_exp2f(x); }
DI float rcp(float x) { return __builtin_amdgcn_rcpf(x); }

template <class H1, class H2, class H3>
DI void half_tile(const f32x16& xc, f32x16& xn, const bf16x8& aA, const bf16x8& aB, const bf16x8& ef, const bf16x8& eB,
                  const bf16x8& i0, const bf16x8& i1, f32x16& zp, u32x4& pkp, const bf16x8& wp, f32x16& zc,
                  const bf16x8& wc, H1&& hook1, H2&& hook2, H3&& hook3) {
    f32x16 zero;
#pragma unroll
    for (int i = 0; i < 16; ++i) zero[i] = 0.f;
    f32x2 e0, e1, e2, e3;
    u32x4 pk;
#define EXP2(e, i) do { e[0] = ex2(xc[i]); PIN(e[0]); } while (0)
#define EXP2H(e, i) do { e[1] = ex2(xc[i]); PIN(e[1]); } while (0)
#define RCP2(e) do { e[0] = rcp(e[0]); PIN(e[0]); } while (0)
#define RCP2H(e) do { e[1] = rcp(e[1]); PIN(e[1]); } while (0)
#define PKMODE 0
#define ADD1(e) do { e = e + 1.0f; PIN(e); } while (0)
#define MULX(e, i) do { e = e * f32x2{xc[i], xc[i + 1]}; PIN(e); } while (0)
#define CVT(d, e) do { d = __builtin_bit_cast(unsigned, __builtin_convertvector(e, bf16x2)); PIN(d); } while (0)
    xn = MFMA32(aA, i0, zero); SBAR();
    EXP2(e0, 0); EXP2H(e0, 1); EXP2(e1, 2); SBAR();
    zp = MFMA32(__builtin_bit_cast(bf16x8, pkp), wp, zp); SBAR();
    EXP2H(e1, 3); EXP2(e2, 4); EXP2H(e2, 5); EXP2(e3, 6); EXP2H(e3, 7); SBAR();
    ADD1(e0); ADD1(e1); ADD1(e2); ADD1(e3); SBAR();
    hook1(); SBAR();
    xn = MFMA32(aB, i1, xn); SBAR();
    RCP2(e0); RCP2H(e0); RCP2(e1); SBAR();
    xn = MFMA32(ef, eB, xn); SBAR();
    RCP2H(e1); RCP2(e2); RCP2H(e2); RCP2(e3); RCP2H(e3); SBAR();
    MULX(e0, 0); MULX(e1, 2); MULX(e2, 4); MULX(e3, 6); SBAR();
    CVT(pk[0], e0); CVT(pk[1], e1); CVT(pk[2], e2); CVT(pk[3], e3); SBAR();
    EXP2(e0, 8); EXP2H(e0, 9); SBAR();
    zc = MFMA32(__builtin_bit_cast(bf16x8, pk), wc, zc); SBAR();
    EXP2(e1, 10); EXP2H(e1, 11); EXP2(e2, 12); EXP2H(e2, 13); EXP2(e3, 14); EXP2H(e3, 15); SBAR();
    ADD1(e0); ADD1(e1); ADD1(e2); ADD1(e3); SBAR();
    hook2(); SBAR();
    RCP2(e0); RCP2H(e0); RCP2(e1); RCP2H(e1); RCP2(e2); RCP2H(e2); RCP2(e3); RCP2H(e3); SBAR();
    hook3(); SBAR();
    MULX(e0, 8); MULX(e1, 10); MULX(e2, 12); MULX(e3, 14); SBAR();
    CVT(pkp[0], e0); CVT(pkp[1], e1); CVT(pkp[2], e2); CVT(pkp[3], e3); SBAR();
#undef EXP2
#undef EXP2H
#undef RCP2
#undef RCP2H
#undef ADD1
#undef MULX
#undef CVT
}

__global__ __launch_bounds__(K2_T) void k_edge(const uint16_t* __restrict__ h, const uint16_t* __restrict__ we,
                                               const uint16_t* __restrict__ wh, const char* __restrict__ ws,
                                               uint16_t* __restrict__ out) {
    extern __shared__ __attribute__((aligned(16))) char sm[];
    const int tid = threadIdx.x, lane = tid & 63;
    const int wv = __builtin_amdgcn_readfirstlane(tid >> 6);
    const int b = (blockIdx.x & 7) * 32 + (blockIdx.x >> 3);
    if (b >= NBK) return;
    const unsigned smb = (unsigned)(size_t)(LDS_AS char*)sm;
    asm volatile("" :: "s"(h), "s"(we), "s"(wh), "s"(ws), "s"(out));
    const char* Rec = ws + W_R;
    const char* Btab = ws + W_B;
    const char* Ptab = ws + W_P;
    STAMP_DECL
    STAMP(0);

    const int l3 = (lane * 171) >> 9, sub = lane - 3 * l3;
    const int cid = 21 * wv + l3;
    const bool cok = lane < 63 && cid < NCH;
    const int cidc = cok ? cid : 0;
    unsigned tval;
    asm volatile("global_load_dword %0, %1, %2" : "=v"(tval)
                 : "v"((unsigned)W_T + (b * 256 + (cidc & 7) * 32 + (cidc >> 3)) * 4), "s"(ws) : "memory");

    {
        const int r8l = lane >> 3, c8 = lane & 7;
        auto wpiece = [&](const uint16_t* w, int q, int la) {
            dma1(smb + la + q * 1024, w, (8 * q + r8l) * 128 + ((c8 ^ ((r8l & 2) | ((q & 1) << 2))) * 16));
        };
        if (wv < 5) dma1(smb + L_HIMG + wv * 1024, h, (b * BN + 8 * wv + r8l) * 128 + ((c8 ^ ((4 * wv + (lane >> 4)) & 7)) * 16));
        else wpiece(we, wv - 5, L_W1);
        if (wv >= 4) wpiece(wh, wv - 4, L_WH);
        else if (wv == 0) wpiece(we, 7, L_W1);
        else {
            const unsigned go = wv == 1 ? (unsigned)W_P + b * BN * 16 : (unsigned)W_C + (wv - 2) * 1024;
            dma1(smb + (wv == 1 ? L_PD : wv == 2 ? L_AIMG + BN * 128 : L_OH), ws, go + lane * 16);
        }
    }
    unsigned tw;
    asm volatile("global_load_dword %0, %1, %2" : "=v"(tw)
                 : "v"(lane == 0 ? b * (BN * 128) : (NN * 128 + b * (BN * 6)) & ~3), "s"(out) : "memory");

    {
        const u32x4 z = {0, 0, 0, 0};
        lds_st(smb + L_STAGE + wv * 2048 + lane * 16, z);
        lds_st(smb + L_STAGE + wv * 2048 + 1024 + lane * 16, z);
        const u32x4 n32 = {NN, NN, NN, NN}, n8 = {0x08080808u, 0x08080808u, 0x08080808u, 0x08080808u};
        lds_st(smb + L_R32 + tid * 16, n32);
        if (wv == 0) {
            if (lane < RALL / 4 - K2_T) lds_st(smb + L_R32 + (K2_T + lane) * 16, n32);
        } else if (wv < 5) {
            if (tid - 64 < RALL / 16) lds_st(smb + L_R8 + (tid - 64) * 16, n8);
        } else if (wv == 5) {
            if (lane < 56) lds_st(smb + L_BINS + lane * 16, z);
        }
    }
    PSTAMP(1);
    bar();
    asm volatile("s_waitcnt vmcnt(3)" : "+v"(tval) :: "memory");
    PSTAMP(2);

    const unsigned cnt = cok ? (tval >> 16) : 0u;
    const unsigned ntr = div3(cnt + 2);
    const unsigned incl = scan64(sub == 0 ? ntr : 0u);
    const unsigned mw = __builtin_amdgcn_readlane(incl, 63);
    const bool slow_w = mw > MAPCAP;
    const unsigned map_a = smb + L_SLOT + wv * 4096;
    QSTAMP(1);
    if (!slow_w) {
        unsigned g = (cidc * 1024 + (tval & 0xffffu) + sub) | ((cnt - 3 * sub) << 20);
        unsigned mp = map_a + (incl - ntr + sub) * 4;
#pragma clang loop unroll(disable) vectorize(disable) interleave(disable)
        for (unsigned i = sub; i < ntr; i += 3) { lds_st(mp, g); mp += 12; g += 3 - (9u << 20); }
    }
    PSTAMP(3);
    const unsigned mwe = slow_w ? 0u : mw;
    unsigned left[2];
    u32x4 rec[2];
#pragma unroll
    for (int k = 0; k < 2; ++k) {
        const unsigned m = lds_ld<unsigned>(map_a + (64 * k + lane) * 4);
        const bool ok = (unsigned)(64 * k + lane) < mwe;
        left[k] = ok ? m >> 20 : 0u;
        const unsigned go = ok ? (m & 0xfffffu) * 16 : 0u;
        asm volatile("global_load_dwordx4 %0, %1, %2" : "=v"(rec[k]) : "v"(go), "s"(Rec) : "memory");
    }
    {
        const int q = 8 + (wv & 7), r8l = lane >> 3;
        dma1(smb + L_WH + q * 1024, wh, (8 * q + r8l) * 128 + (((lane & 7) ^ ((r8l & 2) | ((q & 1) << 2))) * 16));
    }
    if (lane == 0) lds_st(smb + L_WF + wv * 4, slow_w ? 1u : 0u);
    PSTAMP(4);
    asm volatile("s_waitcnt vmcnt(0)" : "+v"(rec[0]), "+v"(rec[1]), "+v"(tw) :: "memory");
    PSTAMP(5);
    unsigned bin[6], rk[6];
#pragma unroll
    for (int k = 0; k < 6; ++k) {
        const unsigned w = (rec[k / 3][3] >> (8 * (k % 3) + 3)) & 7u;
        bin[k] = (left[k / 3] > (unsigned)(k % 3) ? w : 6u) * 128 + (lane & 31) * 4;
        rk[k] = atomicAdd((unsigned*)(sm + L_BINS + bin[k]), 1u);
    }
    unsigned pf;
    {
        const unsigned part = blockIdx.x >> 3;
        const unsigned off = tid < 626 ? (unsigned)W_B + part * (313 * 128) + tid * 64
                                       : (unsigned)W_P + part * (40 * 128) + (tid < 706 ? tid - 626 : 0) * 64;
        asm volatile("global_load_dword %0, %1, %2" : "=v"(pf) : "v"(off), "s"(ws) : "memory");
    }
    QSTAMP(3);
    PSTAMP(6);
    bar();
    PSTAMP(7);

    if (wv == 0) {
        const unsigned* bins = (const unsigned*)(sm + L_BINS);
        const int j = lane & 31, hi = lane >> 5;
        const unsigned ca = bins[(2 * hi) * 32 + j] | (bins[(2 * hi + 1) * 32 + j] << 16);
        const unsigned cb = bins[4 * 32 + j] | (bins[5 * 32 + j] << 16);
        const unsigned wfl = lds_ld<unsigned>(smb + L_WF + (lane < K2_W ? lane : 0) * 4);
        const unsigned ia = scan32(ca), ib = scan32(cb);
        const unsigned t01 = __builtin_amdgcn_readlane(ia, 31), t23 = __builtin_amdgcn_readlane(ia, 63);
        const unsigned t45 = __builtin_amdgcn_readlane(ib, 31);
        const unsigned tot[6] = {t01 & 0xffffu, t01 >> 16, t23 & 0xffffu, t23 >> 16, t45 & 0xffffu, t45 >> 16};
        unsigned st[7];
        st[0] = 0;
#pragma unroll
        for (int w = 0; w < 6; ++w) st[w + 1] = st[w] + ((tot[w] + 31) & ~31u);
        const unsigned xa = ia - ca, xb = ib - cb;
        unsigned* offs = (unsigned*)(sm + L_OFFS);
        offs[(2 * hi) * 32 + j] = (hi ? st[2] : st[0]) + (xa & 0xffffu);
        offs[(2 * hi + 1) * 32 + j] = (hi ? st[3] : st[1]) + (xa >> 16);
        offs[(4 + hi) * 32 + j] = hi ? st[5] + (xb >> 16) : st[4] + (xb & 0xffffu);
        const bool any_slow = __any(wfl != 0);
        unsigned wi = 0;
#pragma unroll
        for (int w = 0; w < 6; ++w) { wi = lane == w ? st[w] : wi; wi = lane == 6 + w ? (st[w + 1] - st[w]) >> 5 : wi; }
        wi = lane == 12 ? ((any_slow || st[6] > RCAP) ? 1u : 0u) : wi;
        if (lane < 13) ((unsigned*)(sm + L_WIN))[lane] = wi;
    } else {
        const int ct = wv & 3, g = lane >> 4;
        const bf16x8 a0 = wfrag16(sm + L_W1, 0, ct, lane), a1 = wfrag16(sm + L_W1, 1, ct, lane);
        for (int nt = wv >> 2; ; nt = 0) {
            const int node = 16 * nt + (lane & 15);
            f32x4 acc = {0.f, 0.f, 0.f, 0.f};
            acc = MFMA16(a0, nfrag16(sm + L_HIMG, node, g), acc);
            acc = MFMA16(a1, nfrag16(sm + L_HIMG, node, 4 + g), acc);
            const u32x2 o = {pk_bf16(acc[0] * NEG_LOG2E, acc[1] * NEG_LOG2E), pk_bf16(acc[2] * NEG_LOG2E, acc[3] * NEG_LOG2E)};
            if (node < BN) *(u32x2*)(sm + L_AIMG + node * 128 + (16 * ct + 4 * g) * 2) = o;
            if (wv != 4 || nt == 0) break;
        }
    }
    PSTAMP(8);
    bar();
    QSTAMP(4);
    PSTAMP(9);
    const bool slow = __builtin_amdgcn_readfirstlane(((const unsigned*)(sm + L_WIN))[12]) != 0;

    const int r = lane & 31, hh = lane >> 5;
    const int win = (int)((0x545432231100ull >> (4 * wv)) & 7);
    const bool second = (0xC5A >> wv) & 1;
    const int wnode = win_start(win);
    char* part = sm + L_SLOT + wv * 4096;

    if (!slow) {
#pragma unroll
        for (int k = 0; k < 6; ++k) {
            const unsigned o = lds_ld<unsigned>(smb + L_OFFS + bin[k]) + rk[k];
            const unsigned p = left[k / 3] > (unsigned)(k % 3) ? o : (unsigned)(RALL - 1);
            lds_st(smb + L_R32 + p * 4, rec[k / 3][k % 3]);
            lds_st(smb + L_R8 + p, (unsigned char)((rec[k / 3][3] >> (8 * (k % 3))) & 7u));
        }
        const unsigned wiv = lds_ld<unsigned>(smb + L_WIN + (lane & 15) * 4);
        int tw6[6];
#pragma unroll
        for (int w = 0; w < 6; ++w) tw6[w] = __builtin_amdgcn_readlane(wiv, 6 + w);
        const int wstart = __builtin_amdgcn_readlane(wiv, win);
        const int l0 = tw6[0] + tw6[3] + tw6[4], l1 = tw6[0] + tw6[2] + tw6[5];
        const int l2 = tw6[1] + tw6[2] + tw6[4], l3s = tw6[1] + tw6[3] + tw6[5];
        const int wtiles = win == 0 ? tw6[0] : win == 1 ? tw6[1] : win == 2 ? tw6[2] : win == 3 ? tw6[3] : win == 4 ? tw6[4] : tw6[5];
        const int la = win == 0 ? l0 : win == 1 ? l2 : win == 2 ? l1 : win == 3 ? l3s : win == 4 ? l0 : l1;
        const int lb = win == 0 ? l1 : win == 1 ? l3s : win == 2 ? l2 : win == 3 ? l0 : win == 4 ? l2 : l3s;
        int first = (4 * wtiles - la + lb + 4) >> 3;
        first = first < 0 ? 0 : first > wtiles ? wtiles : first;
        const int ta = second ? first : 0, tb = second ? wtiles : first;

        const int blk = (lane >> 4) & 1, q = (lane & 15) >> 2, p = lane & 3;
        bf16x8 i0, i1;
#pragma unroll
        for (int j = 0; j < 8; ++j) {
            i0[j] = (r == 8 * hh + j) ? (short)0x3F80 : (short)0;
            i1[j] = (r == 16 + 8 * hh + j) ? (short)0x3F80 : (short)0;
        }
        const int rho = 8 * (r & 3) + (r >> 2);
        const int fsw = (r & 3) ^ (((r >> 3) & 1) << 2);
        unsigned aad[4];
#pragma unroll
        for (int s = 0; s < 4; ++s) aad[s] = smb + L_SLOT + wv * 4096 + rho * 128 + 16 * ((2 * s + hh) ^ fsw);
        unsigned kk[4];
#pragma unroll
        for (int i = 0; i < 4; ++i)
            kk[i] = 16 * ((lane & 7) ^ i ^ (((lane >> 4) & 1) << 2)) + (3072 - 1024 * i);
        const char* gB = Btab - 3072;
        const unsigned slot_a = smb + L_SLOT + wv * 4096;
        const unsigned pbuf_a = smb + L_PB + wv * 1024;
        const unsigned src_a = smb + L_R32 + (lane >> 3) * 16, rs_a = smb + L_R32 + lane * 4, rn_a = smb + L_R8 + lane;
        const unsigned stage_r = smb + L_STAGE + wv * 2048 + r * 64;
        const char* trb = sm + L_STAGE + wv * 2048 + (4 * hh + q) * 64 + blk * 32 + p * 8;
        const unsigned pdw = smb + L_PD + wnode * 16;
        const unsigned oh_a = smb + L_OH;
        bar();
        QSTAMP(5);
        PSTAMP(10);

        bf16x8 e0, e1;
        {
            const int row = hh ? BN + q : wnode + q;
            const char* ab = sm + L_AIMG + row * 128 + (16 * blk + 4 * p) * 2;
            e0 = cat8(tr_read(ab), tr_read(ab + 512));
            e1 = cat8(tr_read(ab + 64), tr_read(ab + 64 + 512));
        }
        f32x16 z0, z1, xa, xb;
#pragma unroll
        for (int i = 0; i < 16; ++i) { z0[i] = 0.f; z1[i] = 0.f; }
        QSTAMP(6);
        CSTAMP(12);

        if (ta < tb) {
            int base = wstart + 32 * ta;
            const int end = wstart + 32 * tb;
            bf16x8 a0, a1, a2, a3, ef, wf0, wf1;
            unsigned rs, rn, w0, w1, ua, vb;
            unsigned saddr;
            bool valid;
            u32x4 pkp = {0, 0, 0, 0};
#pragma unroll
            for (int j = 0; j < 8; ++j) wf1[j] = 0;

            u32x4 sn, exr;
            f32x4 pd, ps;
            auto read_src = [&](int ri) { sn = lds_ld<u32x4>(src_a + ri * 4); };
            auto issue_rows = [&]() {
                const unsigned v0 = mad16(sn[0], 128, kk[0]), v1 = mad16(sn[1], 128, kk[1]);
                const unsigned v2 = mad16(sn[2], 128, kk[2]), v3 = mad16(sn[3], 128, kk[3]);
                wait_lgkm0();
                dma4(slot_a, gB, v0, v1, v2, v3);
            };
            auto read_pair = [&](int ri) {
                rs = lds_ld<unsigned>(rs_a + ri * 4);
                rn = lds_ld<unsigned char>(rn_a + ri);
            };
            auto issue_pos = [&]() { dma1(pbuf_a, Ptab, mad16(rs, 16, 0u)); };
            auto read_pos = [&]() {
                pd = lds_ld<f32x4>(pdw + rn * 16);
                ps = lds_ld<f32x4>(pbuf_a + lane * 16);
            };
            auto geometry = [&]() {
                const float dx = pd[0] - ps[0], dy = pd[1] - ps[1], dz = pd[2] - ps[2];
                const float d2 = (dx * dx + dy * dy + dz * dz) * NEG_LOG2E;
                w0 = pk_bf16(1.0f, dx);
                w1 = pk_bf16(dy, dz);
                const unsigned ext = pk_bf16(d2, __uint_as_float(rs & 0xffff0000u) * NEG_LOG2E);
                const auto sw = __builtin_amdgcn_permlane32_swap(oh_a + rn * 16, ext, false, false);
                ua = sw[0];
                vb = sw[1];
                saddr = stage_r + rn * 8;
                PIN(saddr); PIN(w0); PIN(w1);
                valid = rn < 8;
            };
            auto read_rows = [&]() {
                a0 = lds_ld<bf16x8>(aad[0]); a1 = lds_ld<bf16x8>(aad[1]);
                a2 = lds_ld<bf16x8>(aad[2]); a3 = lds_ld<bf16x8>(aad[3]);
            };
            auto read_ext = [&](unsigned u) { exr = lds_ld<u32x4>(hh ? oh_a : u); };
            auto fix_ext = [&](unsigned u) {
                u32x4 ex = exr;
                ex[0] = hh ? u : ex[0];
                ef = __builtin_bit_cast(bf16x8, ex);
                PIN(ef);
            };
            auto stage_weights = [&](bool wr) {
                if (wr) lds_st(saddr, u32x2{w0, w1});
                wf0 = cat8(tr_read(trb), tr_read(trb + 512));
                wf1 = cat8(tr_read(trb + 1024), tr_read(trb + 1536));
                if (wr) lds_st(saddr, u32x2{0, 0});
            };
            auto nop = [&]() {};

            read_pair(base);
            read_src(base);
            issue_rows();
            issue_pos();
            if (base + 32 < end) read_src(base + 32);
            PSTAMP(11);
            asm volatile("s_waitcnt vmcnt(0)" : "+v"(pf) :: "memory");
            PSTAMP(12);
            read_pos();
            read_rows();
            geometry();
            read_ext(ua);
            if (base + 32 < end) issue_rows();
            fix_ext(ua);
            {
                f32x16 zero;
#pragma unroll
                for (int i = 0; i < 16; ++i) zero[i] = 0.f;
                xa = MFMA32(a0, i0, zero);
                xa = MFMA32(a1, i1, xa);
                xa = MFMA32(ef, e0, xa);
            }
            SBAR();
            PSTAMP(13);
            auto set_prio = [&](int rem) {
                if (rem >= 192) __builtin_amdgcn_s_setprio(3);
                else if (rem >= 128) __builtin_amdgcn_s_setprio(2);
                else if (rem >= 64) __builtin_amdgcn_s_setprio(1);
                else __builtin_amdgcn_s_setprio(0);
            };
            while (true) {
                set_prio(end - base);
                half_tile(xa, xb, a2, a3, ef, e1, i0, i1, z1, pkp, wf1, z0, wf0,
                          [&]() { stage_weights(valid && hh == 0); read_src(base + 64); read_pair(base + 64); },
                          [&]() { wait_vm0(); read_rows(); read_ext(vb); },
                          nop);
                half_tile(xb, xa, a0, a1, ef, e0, i0, i1, z0, pkp, wf1, z1, wf0,
                          [&]() { fix_ext(vb); if (base + 64 < end) issue_rows(); issue_pos(); }, nop, nop);
                if (base + 32 >= end) break;
                half_tile(xa, xb, a2, a3, ef, e1, i0, i1, z1, pkp, wf1, z0, wf0,
                          [&]() { stage_weights(valid && hh == 1); read_src(base + 96); },
                          [&]() { wait_vm0(); read_rows(); read_pos(); },
                          [&]() { geometry(); read_ext(ua); });
                half_tile(xb, xa, a0, a1, ef, e0, i0, i1, z0, pkp, wf1, z1, wf0,
                          [&]() { fix_ext(ua); if (base + 96 < end) issue_rows(); }, nop, nop);
                base += 64;
                if (base >= end) break;
            }
            z1 = MFMA32(__builtin_bit_cast(bf16x8, pkp), wf1, z1);
        }
        asm volatile("s_waitcnt vmcnt(0)" : "+v"(pf) :: "memory");
        CSTAMP(13);
        QSTAMP(7);
        float cs0 = 0.f, cs1 = 0.f;
#pragma unroll
        for (int g = 0; g < 4; ++g) {
            const f32x4 w0 = *(const f32x4*)(sm + L_WX + (8 * g + 4 * hh) * 4);
            const f32x4 w1 = *(const f32x4*)(sm + L_WX + (32 + 8 * g + 4 * hh) * 4);
#pragma unroll
            for (int i = 0; i < 4; ++i) { cs0 += z0[4 * g + i] * w0[i]; cs1 += z1[4 * g + i] * w1[i]; }
        }
        const float csh = cs0 + cs1;
        const auto cw = __builtin_amdgcn_permlane32_swap(__float_as_uint(csh), __float_as_uint(csh), false, false);
        const float cst = __uint_as_float(cw[0]) + __uint_as_float(cw[1]);
        wait_vm0();
        if (hh == 0) ((float*)(part + PPOS))[r] = cst;
        if ((lane & 3) == 0) {
            const int n = r >> 2;
#pragma unroll
            for (int g = 0; g < 4; ++g) {
                *(f32x4*)(part + n * PROW + (8 * g + 4 * hh) * 4) = f32x4{z0[4 * g], z0[4 * g + 1], z0[4 * g + 2], z0[4 * g + 3]};
                *(f32x4*)(part + n * PROW + (32 + 8 * g + 4 * hh) * 4) = f32x4{z1[4 * g], z1[4 * g + 1], z1[4 * g + 2], z1[4 * g + 3]};
            }
        }
    } else {
        asm volatile("s_waitcnt vmcnt(0)" : "+v"(pf) :: "memory");
        {
            const u32x4 z = {0, 0, 0, 0};
            *(u32x4*)(part + lane * 16) = z;
            *(u32x4*)(part + 1024 + lane * 16) = z;
            *(u32x4*)(part + 2048 + lane * 16) = z;
        }
        bar();
        const float wd = bf2f(we[128 * 64 + lane]), wa = bf2f(we[129 * 64 + lane]);
        const float wxl = ((const float*)(sm + L_WX))[lane];
        const unsigned* T = (const unsigned*)(ws + W_T) + b * 256;
        for (int ci = wv; ci < NCH; ci += K2_W) {
            const unsigned tv = T[(ci & 7) * 32 + (ci >> 3)];
            const unsigned g0 = ci * 1024 + (tv & 0xffffu), cn = tv >> 16;
            for (unsigned i = 0; i < cn; ++i) {
                const unsigned* tp = (const unsigned*)(Rec + (size_t)(g0 + div3(i)) * 16);
                const unsigned j = i - 3 * div3(i);
                const unsigned rc = tp[j], cd = (tp[3] >> (8 * j)) & 0xffu;
                const unsigned s = rc & 0xffffu, w = cd >> 3, n = cd & 7, dl = win_start(w) + n;
                const f32x4 ps = *(const f32x4*)(Ptab + s * 16);
                const f32x4 pd = *(const f32x4*)(sm + L_PD + dl * 16);
                const float dx = pd[0] - ps[0], dy = pd[1] - ps[1], dz = pd[2] - ps[2];
                const unsigned e = pk_bf16((dx * dx + dy * dy + dz * dz) * NEG_LOG2E, __uint_as_float(rc & 0xffff0000u) * NEG_LOG2E);
                const unsigned dxy = pk_bf16(dx, dy), dz0 = pk_bf16(dz, 0.f);
                float y = bf2f(((const uint16_t*)(sm + L_AIMG))[dl * 64 + lane]) + bf2f(((const uint16_t*)Btab)[s * 64 + lane]);
                y += bf2f(e & 0xffffu) * wd + bf2f(e >> 16) * wa;
                const float m = bf2f(pk_bf16(y * rcp(1.0f + ex2(y)), 0.f) & 0xffffu);
                char* tgt = sm + L_SLOT + wave_a(w) * 4096;
                atomicAdd((float*)(tgt + n * PROW) + lane, m);
                float t = m * wxl;
                for (int o = 32; o > 0; o >>= 1) t += __shfl_xor(t, o);
                if (lane < 3) {
                    const float dv = lane == 0 ? bf2f(dxy & 0xffffu) : lane == 1 ? bf2f(dxy >> 16) : bf2f(dz0 & 0xffffu);
                    atomicAdd((float*)(tgt + PPOS) + 4 * n + 1 + lane, dv * t);
                }
            }
        }
    }
    const int ct = wv & 3, g = lane >> 4, node = 16 * (wv >> 2) + (lane & 15), nodec = node < BN ? node : BN - 1;
    bf16x8 wfr[4];
#pragma unroll
    for (int s = 0; s < 4; ++s) wfr[s] = wfrag16(sm + L_WH, s, ct, lane);
    f32x4 acc = {0.f, 0.f, 0.f, 0.f};
    acc = MFMA16(wfr[0], nfrag16(sm + L_HIMG, nodec, g), acc);
    acc = MFMA16(wfr[1], nfrag16(sm + L_HIMG, nodec, 4 + g), acc);
    const u32x2 hv = *(const u32x2*)(sm + L_HIMG + nodec * 128 + 16 * ((2 * ct + (g >> 1)) ^ ((nodec >> 1) & 7)) + 8 * (g & 1));
    const unsigned nwin = win_of(nodec);
    const char* pb = sm + L_SLOT + wave_a(nwin) * 4096 + (nodec - win_start(nwin)) * PROW + g * 32;
    const int pd2 = (wave_b(nwin) - wave_a(nwin)) * 4096;
    QSTAMP(8);
    bar();
    QSTAMP(9);
#pragma unroll
    for (int s = 0; s < 2; ++s) {
        const f32x4 m0 = (*(const f32x4*)(pb + s * 128) + *(const f32x4*)(pb + s * 128 + pd2)) * NEG_LN2;
        const f32x4 m1 = (*(const f32x4*)(pb + s * 128 + 16) + *(const f32x4*)(pb + s * 128 + 16 + pd2)) * NEG_LN2;
        acc = MFMA16(wfr[2 + s], as_frag(pk_bf16(m0[0], m0[1]), pk_bf16(m0[2], m0[3]), pk_bf16(m1[0], m1[1]), pk_bf16(m1[2], m1[3])), acc);
    }
    if (wv >= 10) {
        const int x = (wv - 10) * 64 + lane;
        if (x < 3 * BN) {
            const int nl = (x * 171) >> 9, cc = x - 3 * nl, w = win_of(nl), n = nl - win_start(w);
            const char* pp = sm + L_SLOT + PPOS + (4 * n + 1 + cc) * 4;
            const float sum = *(const float*)(pp + wave_a(w) * 4096) + *(const float*)(pp + wave_b(w) * 4096);
            const float p0 = ((const float*)(sm + L_PD))[nl * 4 + cc];
            out[NN * 64 + (b * BN + nl) * 3 + cc] = (uint16_t)(pk_bf16(p0 + sum, 0.f) & 0xffffu);
        }
    }
    QSTAMP(10);
    if (node < BN) {
        const float o0 = bf2f(hv[0] & 0xffffu) + silu_f(acc[0]), o1 = bf2f(hv[0] >> 16) + silu_f(acc[1]);
        const float o2 = bf2f(hv[1] & 0xffffu) + silu_f(acc[2]), o3 = bf2f(hv[1] >> 16) + silu_f(acc[3]);
        *(u32x2*)(out + (b * BN + node) * 64 + 16 * ct + 4 * g) = u32x2{pk_bf16(o0, o1), pk_bf16(o2, o3)};
    }
    QSTAMP(11);
    STAMP_OUT(1, ws);
}

}

extern "C" void kernel_launch(void* const* d_in, const int* in_sizes, int n_in, void* d_out, int out_size, void* d_ws,
                              size_t ws_size, hipStream_t stream) {
    using namespace egnn;
    const uint16_t* h = (const uint16_t*)d_in[0];
    const uint16_t* pos = (const uint16_t*)d_in[1];
    const uint16_t* ea = (const uint16_t*)d_in[2];
    const int* src = (const int*)d_in[3];
    const int* dst = (const int*)d_in[4];
    const uint16_t* we = (const uint16_t*)d_in[5];
    const uint16_t* wx = (const uint16_t*)d_in[6];
    const uint16_t* wh = (const uint16_t*)d_in[7];
    (void)hipFuncSetAttribute((const void*)k_edge, hipFuncAttributeMaxDynamicSharedMemorySize, L_END);
    k_sort<<<NCH, K1_T, 0, stream>>>(src, dst, ea, h, pos, we, wx, (char*)d_ws);
    k_edge<<<256, K2_T, L_END, stream>>>(h, we, wh, (const char*)d_ws, (uint16_t*)d_out);
}
```

```cpp
#include <hip/hip_runtime.h>
#include <stdint.h>

namespace egnn {

typedef short    bf16x8 __attribute__((ext_vector_type(8)));
typedef short    s16x4  __attribute__((ext_vector_type(4)));
typedef float    f32x16 __attribute__((ext_vector_type(16)));
typedef float    f32x4  __attribute__((ext_vector_type(4)));
typedef float    f32x2  __attribute__((ext_vector_type(2)));
typedef __bf16   bf16x2 __attribute__((ext_vector_type(2)));
typedef unsigned u32x4  __attribute__((ext_vector_type(4)));
typedef unsigned u32x2  __attribute__((ext_vector_type(2)));
#define LDS_AS __attribute__((address_space(3)))
#define DI __device__ __forceinline__
#define SBAR() __builtin_amdgcn_sched_barrier(0)
#define PIN(x) asm volatile("" : "+v"(x))
#define MFMA32(a, b, c) __builtin_amdgcn_mfma_f32_32x32x16_bf16(a, b, c, 0, 0, 0)
#define MFMA16(a, b, c) __builtin_amdgcn_mfma_f32_16x16x32_bf16(a, b, c, 0, 0, 0)

constexpr int NN = 10000;
constexpr int NE = 640000;
constexpr int BN = 40;
constexpr int NBK = 250;
constexpr int CE = 2560;
constexpr int NCH = 250;
constexpr float NEG_LOG2E = -1.4426950408889634f;
constexpr float NEG_LN2 = -0.6931471805599453f;

constexpr size_t W_E   = 0;
constexpr size_t W_T   = 4096;
constexpr size_t W_R   = 1u << 20;
constexpr size_t W_B   = (5u << 20) + (512u << 10);
constexpr size_t W_P   = 7u << 20;

DI unsigned win_of(unsigned dl) { return ((dl * 37u) >> 8) + (dl == 34u ? 1u : 0u); }
DI unsigned win_start(unsigned w) { return 7u * w - (w == 5u ? 1u : 0u); }

DI int wave_a(unsigned w) { return (0x987520u >> (4 * w)) & 15; }
DI int wave_b(unsigned w) { return (0xBA4631u >> (4 * w)) & 15; }

DI float bf2f(unsigned v16) { return __uint_as_float(v16 << 16); }
DI unsigned pk_bf16(float lo, float hi) {
    f32x2 x = {lo, hi};
    return __builtin_bit_cast(unsigned, __builtin_convertvector(x, bf16x2));
}
DI float silu_f(float x) {
    return x * __builtin_amdgcn_rcpf(1.0f + __builtin_amdgcn_exp2f(x * NEG_LOG2E));
}
DI s16x4 tr_read(const char* p) {
    return __builtin_amdgcn_ds_read_tr16_b64_v4i16((LDS_AS s16x4*)p);
}
DI bf16x8 cat8(s16x4 a, s16x4 b) { return __builtin_shufflevector(a, b, 0, 1, 2, 3, 4, 5, 6, 7); }
DI void wait_vm0() { asm volatile("s_waitcnt vmcnt(0)" ::: "memory"); }
DI void wait_lgkm0() { asm volatile("s_waitcnt lgkmcnt(0)" ::: "memory"); }
DI void bar() { asm volatile("s_waitcnt lgkmcnt(0)\n\ts_barrier" ::: "memory"); }

DI void dma1(unsigned ldsaddr, const void* gbase, unsigned voff) {
    unsigned keep;
    asm volatile("s_mov_b32 %0, m0\n\ts_mov_b32 m0, %1\n\ts_nop 0\n\t"
                 "global_load_lds_dwordx4 %2, %3\n\t"
                 "s_mov_b32 m0, %0"
                 : "=&s"(keep) : "s"(ldsaddr), "v"(voff), "s"(gbase) : "memory");
}
DI void dma1_dev(unsigned ldsaddr, const void* gbase, unsigned voff) {
    unsigned keep;
    asm volatile("s_mov_b32 %0, m0\n\ts_mov_b32 m0, %1\n\ts_nop 0\n\t"
                 "global_load_lds_dwordx4 %2, %3 sc1\n\t"
                 "s_mov_b32 m0, %0"
                 : "=&s"(keep) : "s"(ldsaddr), "v"(voff), "s"(gbase) : "memory");
}
DI void dma4_dev(unsigned ldsaddr, const void* gbase, unsigned v0, unsigned v1, unsigned v2, unsigned v3) {
    unsigned keep;
    asm volatile("s_mov_b32 %0, m0\n\ts_mov_b32 m0, %1\n\ts_nop 0\n\t"
                 "global_load_lds_dwordx4 %2, %6 sc1\n\t"
                 "global_load_lds_dwordx4 %3, %6 offset:1024 sc1\n\t"
                 "global_load_lds_dwordx4 %4, %6 offset:2048 sc1\n\t"
                 "global_load_lds_dwordx4 %5, %6 offset:3072 sc1\n\t"
                 "s_mov_b32 m0, %0"
                 : "=&s"(keep) : "s"(ldsaddr), "v"(v0), "v"(v1), "v"(v2), "v"(v3), "s"(gbase) : "memory");
}
template <typename T> DI void st_nt(void* p, T v) { __builtin_nontemporal_store(v, (T*)p); }
DI void st_dev(const char* base, unsigned off, u32x4 v) {
    asm volatile("global_store_dwordx4 %0, %1, %2 sc1\n\ts_nop 1" :: "v"(off), "v"(v), "s"(base) : "memory");
}
DI unsigned div3(unsigned x) { return __umul24(x, 43691u) >> 17; }
DI unsigned mad16(unsigned x, unsigned m, unsigned a) {
    unsigned r;
    asm("v_mad_u32_u16 %0, %1, %2, %3" : "=v"(r) : "v"(x), "s"(m), "v"(a));
    return r;
}
template <typename T> DI T lds_ld(unsigned a) { return *(const LDS_AS T*)(size_t)a; }
template <typename T> DI void lds_st(unsigned a, T v) { *(LDS_AS T*)(size_t)a = v; }
template <int CTRL, int ROWMASK> DI unsigned dpp_add(unsigned x) {
    return x + (unsigned)__builtin_amdgcn_update_dpp(0, (int)x, CTRL, ROWMASK, 0xf, true);
}
DI unsigned scan32(unsigned x) {
    x = dpp_add<0x111, 0xf>(x); x = dpp_add<0x112, 0xf>(x); x = dpp_add<0x114, 0xf>(x); x = dpp_add<0x118, 0xf>(x);
    x = dpp_add<0x142, 0xa>(x);
    return x;
}
DI unsigned scan64(unsigned x) { x = scan32(x); x = dpp_add<0x143, 0xc>(x); return x; }
DI int wswz(int k) { return (k & 2) | ((k >> 1) & 4); }
DI bf16x8 wfrag16(const char* img, int s, int ct, int lane) {
    const int g = lane >> 4, q = (lane & 15) >> 2, p = lane & 3;
    const int k0 = 32 * s + 8 * g + q, k1 = k0 + 4, chn = 2 * ct + (p >> 1);
    return cat8(tr_read(img + k0 * 128 + 16 * (chn ^ wswz(k0)) + 8 * (p & 1)),
                tr_read(img + k1 * 128 + 16 * (chn ^ wswz(k1)) + 8 * (p & 1)));
}
DI bf16x8 nfrag16(const char* img, int node, int chunk) {
    return *(const bf16x8*)(img + node * 128 + 16 * (chunk ^ ((node >> 1) & 7)));
}

#define ASTAMP(i)
#define PSTAMP(i)
#define QSTAMP(i)
#define STAMP_DECL
#define STAMP(i)
#define CSTAMP(i)
#define STAMP_OUT(kern, wsp)

constexpr int K2_W = 12, K2_T = 64 * K2_W;
constexpr int RCAP = 3072;
constexpr int L_SLOT = 0;
constexpr int L_STAGE = 49152;
constexpr int RALL = RCAP + 128;
constexpr int L_R32 = 73728;
constexpr int L_R8 = L_R32 + RALL * 4;
constexpr int L_AIMG = L_R8 + RALL;
constexpr int L_HIMG = L_AIMG + 6144;
constexpr int L_WH = L_HIMG + 6144;
constexpr int L_PD = L_WH + 16384;
constexpr int L_OH = L_PD + 768;
constexpr int L_WX = L_OH + 256;
constexpr int L_BINS = L_WX + 256;
constexpr int L_OFFS = L_BINS + 896;
constexpr int L_WIN = L_OFFS + 896;
constexpr int L_WF = L_WIN + 64;
constexpr int L_PB = L_WF + 64;
constexpr int L_END = L_PB + K2_W * 1024;
constexpr int A_CNT = L_SLOT, A_OFF = A_CNT + 1024, A_TOT = A_OFF + 1024, A_R = A_TOT + 16, A_W = A_R + 16384, A_O = A_W + 16384;
static_assert(A_O + BN * 128 <= L_STAGE, "phase A scratch");
constexpr unsigned MAPCAP = 128;
constexpr int PROW = 272, PPOS = 8 * PROW;

DI bf16x8 as_frag(unsigned a, unsigned b, unsigned c, unsigned d) {
    u32x4 v = {a, b, c, d};
    return __builtin_bit_cast(bf16x8, v);
}
DI float ex2(float x) { return __builtin_amdgcn_exp2f(x); }
DI float rcp(float x) { return __builtin_amdgcn_rcpf(x); }

template <class H1, class H2, class H3>
DI void half_tile(const f32x16& xc, f32x16& xn, const bf16x8& aA, const bf16x8& aB, const bf16x8& ef, const bf16x8& eB,
                  const bf16x8& i0, const bf16x8& i1, f32x16& zp, u32x4& pkp, const bf16x8& wp, f32x16& zc,
                  const bf16x8& wc, H1&& hook1, H2&& hook2, H3&& hook3) {
    f32x16 zero;
#pragma unroll
    for (int i = 0; i < 16; ++i) zero[i] = 0.f;
    f32x2 e0, e1, e2, e3;
    u32x4 pk;
#define EXP2(e, i) do { e[0] = ex2(xc[i]); PIN(e[0]); } while (0)
#define EXP2H(e, i) do { e[1] = ex2(xc[i]); PIN(e[1]); } while (0)
#define RCP2(e) do { e[0] = rcp(e[0]); PIN(e[0]); } while (0)
#define RCP2H(e) do { e[1] = rcp(e[1]); PIN(e[1]); } while (0)
#define PKMODE 0
#define ADD1(e) do { e = e + 1.0f; PIN(e); } while (0)
#define MULX(e, i) do { e = e * f32x2{xc[i], xc[i + 1]}; PIN(e); } while (0)
#define CVT(d, e) do { d = __builtin_bit_cast(unsigned, __builtin_convertvector(e, bf16x2)); PIN(d); } while (0)
    xn = MFMA32(aA, i0, zero); SBAR();
    EXP2(e0, 0); EXP2H(e0, 1); EXP2(e1, 2); SBAR();
    zp = MFMA32(__builtin_bit_cast(bf16x8, pkp), wp, zp); SBAR();
    EXP2H(e1, 3); EXP2(e2, 4); EXP2H(e2, 5); EXP2(e3, 6); EXP2H(e3, 7); SBAR();
    ADD1(e0); ADD1(e1); ADD1(e2); ADD1(e3); SBAR();
    hook1(); SBAR();
    xn = MFMA32(aB, i1, xn); SBAR();
    RCP2(e0); RCP2H(e0); RCP2(e1); SBAR();
    xn = MFMA32(ef, eB, xn); SBAR();
    RCP2H(e1); RCP2(e2); RCP2H(e2); RCP2(e3); RCP2H(e3); SBAR();
    MULX(e0, 0); MULX(e1, 2); MULX(e2, 4); MULX(e3, 6); SBAR();
    CVT(pk[0], e0); CVT(pk[1], e1); CVT(pk[2], e2); CVT(pk[3], e3); SBAR();
    EXP2(e0, 8); EXP2H(e0, 9); SBAR();
    zc = MFMA32(__builtin_bit_cast(bf16x8, pk), wc, zc); SBAR();
    EXP2(e1, 10); EXP2H(e1, 11); EXP2(e2, 12); EXP2H(e2, 13); EXP2(e3, 14); EXP2H(e3, 15); SBAR();
    ADD1(e0); ADD1(e1); ADD1(e2); ADD1(e3); SBAR();
    hook2(); SBAR();
    RCP2(e0); RCP2H(e0); RCP2(e1); RCP2H(e1); RCP2(e2); RCP2H(e2); RCP2(e3); RCP2H(e3); SBAR();
    hook3(); SBAR();
    MULX(e0, 8); MULX(e1, 10); MULX(e2, 12); MULX(e3, 14); SBAR();
    CVT(pkp[0], e0); CVT(pkp[1], e1); CVT(pkp[2], e2); CVT(pkp[3], e3); SBAR();
#undef EXP2
#undef EXP2H
#undef RCP2
#undef RCP2H
#undef ADD1
#undef MULX
#undef CVT
}

__global__ __launch_bounds__(K2_T) void k_egnn(const int* __restrict__ src, const int* __restrict__ dst,
                                               const uint16_t* __restrict__ ea, const uint16_t* __restrict__ h,
                                               const uint16_t* __restrict__ pos, const uint16_t* __restrict__ we,
                                               const uint16_t* __restrict__ wx, const uint16_t* __restrict__ wh,
                                               char* __restrict__ ws, uint16_t* __restrict__ out) {
    extern __shared__ __attribute__((aligned(16))) char sm[];
    const int tid = threadIdx.x, lane = tid & 63;
    const int wv = __builtin_amdgcn_readfirstlane(tid >> 6);
    const int xc = blockIdx.x & 7;
    const int b = 31 * xc + (xc < 2 ? xc : 2) + (blockIdx.x >> 3);
    const unsigned smb = (unsigned)(size_t)(LDS_AS char*)sm;
    asm volatile("" :: "s"(src), "s"(dst), "s"(ea), "s"(h), "s"(pos), "s"(we), "s"(wx), "s"(wh), "s"(ws), "s"(out));
    const char* Rec = ws + W_R;
    const char* Btab = ws + W_B;
    const char* Ptab = ws + W_P;
    STAMP_DECL
    STAMP(0);

    u32x2 ep;
    asm volatile("global_load_dwordx2 %0, %1, %2" : "=v"(ep) : "v"(0u), "s"(ws + W_E) : "memory");
    u32x4 d4 = {0, 0, 0, 0}, s4 = {0, 0, 0, 0};
    u32x2 a4 = {0, 0};
    unsigned sm0 = 0, sm1 = 0;
    const int r8l = lane >> 3, c8 = lane & 7;
    const int gnode = b * BN + (lane < BN ? lane : 0);
    {
        auto piece = [&](int p) {
            if (p < 5) dma1(smb + L_HIMG + p * 1024, h, (b * BN + 8 * p + r8l) * 128 + ((c8 ^ ((4 * p + (lane >> 4)) & 7)) * 16));
            else if (p < 21) dma1(smb + A_W + (p - 5) * 1024, we, (8 * (p - 5) + r8l) * 128 + ((c8 ^ ((r8l & 2) | (((p - 5) & 1) << 2))) * 16));
            else if (lane < 16) dma1(smb + L_AIMG + BN * 128, we, 128 * 128 + lane * 16);
        };
        if (wv < 10) {
            const int e0 = b * CE + 4 * tid;
            asm volatile("global_load_dwordx4 %0, %3, %4\n\tglobal_load_dwordx4 %1, %3, %5\n\tglobal_load_dwordx2 %2, %6, %7"
                         : "=&v"(d4), "=&v"(s4), "=&v"(a4)
                         : "v"(e0 * 4), "s"(dst), "s"(src), "v"(e0 * 2), "s"(ea) : "memory");
            piece(wv);
        } else {
            if (wv == 10)
                asm volatile("global_load_dword %0, %2, %3\n\tglobal_load_ushort %1, %2, %3 offset:4"
                             : "=&v"(sm0), "=&v"(sm1) : "v"(gnode * 6), "s"(pos) : "memory");
            else
                asm volatile("global_load_ushort %0, %1, %2" : "=v"(sm0) : "v"(lane * 2), "s"(wx) : "memory");
#pragma clang loop unroll(disable)
            for (int p = 10 + 6 * (wv - 10); p < 16 + 6 * (wv - 10); ++p) piece(p);
        }
    }
    {
        const u32x4 z = {0, 0, 0, 0};
        lds_st(smb + L_STAGE + wv * 2048 + lane * 16, z);
        lds_st(smb + L_STAGE + wv * 2048 + 1024 + lane * 16, z);
        const u32x4 n32 = {NN, NN, NN, NN}, n8 = {0x08080808u, 0x08080808u, 0x08080808u, 0x08080808u};
        lds_st(smb + L_R32 + tid * 16, n32);
        if (wv == 0) {
            lds_st(smb + A_CNT + lane * 16, z);
            if (lane < RALL / 4 - K2_T) lds_st(smb + L_R32 + (K2_T + lane) * 16, n32);
        } else if (wv < 5) {
            if (tid - 64 < RALL / 16) lds_st(smb + L_R8 + (tid - 64) * 16, n8);
        } else if (wv == 5) {
            if (lane < 56) lds_st(smb + L_BINS + lane * 16, z);
        } else if (wv == 6) {
            if (lane < 48) lds_st(smb + L_AIMG + (BN + 2) * 128 + lane * 16, z);
        } else if (wv == 7) {
            const int n = lane >> 2, j = lane & 3;
            if (lane < 36) lds_st(smb + L_OH + lane * 4, (n < 8 && (n >> 1) == j) ? (0x3F80u << (16 * (n & 1))) : 0u);
        }
    }
    bar();
    ASTAMP(1);
    asm volatile("s_waitcnt vmcnt(0)" : "+v"(ep), "+v"(d4), "+v"(s4), "+v"(a4), "+v"(sm0), "+v"(sm1) :: "memory");
    ASTAMP(2);
    const unsigned e_lo = __builtin_amdgcn_readfirstlane(ep[0]), e_hi = __builtin_amdgcn_readfirstlane(ep[1]);
    const unsigned n_lo = e_lo + 1u, n_hi = e_hi + (n_lo == 0u ? 1u : 0u);
    const unsigned tag_w1 = (n_lo >> 8) | (n_hi << 24), tag_w0 = n_lo << 24;

    unsigned bk[4], ark[4], code = 0;
    if (wv < 10) {
#pragma unroll
        for (int i = 0; i < 4; ++i) {
            bk[i] = __umul24(d4[i], 52429u) >> 21;
            ark[i] = atomicAdd((unsigned*)(sm + A_CNT) + bk[i], 1u);
        }
#pragma unroll
        for (int i = 0; i < 4; ++i) {
            const unsigned dl = d4[i] - bk[i] * BN, w = win_of(dl);
            code |= (w * 8 + dl - win_start(w)) << (8 * i);
        }
    } else if (wv == 10) {
        const f32x4 p = {bf2f(sm0 & 0xffffu), __uint_as_float(sm0 & 0xffff0000u), bf2f(sm1), 0.f};
        if (lane < 48) lds_st(smb + L_PD + lane * 16, lane < BN ? p : f32x4{0.f, 0.f, 0.f, 0.f});
        if (lane < BN) st_dev(Ptab, gnode * 16, __builtin_bit_cast(u32x4, p));
    } else {
        lds_st(smb + L_WX + lane * 4, bf2f(sm0) * NEG_LN2);
        if (b == 0) {
            const u32x4 z = {0, 0, 0, 0};
            st_dev(Ptab, NN * 16 + lane * 16, z);
            if (lane < 8) st_dev(Btab, NN * 128 + lane * 16, z);
        }
    }
    ASTAMP(3);
    bar();
    ASTAMP(4);

    u32x4 t_lo = {0, 0, 0, 0};
    if (wv == 11) {
        const u32x4 cn = lds_ld<u32x4>(smb + A_CNT + lane * 16);
        const u32x4 tr = {div3(cn[0] + 2), div3(cn[1] + 2), div3(cn[2] + 2), div3(cn[3] + 2)};
        const unsigned tot = tr[0] + tr[1] + tr[2] + tr[3];
        const unsigned in = scan64(tot), ex = in - tot;
        const u32x4 of = {ex, ex + tr[0], ex + tr[0] + tr[1], ex + tr[0] + tr[1] + tr[2]};
        lds_st(smb + A_OFF + lane * 16, of);
        if (lane == 63) lds_st(smb + A_TOT, in);
#pragma unroll
        for (int i = 0; i < 4; ++i) t_lo[i] = tag_w0 | (cn[i] << 12) | of[i];
    } else if (wv < 8) {
        const int ct = wv & 3, g = lane >> 4;
        const char* wimg = sm + A_W + (wv >> 2) * 8192;
        const bf16x8 a0 = wfrag16(wimg, 0, ct, lane), a1 = wfrag16(wimg, 1, ct, lane);
#pragma unroll
        for (int nt = 0; nt < 3; ++nt) {
            const int node = 16 * nt + (lane & 15);
            f32x4 acc = {0.f, 0.f, 0.f, 0.f};
            acc = MFMA16(a0, nfrag16(sm + L_HIMG, node, g), acc);
            acc = MFMA16(a1, nfrag16(sm + L_HIMG, node, 4 + g), acc);
            const u32x2 o = {pk_bf16(acc[0] * NEG_LOG2E, acc[1] * NEG_LOG2E), pk_bf16(acc[2] * NEG_LOG2E, acc[3] * NEG_LOG2E)};
            const unsigned oa = wv < 4 ? L_AIMG + node * 128 + (16 * ct + 4 * g) * 2
                                       : A_O + node * 128 + 16 * ((2 * ct + (g >> 1)) ^ (node & 7)) + 8 * (g & 1);
            if (node < BN) lds_st(smb + oa, o);
        }
    }
    ASTAMP(5);
    bar();
    ASTAMP(6);

    if (wv < 10) {
#pragma unroll
        for (int i = 0; i < 4; ++i) {
            const unsigned q = div3(ark[i]), j = ark[i] - 3 * q;
            const unsigned t = smb + A_R + (lds_ld<unsigned>(smb + A_OFF + bk[i] * 4) + q) * 16;
            const unsigned av = (i & 1) ? (a4[i >> 1] & 0xffff0000u) : (a4[i >> 1] << 16);
            lds_st(t + j * 4, s4[i] | av);
            lds_st(t + 12 + j, (unsigned char)(code >> (8 * i)));
        }
    } else {
#pragma clang loop unroll(disable)
        for (int x = tid - 640; x < BN * 8; x += 128) {
            const int node = x >> 3, ch = (x & 7) ^ (node & 7);
            st_dev(Btab, (b * BN + node) * 128 + ch * 16, lds_ld<u32x4>(smb + A_O + x * 16));
        }
    }
    bar();
    ASTAMP(7);
    {
        const unsigned ntot = lds_ld<unsigned>(smb + A_TOT);
        if ((unsigned)tid < ntot) st_dev(Rec, b * 16384 + tid * 16, lds_ld<u32x4>(smb + A_R + tid * 16));
        if ((unsigned)tid + K2_T < ntot) st_dev(Rec, b * 16384 + (tid + K2_T) * 16, lds_ld<u32x4>(smb + A_R + (tid + K2_T) * 16));
    }
    ASTAMP(8);
    wait_vm0();
    ASTAMP(9);
    bar();
    if (wv == 11) {
        st_dev(ws + W_T, b * 2048 + lane * 32, u32x4{t_lo[0], tag_w1, t_lo[1], tag_w1});
        st_dev(ws + W_T, b * 2048 + lane * 32 + 16, u32x4{t_lo[2], tag_w1, t_lo[3], tag_w1});
    }
    ASTAMP(10);

#pragma clang loop unroll(disable)
    for (int q = wv; q < 16; q += K2_W)
        dma1(smb + L_WH + q * 1024, wh, (8 * q + r8l) * 128 + ((c8 ^ ((r8l & 2) | ((q & 1) << 2))) * 16));
    unsigned tw;
    asm volatile("global_load_dword %0, %1, %2" : "=v"(tw)
                 : "v"(lane == 0 ? b * (BN * 128) : (NN * 128 + b * (BN * 6)) & ~3), "s"(out) : "memory");

    const int l3 = (lane * 171) >> 9, sub = lane - 3 * l3;
    const int cid = 21 * wv + l3;
    const bool cok = lane < 63 && cid < NCH;
    const int cidc = cok ? cid : 0;
    unsigned tval = 0;
    bool lost = true;
#pragma clang loop unroll(disable)
    for (int spin = 0; spin < (1 << 16); ++spin) {
        u32x2 tv;
        asm volatile("global_load_dwordx2 %0, %2, %3 sc1\n\ts_waitcnt vmcnt(0)"
                     : "=&v"(tv), "+v"(tw) : "v"((unsigned)W_T + cidc * 2048 + b * 8), "s"(ws) : "memory");
        tval = tv[0];
        if (__all(!cok || (tv[1] == tag_w1 && (tv[0] >> 24) == (tag_w0 >> 24)))) { lost = false; break; }
        __builtin_amdgcn_s_sleep(2);
    }
    PSTAMP(2);

    const unsigned cnt = (cok && !lost) ? ((tval >> 12) & 0xfffu) : 0u;
    const unsigned ntr = div3(cnt + 2);
    const unsigned incl = scan64(sub == 0 ? ntr : 0u);
    const unsigned mw = __builtin_amdgcn_readlane(incl, 63);
    const bool slow_w = mw > MAPCAP;
    const unsigned map_a = smb + L_SLOT + wv * 4096;
    QSTAMP(1);
    if (!slow_w) {
        unsigned g = (cidc * 1024 + (tval & 0xfffu) + sub) | ((cnt - 3 * sub) << 20);
        unsigned mp = map_a + (incl - ntr + sub) * 4;
#pragma clang loop unroll(disable) vectorize(disable) interleave(disable)
        for (unsigned i = sub; i < ntr; i += 3) { lds_st(mp, g); mp += 12; g += 3 - (9u << 20); }
    }
    PSTAMP(3);
    const unsigned mwe = slow_w ? 0u : mw;
    unsigned left[2];
    u32x4 rec[2];
#pragma unroll
    for (int k = 0; k < 2; ++k) {
        const unsigned m = lds_ld<unsigned>(map_a + (64 * k + lane) * 4);
        const bool ok = (unsigned)(64 * k + lane) < mwe;
        left[k] = ok ? m >> 20 : 0u;
        const unsigned go = ok ? (m & 0xfffffu) * 16 : 0u;
        asm volatile("global_load_dwordx4 %0, %1, %2 sc1" : "=v"(rec[k]) : "v"(go), "s"(Rec) : "memory");
    }
    if (lane == 0) lds_st(smb + L_WF + wv * 4, lost ? 2u : slow_w ? 1u : 0u);
    PSTAMP(4);
    asm volatile("s_waitcnt vmcnt(0)" : "+v"(rec[0]), "+v"(rec[1]) :: "memory");
    PSTAMP(5);
    unsigned bin[6], rk[6];
#pragma unroll
    for (int k = 0; k < 6; ++k) {
        const unsigned w = (rec[k / 3][3] >> (8 * (k % 3) + 3)) & 7u;
        bin[k] = (left[k / 3] > (unsigned)(k % 3) ? w : 6u) * 128 + (lane & 31) * 4;
        rk[k] = atomicAdd((unsigned*)(sm + L_BINS + bin[k]), 1u);
    }
    QSTAMP(3);
    PSTAMP(6);
    bar();
    PSTAMP(7);
    unsigned pf;
    {
        const unsigned part = blockIdx.x >> 3;
        const unsigned off = tid < 626 ? (unsigned)W_B + part * (313 * 128) + tid * 64
                                       : (unsigned)W_P + part * (40 * 128) + (tid < 706 ? tid - 626 : 0) * 64;
        asm volatile("global_load_dword %0, %1, %2 sc1" : "=v"(pf) : "v"(off), "s"(ws) : "memory");
    }

    if (wv == 0) {
        const unsigned* bins = (const unsigned*)(sm + L_BINS);
        const int j = lane & 31, hi = lane >> 5;
        const unsigned ca = bins[(2 * hi) * 32 + j] | (bins[(2 * hi + 1) * 32 + j] << 16);
        const unsigned cb = bins[4 * 32 + j] | (bins[5 * 32 + j] << 16);
        const unsigned wfl = lds_ld<unsigned>(smb + L_WF + (lane < K2_W ? lane : 0) * 4);
        const unsigned ia = scan32(ca), ib = scan32(cb);
        const unsigned t01 = __builtin_amdgcn_readlane(ia, 31), t23 = __builtin_amdgcn_readlane(ia, 63);
        const unsigned t45 = __builtin_amdgcn_readlane(ib, 31);
        const unsigned tot[6] = {t01 & 0xffffu, t01 >> 16, t23 & 0xffffu, t23 >> 16, t45 & 0xffffu, t45 >> 16};
        unsigned st[7];
        st[0] = 0;
#pragma unroll
        for (int w = 0; w < 6; ++w) st[w + 1] = st[w] + ((tot[w] + 31) & ~31u);
        const unsigned xa = ia - ca, xb = ib - cb;
        unsigned* offs = (unsigned*)(sm + L_OFFS);
        offs[(2 * hi) * 32 + j] = (hi ? st[2] : st[0]) + (xa & 0xffffu);
        offs[(2 * hi + 1) * 32 + j] = (hi ? st[3] : st[1]) + (xa >> 16);
        offs[(4 + hi) * 32 + j] = hi ? st[5] + (xb >> 16) : st[4] + (xb & 0xffffu);
        const bool any_slow = __any(wfl != 0), any_lost = __any(wfl == 2);
        unsigned wi = 0;
#pragma unroll
        for (int w = 0; w < 6; ++w) { wi = lane == w ? st[w] : wi; wi = lane == 6 + w ? (st[w + 1] - st[w]) >> 5 : wi; }
        wi = lane == 12 ? (any_lost ? 2u : (any_slow || st[6] > RCAP) ? 1u : 0u) : wi;
        if (lane < 13) ((unsigned*)(sm + L_WIN))[lane] = wi;
    } else if (wv == 2) {
        if (b == 0 && lane == 0) *(u32x2*)(ws + W_E) = u32x2{n_lo, n_hi};
    }
    PSTAMP(8);
    bar();
    QSTAMP(4);
    PSTAMP(9);
    const unsigned mode = __builtin_amdgcn_readfirstlane(((const unsigned*)(sm + L_WIN))[12]);
    if (mode == 2) { asm volatile("s_waitcnt vmcnt(0)" : "+v"(pf) :: "memory"); return; }
    const bool slow = mode != 0;

    const int r = lane & 31, hh = lane >> 5;
    const int win = (int)((0x545432231100ull >> (4 * wv)) & 7);
    const bool second = (0xC5A >> wv) & 1;
    const int wnode = win_start(win);
    char* part = sm + L_SLOT + wv * 4096;

    if (!slow) {
#pragma unroll
        for (int k = 0; k < 6; ++k) {
            const unsigned o = lds_ld<unsigned>(smb + L_OFFS + bin[k]) + rk[k];
            const unsigned p = left[k / 3] > (unsigned)(k % 3) ? o : (unsigned)(RALL - 1);
            lds_st(smb + L_R32 + p * 4, rec[k / 3][k % 3]);
            lds_st(smb + L_R8 + p, (unsigned char)((rec[k / 3][3] >> (8 * (k % 3))) & 7u));
        }
        const unsigned wiv = lds_ld<unsigned>(smb + L_WIN + (lane & 15) * 4);
        int tw6[6];
#pragma unroll
        for (int w = 0; w < 6; ++w) tw6[w] = __builtin_amdgcn_readlane(wiv, 6 + w);
        const int wstart = __builtin_amdgcn_readlane(wiv, win);
        const int l0 = tw6[0] + tw6[3] + tw6[4], l1 = tw6[0] + tw6[2] + tw6[5];
        const int l2 = tw6[1] + tw6[2] + tw6[4], l3s = tw6[1] + tw6[3] + tw6[5];
        const int wtiles = win == 0 ? tw6[0] : win == 1 ? tw6[1] : win == 2 ? tw6[2] : win == 3 ? tw6[3] : win == 4 ? tw6[4] : tw6[5];
        const int la = win == 0 ? l0 : win == 1 ? l2 : win == 2 ? l1 : win == 3 ? l3s : win == 4 ? l0 : l1;
        const int lb = win == 0 ? l1 : win == 1 ? l3s : win == 2 ? l2 : win == 3 ? l0 : win == 4 ? l2 : l3s;
        int first = (4 * wtiles - la + lb + 4) >> 3;
        first = first < 0 ? 0 : first > wtiles ? wtiles : first;
        const int ta = second ? first : 0, tb = second ? wtiles : first;

        const int blk = (lane >> 4) & 1, q = (lane & 15) >> 2, p = lane & 3;
        bf16x8 i0, i1;
#pragma unroll
        for (int j = 0; j < 8; ++j) {
            i0[j] = (r == 8 * hh + j) ? (short)0x3F80 : (short)0;
            i1[j] = (r == 16 + 8 * hh + j) ? (short)0x3F80 : (short)0;
        }
        const int rho = 8 * (r & 3) + (r >> 2);
        const int fsw = (r & 3) ^ (((r >> 3) & 1) << 2);
        unsigned aad[4];
#pragma unroll
        for (int s = 0; s < 4; ++s) aad[s] = smb + L_SLOT + wv * 4096 + rho * 128 + 16 * ((2 * s + hh) ^ fsw);
        unsigned kk[4];
#pragma unroll
        for (int i = 0; i < 4; ++i)
            kk[i] = 16 * ((lane & 7) ^ i ^ (((lane >> 4) & 1) << 2)) + (3072 - 1024 * i);
        const char* gB = Btab - 3072;
        const unsigned slot_a = smb + L_SLOT + wv * 4096;
        const unsigned pbuf_a = smb + L_PB + wv * 1024;
        const unsigned src_a = smb + L_R32 + (lane >> 3) * 16, rs_a = smb + L_R32 + lane * 4, rn_a = smb + L_R8 + lane;
        const unsigned stage_r = smb + L_STAGE + wv * 2048 + r * 64;
        const char* trb = sm + L_STAGE + wv * 2048 + (4 * hh + q) * 64 + blk * 32 + p * 8;
        const unsigned pdw = smb + L_PD + wnode * 16;
        const unsigned oh_a = smb + L_OH;
        bar();
        QSTAMP(5);
        PSTAMP(10);

        bf16x8 e0, e1;
        {
            const int row = hh ? BN + q : wnode + q;
            const char* ab = sm + L_AIMG + row * 128 + (16 * blk + 4 * p) * 2;
            e0 = cat8(tr_read(ab), tr_read(ab + 512));
            e1 = cat8(tr_read(ab + 64), tr_read(ab + 64 + 512));
        }
        f32x16 z0, z1, xa, xb;
#pragma unroll
        for (int i = 0; i < 16; ++i) { z0[i] = 0.f; z1[i] = 0.f; }
        QSTAMP(6);
        CSTAMP(12);

        if (ta < tb) {
            int base = wstart + 32 * ta;
            const int end = wstart + 32 * tb;
            bf16x8 a0, a1, a2, a3, ef, wf0, wf1;
            unsigned rs, rn, w0, w1, ua, vb;
            unsigned saddr;
            bool valid;
            u32x4 pkp = {0, 0, 0, 0};
#pragma unroll
            for (int j = 0; j < 8; ++j) wf1[j] = 0;

            u32x4 sn, exr;
            f32x4 pd, ps;
            auto read_src = [&](int ri) { sn = lds_ld<u32x4>(src_a + ri * 4); };
            auto issue_rows = [&]() {
                const unsigned v0 = mad16(sn[0], 128, kk[0]), v1 = mad16(sn[1], 128, kk[1]);
                const unsigned v2 = mad16(sn[2], 128, kk[2]), v3 = mad16(sn[3], 128, kk[3]);
                wait_lgkm0();
                dma4_dev(slot_a, gB, v0, v1, v2, v3);
            };
            auto read_pair = [&](int ri) {
                rs = lds_ld<unsigned>(rs_a + ri * 4);
                rn = lds_ld<unsigned char>(rn_a + ri);
            };
            auto issue_pos = [&]() { dma1_dev(pbuf_a, Ptab, mad16(rs, 16, 0u)); };
            auto read_pos = [&]() {
                pd = lds_ld<f32x4>(pdw + rn * 16);
                ps = lds_ld<f32x4>(pbuf_a + lane * 16);
            };
            auto geometry = [&]() {
                const float dx = pd[0] - ps[0], dy = pd[1] - ps[1], dz = pd[2] - ps[2];
                const float d2 = (dx * dx + dy * dy + dz * dz) * NEG_LOG2E;
                w0 = pk_bf16(1.0f, dx);
                w1 = pk_bf16(dy, dz);
                const unsigned ext = pk_bf16(d2, __uint_as_float(rs & 0xffff0000u) * NEG_LOG2E);
                const auto sw = __builtin_amdgcn_permlane32_swap(oh_a + rn * 16, ext, false, false);
                ua = sw[0];
                vb = sw[1];
                saddr = stage_r + rn * 8;
                PIN(saddr); PIN(w0); PIN(w1);
                valid = rn < 8;
            };
            auto read_rows = [&]() {
                a0 = lds_ld<bf16x8>(aad[0]); a1 = lds_ld<bf16x8>(aad[1]);
                a2 = lds_ld<bf16x8>(aad[2]); a3 = lds_ld<bf16x8>(aad[3]);
            };
            auto read_ext = [&](unsigned u) { exr = lds_ld<u32x4>(hh ? oh_a : u); };
            auto fix_ext = [&](unsigned u) {
                u32x4 ex = exr;
                ex[0] = hh ? u : ex[0];
                ef = __builtin_bit_cast(bf16x8, ex);
                PIN(ef);
            };
            auto stage_weights = [&](bool wr) {
                if (wr) lds_st(saddr, u32x2{w0, w1});
                wf0 = cat8(tr_read(trb), tr_read(trb + 512));
                wf1 = cat8(tr_read(trb + 1024), tr_read(trb + 1536));
                if (wr) lds_st(saddr, u32x2{0, 0});
            };
            auto nop = [&]() {};

            read_pair(base);
            read_src(base);
            issue_rows();
            issue_pos();
            if (base + 32 < end) read_src(base + 32);
            PSTAMP(11);
            asm volatile("s_waitcnt vmcnt(0)" : "+v"(pf) :: "memory");
            PSTAMP(12);
            read_pos();
            read_rows();
            geometry();
            read_ext(ua);
            if (base + 32 < end) issue_rows();
            fix_ext(ua);
            {
                f32x16 zero;
#pragma unroll
                for (int i = 0; i < 16; ++i) zero[i] = 0.f;
                xa = MFMA32(a0, i0, zero);
                xa = MFMA32(a1, i1, xa);
                xa = MFMA32(ef, e0, xa);
            }
            SBAR();
            PSTAMP(13);
            auto set_prio = [&](int rem) {
                if (rem >= 192) __builtin_amdgcn_s_setprio(3);
                else if (rem >= 128) __builtin_amdgcn_s_setprio(2);
                else if (rem >= 64) __builtin_amdgcn_s_setprio(1);
                else __builtin_amdgcn_s_setprio(0);
            };
            while (true) {
                set_prio(end - base);
                half_tile(xa, xb, a2, a3, ef, e1, i0, i1, z1, pkp, wf1, z0, wf0,
                          [&]() { stage_weights(valid && hh == 0); read_src(base + 64); read_pair(base + 64); },
                          [&]() { wait_vm0(); read_rows(); read_ext(vb); },
                          nop);
                half_tile(xb, xa, a0, a1, ef, e0, i0, i1, z0, pkp, wf1, z1, wf0,
                          [&]() { fix_ext(vb); if (base + 64 < end) issue_rows(); issue_pos(); }, nop, nop);
                if (base + 32 >= end) break;
                half_tile(xa, xb, a2, a3, ef, e1, i0, i1, z1, pkp, wf1, z0, wf0,
                          [&]() { stage_weights(valid && hh == 1); read_src(base + 96); },
                          [&]() { wait_vm0(); read_rows(); read_pos(); },
                          [&]() { geometry(); read_ext(ua); });
                half_tile(xb, xa, a0, a1, ef, e0, i0, i1, z0, pkp, wf1, z1, wf0,
                          [&]() { fix_ext(ua); if (base + 96 < end) issue_rows(); }, nop, nop);
                base += 64;
                if (base >= end) break;
            }
            z1 = MFMA32(__builtin_bit_cast(bf16x8, pkp), wf1, z1);
        }
        asm volatile("s_waitcnt vmcnt(0)" : "+v"(pf) :: "memory");
        CSTAMP(13);
        QSTAMP(7);
        float cs0 = 0.f, cs1 = 0.f;
#pragma unroll
        for (int g = 0; g < 4; ++g) {
            const f32x4 w0 = *(const f32x4*)(sm + L_WX + (8 * g + 4 * hh) * 4);
            const f32x4 w1 = *(const f32x4*)(sm + L_WX + (32 + 8 * g + 4 * hh) * 4);
#pragma unroll
            for (int i = 0; i < 4; ++i) { cs0 += z0[4 * g + i] * w0[i]; cs1 += z1[4 * g + i] * w1[i]; }
        }
        const float csh = cs0 + cs1;
        const auto cw = __builtin_amdgcn_permlane32_swap(__float_as_uint(csh), __float_as_uint(csh), false, false);
        const float cst = __uint_as_float(cw[0]) + __uint_as_float(cw[1]);
        wait_vm0();
        if (hh == 0) ((float*)(part + PPOS))[r] = cst;
        if ((lane & 3) == 0) {
            const int n = r >> 2;
#pragma unroll
            for (int g = 0; g < 4; ++g) {
                *(f32x4*)(part + n * PROW + (8 * g + 4 * hh) * 4) = f32x4{z0[4 * g], z0[4 * g + 1], z0[4 * g + 2], z0[4 * g + 3]};
                *(f32x4*)(part + n * PROW + (32 + 8 * g + 4 * hh) * 4) = f32x4{z1[4 * g], z1[4 * g + 1], z1[4 * g + 2], z1[4 * g + 3]};
            }
        }
    } else {
        __builtin_amdgcn_fence(__ATOMIC_ACQUIRE, "agent");
        asm volatile("s_waitcnt vmcnt(0)" : "+v"(pf) :: "memory");
        {
            const u32x4 z = {0, 0, 0, 0};
            *(u32x4*)(part + lane * 16) = z;
            *(u32x4*)(part + 1024 + lane * 16) = z;
            *(u32x4*)(part + 2048 + lane * 16) = z;
        }
        bar();
        const float wd = bf2f(we[128 * 64 + lane]), wa = bf2f(we[129 * 64 + lane]);
        const float wxl = ((const float*)(sm + L_WX))[lane];
        for (int ci = wv; ci < NCH; ci += K2_W) {
            const unsigned tv = *(const unsigned*)(ws + W_T + ci * 2048 + b * 8);
            const unsigned g0 = ci * 1024 + (tv & 0xfffu), cn = (tv >> 12) & 0xfffu;
            for (unsigned i = 0; i < cn; ++i) {
                const unsigned* tp = (const unsigned*)(Rec + (size_t)(g0 + div3(i)) * 16);
                const unsigned j = i - 3 * div3(i);
                const unsigned rc = tp[j], cd = (tp[3] >> (8 * j)) & 0xffu;
                const unsigned s = rc & 0xffffu, w = cd >> 3, n = cd & 7, dl = win_start(w) + n;
                const f32x4 ps = *(const f32x4*)(Ptab + s * 16);
                const f32x4 pd = *(const f32x4*)(sm + L_PD + dl * 16);
                const float dx = pd[0] - ps[0], dy = pd[1] - ps[1], dz = pd[2] - ps[2];
                const unsigned e = pk_bf16((dx * dx + dy * dy + dz * dz) * NEG_LOG2E, __uint_as_float(rc & 0xffff0000u) * NEG_LOG2E);
                const unsigned dxy = pk_bf16(dx, dy), dz0 = pk_bf16(dz, 0.f);
                float y = bf2f(((const uint16_t*)(sm + L_AIMG))[dl * 64 + lane]) + bf2f(((const uint16_t*)Btab)[s * 64 + lane]);
                y += bf2f(e & 0xffffu) * wd + bf2f(e >> 16) * wa;
                const float m = bf2f(pk_bf16(y * rcp(1.0f + ex2(y)), 0.f) & 0xffffu);
                char* tgt = sm + L_SLOT + wave_a(w) * 4096;
                atomicAdd((float*)(tgt + n * PROW) + lane, m);
                float t = m * wxl;
                for (int o = 32; o > 0; o >>= 1) t += __shfl_xor(t, o);
                if (lane < 3) {
                    const float dv = lane == 0 ? bf2f(dxy & 0xffffu) : lane == 1 ? bf2f(dxy >> 16) : bf2f(dz0 & 0xffffu);
                    atomicAdd((float*)(tgt + PPOS) + 4 * n + 1 + lane, dv * t);
                }
            }
        }
    }
    const int ct = wv & 3, g = lane >> 4, node = 16 * (wv >> 2) + (lane & 15), nodec = node < BN ? node : BN - 1;
    bf16x8 wfr[4];
#pragma unroll
    for (int s = 0; s < 4; ++s) wfr[s] = wfrag16(sm + L_WH, s, ct, lane);
    f32x4 acc = {0.f, 0.f, 0.f, 0.f};
    acc = MFMA16(wfr[0], nfrag16(sm + L_HIMG, nodec, g), acc);
    acc = MFMA16(wfr[1], nfrag16(sm + L_HIMG, nodec, 4 + g), acc);
    const u32x2 hv = *(const u32x2*)(sm + L_HIMG + nodec * 128 + 16 * ((2 * ct + (g >> 1)) ^ ((nodec >> 1) & 7)) + 8 * (g & 1));
    const unsigned nwin = win_of(nodec);
    const char* pb = sm + L_SLOT + wave_a(nwin) * 4096 + (nodec - win_start(nwin)) * PROW + g * 32;
    const int pd2 = (wave_b(nwin) - wave_a(nwin)) * 4096;
    QSTAMP(8);
    bar();
    QSTAMP(9);
#pragma unroll
    for (int s = 0; s < 2; ++s) {
        const f32x4 m0 = (*(const f32x4*)(pb + s * 128) + *(const f32x4*)(pb + s * 128 + pd2)) * NEG_LN2;
        const f32x4 m1 = (*(const f32x4*)(pb + s * 128 + 16) + *(const f32x4*)(pb + s * 128 + 16 + pd2)) * NEG_LN2;
        acc = MFMA16(wfr[2 + s], as_frag(pk_bf16(m0[0], m0[1]), pk_bf16(m0[2], m0[3]), pk_bf16(m1[0], m1[1]), pk_bf16(m1[2], m1[3])), acc);
    }
    if (wv >= 10) {
        const int x = (wv - 10) * 64 + lane;
        if (x < 3 * BN) {
            const int nl = (x * 171) >> 9, cc = x - 3 * nl, w = win_of(nl), n = nl - win_start(w);
            const char* pp = sm + L_SLOT + PPOS + (4 * n + 1 + cc) * 4;
            const float sum = *(const float*)(pp + wave_a(w) * 4096) + *(const float*)(pp + wave_b(w) * 4096);
            const float p0 = ((const float*)(sm + L_PD))[nl * 4 + cc];
            out[NN * 64 + (b * BN + nl) * 3 + cc] = (uint16_t)(pk_bf16(p0 + sum, 0.f) & 0xffffu);
        }
    }
    QSTAMP(10);
    if (node < BN) {
        const float o0 = bf2f(hv[0] & 0xffffu) + silu_f(acc[0]), o1 = bf2f(hv[0] >> 16) + silu_f(acc[1]);
        const float o2 = bf2f(hv[1] & 0xffffu) + silu_f(acc[2]), o3 = bf2f(hv[1] >> 16) + silu_f(acc[3]);
        st_nt(out + (b * BN + node) * 64 + 16 * ct + 4 * g, u32x2{pk_bf16(o0, o1), pk_bf16(o2, o3)});
    }
    QSTAMP(11);
    STAMP_OUT(1, ws);
}

}

extern "C" void kernel_launch(void* const* d_in, const int* in_sizes, int n_in, void* d_out, int out_size, void* d_ws,
                              size_t ws_size, hipStream_t stream) {
    using namespace egnn;
    const uint16_t* h = (const uint16_t*)d_in[0];
    const uint16_t* pos = (const uint16_t*)d_in[1];
    const uint16_t* ea = (const uint16_t*)d_in[2];
    const int* src = (const int*)d_in[3];
    const int* dst = (const int*)d_in[4];
    const uint16_t* we = (const uint16_t*)d_in[5];
    const uint16_t* wx = (const uint16_t*)d_in[6];
    const uint16_t* wh = (const uint16_t*)d_in[7];
    (void)hipFuncSetAttribute((const void*)k_egnn, hipFuncAttributeMaxDynamicSharedMemorySize, L_END);
    k_egnn<<<NBK, K2_T, L_END, stream>>>(src, dst, ea, h, pos, we, wx, wh, (char*)d_ws, (uint16_t*)d_out);
}
```

```cpp
#include <hip/hip_runtime.h>
#include <stdint.h>

namespace egnn {

typedef short    bf16x8 __attribute__((ext_vector_type(8)));
typedef short    s16x4  __attribute__((ext_vector_type(4)));
typedef float    f32x16 __attribute__((ext_vector_type(16)));
typedef float    f32x4  __attribute__((ext_vector_type(4)));
typedef float    f32x2  __attribute__((ext_vector_type(2)));
typedef __bf16   bf16x2 __attribute__((ext_vector_type(2)));
typedef unsigned u32x4  __attribute__((ext_vector_type(4)));
typedef unsigned u32x2  __attribute__((ext_vector_type(2)));
#define LDS_AS __attribute__((address_space(3)))
#define DI __device__ __forceinline__
#define SBAR() __builtin_amdgcn_sched_barrier(0)
#define PIN(x) asm volatile("" : "+v"(x))
#define MFMA32(a, b, c) __builtin_amdgcn_mfma_f32_32x32x16_bf16(a, b, c, 0, 0, 0)
#define MFMA16(a, b, c) __builtin_amdgcn_mfma_f32_16x16x32_bf16(a, b, c, 0, 0, 0)

constexpr int NN = 10000;
constexpr int NE = 640000;
constexpr int BN = 40;
constexpr int NBK = 250;
constexpr int CE = 2560;
constexpr int NCH = 250;
constexpr float NEG_LOG2E = -1.4426950408889634f;
constexpr float NEG_LN2 = -0.6931471805599453f;

constexpr size_t W_E   = 0;
constexpr size_t W_T   = 4096;
constexpr size_t W_R   = 1u << 20;
constexpr size_t W_B   = (5u << 20) + (512u << 10);
constexpr size_t W_P   = 7u << 20;

DI unsigned win_of(unsigned dl) { return ((dl * 37u) >> 8) + (dl == 34u ? 1u : 0u); }
DI unsigned win_start(unsigned w) { return 7u * w - (w == 5u ? 1u : 0u); }

DI int wave_a(unsigned w) { return (0x987520u >> (4 * w)) & 15; }
DI int wave_b(unsigned w) { return (0xBA4631u >> (4 * w)) & 15; }

DI float bf2f(unsigned v16) { return __uint_as_float(v16 << 16); }
DI unsigned pk_bf16(float lo, float hi) {
    f32x2 x = {lo, hi};
    return __builtin_bit_cast(unsigned, __builtin_convertvector(x, bf16x2));
}
DI float silu_f(float x) {
    return x * __builtin_amdgcn_rcpf(1.0f + __builtin_amdgcn_exp2f(x * NEG_LOG2E));
}
DI unsigned rtime() { unsigned long long t; asm volatile("s_memrealtime %0\n\ts_waitcnt lgkmcnt(0)" : "=s"(t) :: "memory"); return (unsigned)t; }
DI void probe_wait(unsigned my0, unsigned ticks) {
    for (int i = 0; i < 100000; ++i) { if (rtime() - my0 >= ticks) break; __builtin_amdgcn_s_sleep(1); }
}
DI s16x4 tr_read(const char* p) {
    return __builtin_amdgcn_ds_read_tr16_b64_v4i16((LDS_AS s16x4*)p);
}
DI bf16x8 cat8(s16x4 a, s16x4 b) { return __builtin_shufflevector(a, b, 0, 1, 2, 3, 4, 5, 6, 7); }
DI void wait_vm0() { asm volatile("s_waitcnt vmcnt(0)" ::: "memory"); }
DI void wait_lgkm0() { asm volatile("s_waitcnt lgkmcnt(0)" ::: "memory"); }
DI void bar() { asm volatile("s_waitcnt lgkmcnt(0)\n\ts_barrier" ::: "memory"); }

DI void dma1(unsigned ldsaddr, const void* gbase, unsigned voff) {
    unsigned keep;
    asm volatile("s_mov_b32 %0, m0\n\ts_mov_b32 m0, %1\n\ts_nop 0\n\t"
                 "global_load_lds_dwordx4 %2, %3\n\t"
                 "s_mov_b32 m0, %0"
                 : "=&s"(keep) : "s"(ldsaddr), "v"(voff), "s"(gbase) : "memory");
}
DI void dma1_dev(unsigned ldsaddr, const void* gbase, unsigned voff) {
    unsigned keep;
    asm volatile("s_mov_b32 %0, m0\n\ts_mov_b32 m0, %1\n\ts_nop 0\n\t"
                 "global_load_lds_dwordx4 %2, %3 sc1\n\t"
                 "s_mov_b32 m0, %0"
                 : "=&s"(keep) : "s"(ldsaddr), "v"(voff), "s"(gbase) : "memory");
}
DI void dma4_dev(unsigned ldsaddr, const void* gbase, unsigned v0, unsigned v1, unsigned v2, unsigned v3) {
    unsigned keep;
    asm volatile("s_mov_b32 %0, m0\n\ts_mov_b32 m0, %1\n\ts_nop 0\n\t"
                 "global_load_lds_dwordx4 %2, %6 sc1\n\t"
                 "global_load_lds_dwordx4 %3, %6 offset:1024 sc1\n\t"
                 "global_load_lds_dwordx4 %4, %6 offset:2048 sc1\n\t"
                 "global_load_lds_dwordx4 %5, %6 offset:3072 sc1\n\t"
                 "s_mov_b32 m0, %0"
                 : "=&s"(keep) : "s"(ldsaddr), "v"(v0), "v"(v1), "v"(v2), "v"(v3), "s"(gbase) : "memory");
}
template <typename T> DI void st_nt(void* p, T v) { __builtin_nontemporal_store(v, (T*)p); }
DI void st_dev(const char* base, unsigned off, u32x4 v) {
    asm volatile("global_store_dwordx4 %0, %1, %2 sc1\n\ts_nop 1" :: "v"(off), "v"(v), "s"(base) : "memory");
}
DI unsigned div3(unsigned x) { return __umul24(x, 43691u) >> 17; }
DI unsigned mad16(unsigned x, unsigned m, unsigned a) {
    unsigned r;
    asm("v_mad_u32_u16 %0, %1, %2, %3" : "=v"(r) : "v"(x), "s"(m), "v"(a));
    return r;
}
template <typename T> DI T lds_ld(unsigned a) { return *(const LDS_AS T*)(size_t)a; }
template <typename T> DI void lds_st(unsigned a, T v) { *(LDS_AS T*)(size_t)a = v; }
template <int CTRL, int ROWMASK> DI unsigned dpp_add(unsigned x) {
    return x + (unsigned)__builtin_amdgcn_update_dpp(0, (int)x, CTRL, ROWMASK, 0xf, true);
}
DI unsigned scan32(unsigned x) {
    x = dpp_add<0x111, 0xf>(x); x = dpp_add<0x112, 0xf>(x); x = dpp_add<0x114, 0xf>(x); x = dpp_add<0x118, 0xf>(x);
    x = dpp_add<0x142, 0xa>(x);
    return x;
}
DI unsigned scan64(unsigned x) { x = scan32(x); x = dpp_add<0x143, 0xc>(x); return x; }
DI int wswz(int k) { return (k & 2) | ((k >> 1) & 4); }
DI bf16x8 wfrag16(const char* img, int s, int ct, int lane) {
    const int g = lane >> 4, q = (lane & 15) >> 2, p = lane & 3;
    const int k0 = 32 * s + 8 * g + q, k1 = k0 + 4, chn = 2 * ct + (p >> 1);
    return cat8(tr_read(img + k0 * 128 + 16 * (chn ^ wswz(k0)) + 8 * (p & 1)),
                tr_read(img + k1 * 128 + 16 * (chn ^ wswz(k1)) + 8 * (p & 1)));
}
DI bf16x8 nfrag16(const char* img, int node, int chunk) {
    return *(const bf16x8*)(img + node * 128 + 16 * (chunk ^ ((node >> 1) & 7)));
}

#define ASTAMP(i)
#define PSTAMP(i)
#define QSTAMP(i)
#define STAMP_DECL
#define STAMP(i)
#define CSTAMP(i)
#define STAMP_OUT(kern, wsp)

constexpr int K2_W = 12, K2_T = 64 * K2_W;
constexpr int RCAP = 3072;
constexpr int L_SLOT = 0;
constexpr int L_STAGE = 49152;
constexpr int RALL = RCAP + 128;
constexpr int L_R32 = 73728;
constexpr int L_R8 = L_R32 + RALL * 4;
constexpr int L_AIMG = L_R8 + RALL;
constexpr int L_HIMG = L_AIMG + 6144;
constexpr int L_WH = L_HIMG + 6144;
constexpr int L_PD = L_WH + 16384;
constexpr int L_OH = L_PD + 768;
constexpr int L_WX = L_OH + 256;
constexpr int L_BINS = L_WX + 256;
constexpr int L_OFFS = L_BINS + 896;
constexpr int L_WIN = L_OFFS + 896;
constexpr int L_WF = L_WIN + 64;
constexpr int L_PB = L_WF + 64;
constexpr int L_END = L_PB + K2_W * 1024;
constexpr int A_CNT = L_SLOT, A_OFF = A_CNT + 1024, A_TOT = A_OFF + 1024, A_R = A_TOT + 16, A_W = A_R + 16384, A_O = A_W + 16384;
static_assert(A_O + BN * 128 <= L_STAGE, "phase A scratch");
constexpr unsigned MAPCAP = 128;
constexpr int PROW = 272, PPOS = 8 * PROW;

DI bf16x8 as_frag(unsigned a, unsigned b, unsigned c, unsigned d) {
    u32x4 v = {a, b, c, d};
    return __builtin_bit_cast(bf16x8, v);
}
DI float ex2(float x) { return __builtin_amdgcn_exp2f(x); }
DI float rcp(float x) { return __builtin_amdgcn_rcpf(x); }

template <class H1, class H2, class H3>
DI void half_tile(const f32x16& xc, f32x16& xn, const bf16x8& aA, const bf16x8& aB, const bf16x8& ef, const bf16x8& eB,
                  const bf16x8& i0, const bf16x8& i1, f32x16& zp, u32x4& pkp, const bf16x8& wp, f32x16& zc,
                  const bf16x8& wc, H1&& hook1, H2&& hook2, H3&& hook3) {
    f32x16 zero;
#pragma unroll
    for (int i = 0; i < 16; ++i) zero[i] = 0.f;
    f32x2 e0, e1, e2, e3;
    u32x4 pk;
#define EXP2(e, i) do { e[0] = ex2(xc[i]); PIN(e[0]); } while (0)
#define EXP2H(e, i) do { e[1] = ex2(xc[i]); PIN(e[1]); } while (0)
#define RCP2(e) do { e[0] = rcp(e[0]); PIN(e[0]); } while (0)
#define RCP2H(e) do { e[1] = rcp(e[1]); PIN(e[1]); } while (0)
#define PKMODE 0
#define ADD1(e) do { e = e + 1.0f; PIN(e); } while (0)
#define MULX(e, i) do { e = e * f32x2{xc[i], xc[i + 1]}; PIN(e); } while (0)
#define CVT(d, e) do { d = __builtin_bit_cast(unsigned, __builtin_convertvector(e, bf16x2)); PIN(d); } while (0)
    xn = MFMA32(aA, i0, zero); SBAR();
    EXP2(e0, 0); EXP2H(e0, 1); EXP2(e1, 2); SBAR();
    zp = MFMA32(__builtin_bit_cast(bf16x8, pkp), wp, zp); SBAR();
    EXP2H(e1, 3); EXP2(e2, 4); EXP2H(e2, 5); EXP2(e3, 6); EXP2H(e3, 7); SBAR();
    ADD1(e0); ADD1(e1); ADD1(e2); ADD1(e3); SBAR();
    hook1(); SBAR();
    xn = MFMA32(aB, i1, xn); SBAR();
    RCP2(e0); RCP2H(e0); RCP2(e1); SBAR();
    xn = MFMA32(ef, eB, xn); SBAR();
    RCP2H(e1); RCP2(e2); RCP2H(e2); RCP2(e3); RCP2H(e3); SBAR();
    MULX(e0, 0); MULX(e1, 2); MULX(e2, 4); MULX(e3, 6); SBAR();
    CVT(pk[0], e0); CVT(pk[1], e1); CVT(pk[2], e2); CVT(pk[3], e3); SBAR();
    EXP2(e0, 8); EXP2H(e0, 9); SBAR();
    zc = MFMA32(__builtin_bit_cast(bf16x8, pk), wc, zc); SBAR();
    EXP2(e1, 10); EXP2H(e1, 11); EXP2(e2, 12); EXP2H(e2, 13); EXP2(e3, 14); EXP2H(e3, 15); SBAR();
    ADD1(e0); ADD1(e1); ADD1(e2); ADD1(e3); SBAR();
    hook2(); SBAR();
    RCP2(e0); RCP2H(e0); RCP2(e1); RCP2H(e1); RCP2(e2); RCP2H(e2); RCP2(e3); RCP2H(e3); SBAR();
    hook3(); SBAR();
    MULX(e0, 8); MULX(e1, 10); MULX(e2, 12); MULX(e3, 14); SBAR();
    CVT(pkp[0], e0); CVT(pkp[1], e1); CVT(pkp[2], e2); CVT(pkp[3], e3); SBAR();
#undef EXP2
#undef EXP2H
#undef RCP2
#undef RCP2H
#undef ADD1
#undef MULX
#undef CVT
}

__global__ __launch_bounds__(K2_T) void k_egnn(const int* __restrict__ src, const int* __restrict__ dst,
                                               const uint16_t* __restrict__ ea, const uint16_t* __restrict__ h,
                                               const uint16_t* __restrict__ pos, const uint16_t* __restrict__ we,
                                               const uint16_t* __restrict__ wx, const uint16_t* __restrict__ wh,
                                               char* __restrict__ ws, uint16_t* __restrict__ out) {
    extern __shared__ __attribute__((aligned(16))) char sm[];
    const int tid = threadIdx.x, lane = tid & 63;
    const int wv = __builtin_amdgcn_readfirstlane(tid >> 6);
    const int xc = blockIdx.x & 7;
    const int b = 31 * xc + (xc < 2 ? xc : 2) + (blockIdx.x >> 3);
    const unsigned smb = (unsigned)(size_t)(LDS_AS char*)sm;
    asm volatile("" :: "s"(src), "s"(dst), "s"(ea), "s"(h), "s"(pos), "s"(we), "s"(wx), "s"(wh), "s"(ws), "s"(out));
    const char* Rec = ws + W_R;
    const char* Btab = ws + W_B;
    const char* Ptab = ws + W_P;
    const unsigned my0 = rtime();
    STAMP_DECL
    STAMP(0);

    u32x2 ep;
    asm volatile("global_load_dwordx2 %0, %1, %2" : "=v"(ep) : "v"(0u), "s"(ws + W_E) : "memory");
    u32x4 d4 = {0, 0, 0, 0}, s4 = {0, 0, 0, 0};
    u32x2 a4 = {0, 0};
    unsigned sm0 = 0, sm1 = 0;
    const int r8l = lane >> 3, c8 = lane & 7;
    const int gnode = b * BN + (lane < BN ? lane : 0);
    {
        auto piece = [&](int p) {
            if (p < 5) dma1(smb + L_HIMG + p * 1024, h, (b * BN + 8 * p + r8l) * 128 + ((c8 ^ ((4 * p + (lane >> 4)) & 7)) * 16));
            else if (p < 21) dma1(smb + A_W + (p - 5) * 1024, we, (8 * (p - 5) + r8l) * 128 + ((c8 ^ ((r8l & 2) | (((p - 5) & 1) << 2))) * 16));
            else if (lane < 16) dma1(smb + L_AIMG + BN * 128, we, 128 * 128 + lane * 16);
        };
        if (wv < 10) {
            const int e0 = b * CE + 4 * tid;
            asm volatile("global_load_dwordx4 %0, %3, %4\n\tglobal_load_dwordx4 %1, %3, %5\n\tglobal_load_dwordx2 %2, %6, %7"
                         : "=&v"(d4), "=&v"(s4), "=&v"(a4)
                         : "v"(e0 * 4), "s"(dst), "s"(src), "v"(e0 * 2), "s"(ea) : "memory");
            piece(wv);
        } else {
            if (wv == 10)
                asm volatile("global_load_dword %0, %2, %3\n\tglobal_load_ushort %1, %2, %3 offset:4"
                             : "=&v"(sm0), "=&v"(sm1) : "v"(gnode * 6), "s"(pos) : "memory");
            else
                asm volatile("global_load_ushort %0, %1, %2" : "=v"(sm0) : "v"(lane * 2), "s"(wx) : "memory");
#pragma clang loop unroll(disable)
            for (int p = 10 + 6 * (wv - 10); p < 16 + 6 * (wv - 10); ++p) piece(p);
        }
    }
    {
        const u32x4 z = {0, 0, 0, 0};
        lds_st(smb + L_STAGE + wv * 2048 + lane * 16, z);
        lds_st(smb + L_STAGE + wv * 2048 + 1024 + lane * 16, z);
        const u32x4 n32 = {NN, NN, NN, NN}, n8 = {0x08080808u, 0x08080808u, 0x08080808u, 0x08080808u};
        lds_st(smb + L_R32 + tid * 16, n32);
        if (wv == 0) {
            lds_st(smb + A_CNT + lane * 16, z);
            if (lane < RALL / 4 - K2_T) lds_st(smb + L_R32 + (K2_T + lane) * 16, n32);
        } else if (wv < 5) {
            if (tid - 64 < RALL / 16) lds_st(smb + L_R8 + (tid - 64) * 16, n8);
        } else if (wv == 5) {
            if (lane < 56) lds_st(smb + L_BINS + lane * 16, z);
        } else if (wv == 6) {
            if (lane < 48) lds_st(smb + L_AIMG + (BN + 2) * 128 + lane * 16, z);
        } else if (wv == 7) {
            const int n = lane >> 2, j = lane & 3;
            if (lane < 36) lds_st(smb + L_OH + lane * 4, (n < 8 && (n >> 1) == j) ? (0x3F80u << (16 * (n & 1))) : 0u);
        }
    }
    bar();
    ASTAMP(1);
    asm volatile("s_waitcnt vmcnt(0)" : "+v"(ep), "+v"(d4), "+v"(s4), "+v"(a4), "+v"(sm0), "+v"(sm1) :: "memory");
    ASTAMP(2);
    const unsigned e_lo = __builtin_amdgcn_readfirstlane(ep[0]), e_hi = __builtin_amdgcn_readfirstlane(ep[1]);
    const unsigned n_lo = e_lo + 1u, n_hi = e_hi + (n_lo == 0u ? 1u : 0u);
    const unsigned tag_w1 = (n_lo >> 8) | (n_hi << 24), tag_w0 = n_lo << 24;

    unsigned bk[4], ark[4], code = 0;
    if (wv < 10) {
#pragma unroll
        for (int i = 0; i < 4; ++i) {
            bk[i] = __umul24(d4[i], 52429u) >> 21;
            ark[i] = atomicAdd((unsigned*)(sm + A_CNT) + bk[i], 1u);
        }
#pragma unroll
        for (int i = 0; i < 4; ++i) {
            const unsigned dl = d4[i] - bk[i] * BN, w = win_of(dl);
            code |= (w * 8 + dl - win_start(w)) << (8 * i);
        }
    } else if (wv == 10) {
        const f32x4 p = {bf2f(sm0 & 0xffffu), __uint_as_float(sm0 & 0xffff0000u), bf2f(sm1), 0.f};
        if (lane < 48) lds_st(smb + L_PD + lane * 16, lane < BN ? p : f32x4{0.f, 0.f, 0.f, 0.f});
        if (lane < BN) st_dev(Ptab, gnode * 16, __builtin_bit_cast(u32x4, p));
    } else {
        lds_st(smb + L_WX + lane * 4, bf2f(sm0) * NEG_LN2);
        if (b == 0) {
            const u32x4 z = {0, 0, 0, 0};
            st_dev(Ptab, NN * 16 + lane * 16, z);
            if (lane < 8) st_dev(Btab, NN * 128 + lane * 16, z);
        }
    }
    ASTAMP(3);
    bar();
    ASTAMP(4);

    u32x4 t_lo = {0, 0, 0, 0};
    if (wv == 11) {
        const u32x4 cn = lds_ld<u32x4>(smb + A_CNT + lane * 16);
        const u32x4 tr = {div3(cn[0] + 2), div3(cn[1] + 2), div3(cn[2] + 2), div3(cn[3] + 2)};
        const unsigned tot = tr[0] + tr[1] + tr[2] + tr[3];
        const unsigned in = scan64(tot), ex = in - tot;
        const u32x4 of = {ex, ex + tr[0], ex + tr[0] + tr[1], ex + tr[0] + tr[1] + tr[2]};
        lds_st(smb + A_OFF + lane * 16, of);
        if (lane == 63) lds_st(smb + A_TOT, in);
#pragma unroll
        for (int i = 0; i < 4; ++i) t_lo[i] = tag_w0 | (cn[i] << 12) | of[i];
    } else if (wv < 8) {
        const int ct = wv & 3, g = lane >> 4;
        const char* wimg = sm + A_W + (wv >> 2) * 8192;
        const bf16x8 a0 = wfrag16(wimg, 0, ct, lane), a1 = wfrag16(wimg, 1, ct, lane);
#pragma unroll
        for (int nt = 0; nt < 3; ++nt) {
            const int node = 16 * nt + (lane & 15);
            f32x4 acc = {0.f, 0.f, 0.f, 0.f};
            acc = MFMA16(a0, nfrag16(sm + L_HIMG, node, g), acc);
            acc = MFMA16(a1, nfrag16(sm + L_HIMG, node, 4 + g), acc);
            const u32x2 o = {pk_bf16(acc[0] * NEG_LOG2E, acc[1] * NEG_LOG2E), pk_bf16(acc[2] * NEG_LOG2E, acc[3] * NEG_LOG2E)};
            const unsigned oa = wv < 4 ? L_AIMG + node * 128 + (16 * ct + 4 * g) * 2
                                       : A_O + node * 128 + 16 * ((2 * ct + (g >> 1)) ^ (node & 7)) + 8 * (g & 1);
            if (node < BN) lds_st(smb + oa, o);
        }
    }
    ASTAMP(5);
    bar();
    ASTAMP(6);

    if (wv < 10) {
#pragma unroll
        for (int i = 0; i < 4; ++i) {
            const unsigned q = div3(ark[i]), j = ark[i] - 3 * q;
            const unsigned t = smb + A_R + (lds_ld<unsigned>(smb + A_OFF + bk[i] * 4) + q) * 16;
            const unsigned av = (i & 1) ? (a4[i >> 1] & 0xffff0000u) : (a4[i >> 1] << 16);
            lds_st(t + j * 4, s4[i] | av);
            lds_st(t + 12 + j, (unsigned char)(code >> (8 * i)));
        }
    } else {
#pragma clang loop unroll(disable)
        for (int x = tid - 640; x < BN * 8; x += 128) {
            const int node = x >> 3, ch = (x & 7) ^ (node & 7);
            st_dev(Btab, (b * BN + node) * 128 + ch * 16, lds_ld<u32x4>(smb + A_O + x * 16));
        }
    }
    bar();
    ASTAMP(7);
    {
        const unsigned ntot = lds_ld<unsigned>(smb + A_TOT);
        if ((unsigned)tid < ntot) st_dev(Rec, b * 16384 + tid * 16, lds_ld<u32x4>(smb + A_R + tid * 16));
        if ((unsigned)tid + K2_T < ntot) st_dev(Rec, b * 16384 + (tid + K2_T) * 16, lds_ld<u32x4>(smb + A_R + (tid + K2_T) * 16));
    }
    ASTAMP(8);
    wait_vm0();
    ASTAMP(9);
    bar();
    if (wv == 11) {
        st_dev(ws + W_T, b * 2048 + lane * 32, u32x4{t_lo[0], tag_w1, t_lo[1], tag_w1});
        st_dev(ws + W_T, b * 2048 + lane * 32 + 16, u32x4{t_lo[2], tag_w1, t_lo[3], tag_w1});
    }
    ASTAMP(10);

#pragma clang loop unroll(disable)
    for (int q = wv; q < 16; q += K2_W)
        dma1(smb + L_WH + q * 1024, wh, (8 * q + r8l) * 128 + ((c8 ^ ((r8l & 2) | ((q & 1) << 2))) * 16));
    unsigned tw;
    asm volatile("global_load_dword %0, %1, %2" : "=v"(tw)
                 : "v"(lane == 0 ? b * (BN * 128) : (NN * 128 + b * (BN * 6)) & ~3), "s"(out) : "memory");

    const int l3 = (lane * 171) >> 9, sub = lane - 3 * l3;
    const int cid = 21 * wv + l3;
    const bool cok = lane < 63 && cid < NCH;
    const int cidc = cok ? cid : 0;
    unsigned tval = 0;
    bool lost = true;
#pragma clang loop unroll(disable)
    for (int spin = 0; spin < (1 << 16); ++spin) {
        u32x2 tv;
        asm volatile("global_load_dwordx2 %0, %2, %3 sc1\n\ts_waitcnt vmcnt(0)"
                     : "=&v"(tv), "+v"(tw) : "v"((unsigned)W_T + cidc * 2048 + b * 8), "s"(ws) : "memory");
        tval = tv[0];
        if (__all(!cok || (tv[1] == tag_w1 && (tv[0] >> 24) == (tag_w0 >> 24)))) { lost = false; break; }
        __builtin_amdgcn_s_sleep(2);
    }
    PSTAMP(2);

    const unsigned cnt = (cok && !lost) ? ((tval >> 12) & 0xfffu) : 0u;
    const unsigned ntr = div3(cnt + 2);
    const unsigned incl = scan64(sub == 0 ? ntr : 0u);
    const unsigned mw = __builtin_amdgcn_readlane(incl, 63);
    const bool slow_w = mw > MAPCAP;
    const unsigned map_a = smb + L_SLOT + wv * 4096;
    QSTAMP(1);
    if (!slow_w) {
        unsigned g = (cidc * 1024 + (tval & 0xfffu) + sub) | ((cnt - 3 * sub) << 20);
        unsigned mp = map_a + (incl - ntr + sub) * 4;
#pragma clang loop unroll(disable) vectorize(disable) interleave(disable)
        for (unsigned i = sub; i < ntr; i += 3) { lds_st(mp, g); mp += 12; g += 3 - (9u << 20); }
    }
    PSTAMP(3);
    const unsigned mwe = slow_w ? 0u : mw;
    unsigned left[2];
    u32x4 rec[2];
#pragma unroll
    for (int k = 0; k < 2; ++k) {
        const unsigned m = lds_ld<unsigned>(map_a + (64 * k + lane) * 4);
        const bool ok = (unsigned)(64 * k + lane) < mwe;
        left[k] = ok ? m >> 20 : 0u;
        const unsigned go = ok ? (m & 0xfffffu) * 16 : 0u;
        asm volatile("global_load_dwordx4 %0, %1, %2 sc1" : "=v"(rec[k]) : "v"(go), "s"(Rec) : "memory");
    }
    if (lane == 0) lds_st(smb + L_WF + wv * 4, lost ? 2u : slow_w ? 1u : 0u);
    PSTAMP(4);
    asm volatile("s_waitcnt vmcnt(0)" : "+v"(rec[0]), "+v"(rec[1]) :: "memory");
    PSTAMP(5);
    unsigned bin[6], rk[6];
#pragma unroll
    for (int k = 0; k < 6; ++k) {
        const unsigned w = (rec[k / 3][3] >> (8 * (k % 3) + 3)) & 7u;
        bin[k] = (left[k / 3] > (unsigned)(k % 3) ? w : 6u) * 128 + (lane & 31) * 4;
        rk[k] = atomicAdd((unsigned*)(sm + L_BINS + bin[k]), 1u);
    }
    QSTAMP(3);
    PSTAMP(6);
    bar();
    PSTAMP(7);
    unsigned pf;
    {
        const unsigned part = blockIdx.x >> 3;
        const unsigned off = tid < 626 ? (unsigned)W_B + part * (313 * 128) + tid * 64
                                       : (unsigned)W_P + part * (40 * 128) + (tid < 706 ? tid - 626 : 0) * 64;
        asm volatile("global_load_dword %0, %1, %2 sc1" : "=v"(pf) : "v"(off), "s"(ws) : "memory");
    }

    if (wv == 0) {
        const unsigned* bins = (const unsigned*)(sm + L_BINS);
        const int j = lane & 31, hi = lane >> 5;
        const unsigned ca = bins[(2 * hi) * 32 + j] | (bins[(2 * hi + 1) * 32 + j] << 16);
        const unsigned cb = bins[4 * 32 + j] | (bins[5 * 32 + j] << 16);
        const unsigned wfl = lds_ld<unsigned>(smb + L_WF + (lane < K2_W ? lane : 0) * 4);
        const unsigned ia = scan32(ca), ib = scan32(cb);
        const unsigned t01 = __builtin_amdgcn_readlane(ia, 31), t23 = __builtin_amdgcn_readlane(ia, 63);
        const unsigned t45 = __builtin_amdgcn_readlane(ib, 31);
        const unsigned tot[6] = {t01 & 0xffffu, t01 >> 16, t23 & 0xffffu, t23 >> 16, t45 & 0xffffu, t45 >> 16};
        unsigned st[7];
        st[0] = 0;
#pragma unroll
        for (int w = 0; w < 6; ++w) st[w + 1] = st[w] + ((tot[w] + 31) & ~31u);
        const unsigned xa = ia - ca, xb = ib - cb;
        unsigned* offs = (unsigned*)(sm + L_OFFS);
        offs[(2 * hi) * 32 + j] = (hi ? st[2] : st[0]) + (xa & 0xffffu);
        offs[(2 * hi + 1) * 32 + j] = (hi ? st[3] : st[1]) + (xa >> 16);
        offs[(4 + hi) * 32 + j] = hi ? st[5] + (xb >> 16) : st[4] + (xb & 0xffffu);
        const bool any_slow = __any(wfl != 0), any_lost = __any(wfl == 2);
        unsigned wi = 0;
#pragma unroll
        for (int w = 0; w < 6; ++w) { wi = lane == w ? st[w] : wi; wi = lane == 6 + w ? (st[w + 1] - st[w]) >> 5 : wi; }
        wi = lane == 12 ? (any_lost ? 2u : (any_slow || st[6] > RCAP) ? 1u : 0u) : wi;
        if (lane < 13) ((unsigned*)(sm + L_WIN))[lane] = wi;
    } else if (wv == 2) {
        if (b == 0 && lane == 0) *(u32x2*)(ws + W_E) = u32x2{n_lo, n_hi};
    }
    PSTAMP(8);
    bar();
    QSTAMP(4);
    PSTAMP(9);
    const unsigned mode = __builtin_amdgcn_readfirstlane(((const unsigned*)(sm + L_WIN))[12]);
    if (mode == 2) { asm volatile("s_waitcnt vmcnt(0)" : "+v"(pf) :: "memory"); return; }
    const bool slow = mode != 0;

    const int r = lane & 31, hh = lane >> 5;
    const int win = (int)((0x545432231100ull >> (4 * wv)) & 7);
    const bool second = (0xC5A >> wv) & 1;
    const int wnode = win_start(win);
    char* part = sm + L_SLOT + wv * 4096;

    if (!slow) {
#pragma unroll
        for (int k = 0; k < 6; ++k) {
            const unsigned o = lds_ld<unsigned>(smb + L_OFFS + bin[k]) + rk[k];
            const unsigned p = left[k / 3] > (unsigned)(k % 3) ? o : (unsigned)(RALL - 1);
            lds_st(smb + L_R32 + p * 4, rec[k / 3][k % 3]);
            lds_st(smb + L_R8 + p, (unsigned char)((rec[k / 3][3] >> (8 * (k % 3))) & 7u));
        }
        const unsigned wiv = lds_ld<unsigned>(smb + L_WIN + (lane & 15) * 4);
        int tw6[6];
#pragma unroll
        for (int w = 0; w < 6; ++w) tw6[w] = __builtin_amdgcn_readlane(wiv, 6 + w);
        const int wstart = __builtin_amdgcn_readlane(wiv, win);
        const int l0 = tw6[0] + tw6[3] + tw6[4], l1 = tw6[0] + tw6[2] + tw6[5];
        const int l2 = tw6[1] + tw6[2] + tw6[4], l3s = tw6[1] + tw6[3] + tw6[5];
        const int wtiles = win == 0 ? tw6[0] : win == 1 ? tw6[1] : win == 2 ? tw6[2] : win == 3 ? tw6[3] : win == 4 ? tw6[4] : tw6[5];
        const int la = win == 0 ? l0 : win == 1 ? l2 : win == 2 ? l1 : win == 3 ? l3s : win == 4 ? l0 : l1;
        const int lb = win == 0 ? l1 : win == 1 ? l3s : win == 2 ? l2 : win == 3 ? l0 : win == 4 ? l2 : l3s;
        int first = (4 * wtiles - la + lb + 4) >> 3;
        first = first < 0 ? 0 : first > wtiles ? wtiles : first;
        const int ta = second ? first : 0, tb = second ? wtiles : first;

        const int blk = (lane >> 4) & 1, q = (lane & 15) >> 2, p = lane & 3;
        bf16x8 i0, i1;
#pragma unroll
        for (int j = 0; j < 8; ++j) {
            i0[j] = (r == 8 * hh + j) ? (short)0x3F80 : (short)0;
            i1[j] = (r == 16 + 8 * hh + j) ? (short)0x3F80 : (short)0;
        }
        const int rho = 8 * (r & 3) + (r >> 2);
        const int fsw = (r & 3) ^ (((r >> 3) & 1) << 2);
        unsigned aad[4];
#pragma unroll
        for (int s = 0; s < 4; ++s) aad[s] = smb + L_SLOT + wv * 4096 + rho * 128 + 16 * ((2 * s + hh) ^ fsw);
        unsigned kk[4];
#pragma unroll
        for (int i = 0; i < 4; ++i)
            kk[i] = 16 * ((lane & 7) ^ i ^ (((lane >> 4) & 1) << 2)) + (3072 - 1024 * i);
        const char* gB = Btab - 3072;
        const unsigned slot_a = smb + L_SLOT + wv * 4096;
        const unsigned pbuf_a = smb + L_PB + wv * 1024;
        const unsigned src_a = smb + L_R32 + (lane >> 3) * 16, rs_a = smb + L_R32 + lane * 4, rn_a = smb + L_R8 + lane;
        const unsigned stage_r = smb + L_STAGE + wv * 2048 + r * 64;
        const char* trb = sm + L_STAGE + wv * 2048 + (4 * hh + q) * 64 + blk * 32 + p * 8;
        const unsigned pdw = smb + L_PD + wnode * 16;
        const unsigned oh_a = smb + L_OH;
        bar();
        QSTAMP(5);
        PSTAMP(10);

        bf16x8 e0, e1;
        {
            const int row = hh ? BN + q : wnode + q;
            const char* ab = sm + L_AIMG + row * 128 + (16 * blk + 4 * p) * 2;
            e0 = cat8(tr_read(ab), tr_read(ab + 512));
            e1 = cat8(tr_read(ab + 64), tr_read(ab + 64 + 512));
        }
        f32x16 z0, z1, xa, xb;
#pragma unroll
        for (int i = 0; i < 16; ++i) { z0[i] = 0.f; z1[i] = 0.f; }
        QSTAMP(6);
        CSTAMP(12);

        if (ta < tb) {
            int base = wstart + 32 * ta;
            const int end = wstart + 32 * tb;
            bf16x8 a0, a1, a2, a3, ef, wf0, wf1;
            unsigned rs, rn, w0, w1, ua, vb;
            unsigned saddr;
            bool valid;
            u32x4 pkp = {0, 0, 0, 0};
#pragma unroll
            for (int j = 0; j < 8; ++j) wf1[j] = 0;

            u32x4 sn, exr;
            f32x4 pd, ps;
            auto read_src = [&](int ri) { sn = lds_ld<u32x4>(src_a + ri * 4); };
            auto issue_rows = [&]() {
                const unsigned v0 = mad16(sn[0], 128, kk[0]), v1 = mad16(sn[1], 128, kk[1]);
                const unsigned v2 = mad16(sn[2], 128, kk[2]), v3 = mad16(sn[3], 128, kk[3]);
                wait_lgkm0();
                dma4_dev(slot_a, gB, v0, v1, v2, v3);
            };
            auto read_pair = [&](int ri) {
                rs = lds_ld<unsigned>(rs_a + ri * 4);
                rn = lds_ld<unsigned char>(rn_a + ri);
            };
            auto issue_pos = [&]() { dma1_dev(pbuf_a, Ptab, mad16(rs, 16, 0u)); };
            auto read_pos = [&]() {
                pd = lds_ld<f32x4>(pdw + rn * 16);
                ps = lds_ld<f32x4>(pbuf_a + lane * 16);
            };
            auto geometry = [&]() {
                const float dx = pd[0] - ps[0], dy = pd[1] - ps[1], dz = pd[2] - ps[2];
                const float d2 = (dx * dx + dy * dy + dz * dz) * NEG_LOG2E;
                w0 = pk_bf16(1.0f, dx);
                w1 = pk_bf16(dy, dz);
                const unsigned ext = pk_bf16(d2, __uint_as_float(rs & 0xffff0000u) * NEG_LOG2E);
                const auto sw = __builtin_amdgcn_permlane32_swap(oh_a + rn * 16, ext, false, false);
                ua = sw[0];
                vb = sw[1];
                saddr = stage_r + rn * 8;
                PIN(saddr); PIN(w0); PIN(w1);
                valid = rn < 8;
            };
            auto read_rows = [&]() {
                a0 = lds_ld<bf16x8>(aad[0]); a1 = lds_ld<bf16x8>(aad[1]);
                a2 = lds_ld<bf16x8>(aad[2]); a3 = lds_ld<bf16x8>(aad[3]);
            };
            auto read_ext = [&](unsigned u) { exr = lds_ld<u32x4>(hh ? oh_a : u); };
            auto fix_ext = [&](unsigned u) {
                u32x4 ex = exr;
                ex[0] = hh ? u : ex[0];
                ef = __builtin_bit_cast(bf16x8, ex);
                PIN(ef);
            };
            auto stage_weights = [&](bool wr) {
                if (wr) lds_st(saddr, u32x2{w0, w1});
                wf0 = cat8(tr_read(trb), tr_read(trb + 512));
                wf1 = cat8(tr_read(trb + 1024), tr_read(trb + 1536));
                if (wr) lds_st(saddr, u32x2{0, 0});
            };
            auto nop = [&]() {};

            read_pair(base);
            read_src(base);
            issue_rows();
            issue_pos();
            if (base + 32 < end) read_src(base + 32);
            PSTAMP(11);
            asm volatile("s_waitcnt vmcnt(0)" : "+v"(pf) :: "memory");
            PSTAMP(12);
            read_pos();
            read_rows();
            geometry();
            read_ext(ua);
            if (base + 32 < end) issue_rows();
            fix_ext(ua);
            {
                f32x16 zero;
#pragma unroll
                for (int i = 0; i < 16; ++i) zero[i] = 0.f;
                xa = MFMA32(a0, i0, zero);
                xa = MFMA32(a1, i1, xa);
                xa = MFMA32(ef, e0, xa);
            }
            SBAR();
            PSTAMP(13);
            auto set_prio = [&](int rem) {
                if (rem >= 192) __builtin_amdgcn_s_setprio(3);
                else if (rem >= 128) __builtin_amdgcn_s_setprio(2);
                else if (rem >= 64) __builtin_amdgcn_s_setprio(1);
                else __builtin_amdgcn_s_setprio(0);
            };
            while (true) {
                set_prio(end - base);
                half_tile(xa, xb, a2, a3, ef, e1, i0, i1, z1, pkp, wf1, z0, wf0,
                          [&]() { stage_weights(valid && hh == 0); read_src(base + 64); read_pair(base + 64); },
                          [&]() { wait_vm0(); read_rows(); read_ext(vb); },
                          nop);
                half_tile(xb, xa, a0, a1, ef, e0, i0, i1, z0, pkp, wf1, z1, wf0,
                          [&]() { fix_ext(vb); if (base + 64 < end) issue_rows(); issue_pos(); }, nop, nop);
                if (base + 32 >= end) break;
                half_tile(xa, xb, a2, a3, ef, e1, i0, i1, z1, pkp, wf1, z0, wf0,
                          [&]() { stage_weights(valid && hh == 1); read_src(base + 96); },
                          [&]() { wait_vm0(); read_rows(); read_pos(); },
                          [&]() { geometry(); read_ext(ua); });
                half_tile(xb, xa, a0, a1, ef, e0, i0, i1, z0, pkp, wf1, z1, wf0,
                          [&]() { fix_ext(ua); if (base + 96 < end) issue_rows(); }, nop, nop);
                base += 64;
                if (base >= end) break;
            }
            z1 = MFMA32(__builtin_bit_cast(bf16x8, pkp), wf1, z1);
        }
        asm volatile("s_waitcnt vmcnt(0)" : "+v"(pf) :: "memory");
        CSTAMP(13);
        QSTAMP(7);
        float cs0 = 0.f, cs1 = 0.f;
#pragma unroll
        for (int g = 0; g < 4; ++g) {
            const f32x4 w0 = *(const f32x4*)(sm + L_WX + (8 * g + 4 * hh) * 4);
            const f32x4 w1 = *(const f32x4*)(sm + L_WX + (32 + 8 * g + 4 * hh) * 4);
#pragma unroll
            for (int i = 0; i < 4; ++i) { cs0 += z0[4 * g + i] * w0[i]; cs1 += z1[4 * g + i] * w1[i]; }
        }
        const float csh = cs0 + cs1;
        const auto cw = __builtin_amdgcn_permlane32_swap(__float_as_uint(csh), __float_as_uint(csh), false, false);
        const float cst = __uint_as_float(cw[0]) + __uint_as_float(cw[1]);
        wait_vm0();
        if (hh == 0) ((float*)(part + PPOS))[r] = cst;
        if ((lane & 3) == 0) {
            const int n = r >> 2;
#pragma unroll
            for (int g = 0; g < 4; ++g) {
                *(f32x4*)(part + n * PROW + (8 * g + 4 * hh) * 4) = f32x4{z0[4 * g], z0[4 * g + 1], z0[4 * g + 2], z0[4 * g + 3]};
                *(f32x4*)(part + n * PROW + (32 + 8 * g + 4 * hh) * 4) = f32x4{z1[4 * g], z1[4 * g + 1], z1[4 * g + 2], z1[4 * g + 3]};
            }
        }
    } else {
        __builtin_amdgcn_fence(__ATOMIC_ACQUIRE, "agent");
        asm volatile("s_waitcnt vmcnt(0)" : "+v"(pf) :: "memory");
        {
            const u32x4 z = {0, 0, 0, 0};
            *(u32x4*)(part + lane * 16) = z;
            *(u32x4*)(part + 1024 + lane * 16) = z;
            *(u32x4*)(part + 2048 + lane * 16) = z;
        }
        bar();
        const float wd = bf2f(we[128 * 64 + lane]), wa = bf2f(we[129 * 64 + lane]);
        const float wxl = ((const float*)(sm + L_WX))[lane];
        for (int ci = wv; ci < NCH; ci += K2_W) {
            const unsigned tv = *(const unsigned*)(ws + W_T + ci * 2048 + b * 8);
            const unsigned g0 = ci * 1024 + (tv & 0xfffu), cn = (tv >> 12) & 0xfffu;
            for (unsigned i = 0; i < cn; ++i) {
                const unsigned* tp = (const unsigned*)(Rec + (size_t)(g0 + div3(i)) * 16);
                const unsigned j = i - 3 * div3(i);
                const unsigned rc = tp[j], cd = (tp[3] >> (8 * j)) & 0xffu;
                const unsigned s = rc & 0xffffu, w = cd >> 3, n = cd & 7, dl = win_start(w) + n;
                const f32x4 ps = *(const f32x4*)(Ptab + s * 16);
                const f32x4 pd = *(const f32x4*)(sm + L_PD + dl * 16);
                const float dx = pd[0] - ps[0], dy = pd[1] - ps[1], dz = pd[2] - ps[2];
                const unsigned e = pk_bf16((dx * dx + dy * dy + dz * dz) * NEG_LOG2E, __uint_as_float(rc & 0xffff0000u) * NEG_LOG2E);
                const unsigned dxy = pk_bf16(dx, dy), dz0 = pk_bf16(dz, 0.f);
                float y = bf2f(((const uint16_t*)(sm + L_AIMG))[dl * 64 + lane]) + bf2f(((const uint16_t*)Btab)[s * 64 + lane]);
                y += bf2f(e & 0xffffu) * wd + bf2f(e >> 16) * wa;
                const float m = bf2f(pk_bf16(y * rcp(1.0f + ex2(y)), 0.f) & 0xffffu);
                char* tgt = sm + L_SLOT + wave_a(w) * 4096;
                atomicAdd((float*)(tgt + n * PROW) + lane, m);
                float t = m * wxl;
                for (int o = 32; o > 0; o >>= 1) t += __shfl_xor(t, o);
                if (lane < 3) {
                    const float dv = lane == 0 ? bf2f(dxy & 0xffffu) : lane == 1 ? bf2f(dxy >> 16) : bf2f(dz0 & 0xffffu);
                    atomicAdd((float*)(tgt + PPOS) + 4 * n + 1 + lane, dv * t);
                }
            }
        }
    }
    const int ct = wv & 3, g = lane >> 4, node = 16 * (wv >> 2) + (lane & 15), nodec = node < BN ? node : BN - 1;
    bf16x8 wfr[4];
#pragma unroll
    for (int s = 0; s < 4; ++s) wfr[s] = wfrag16(sm + L_WH, s, ct, lane);
    f32x4 acc = {0.f, 0.f, 0.f, 0.f};
    acc = MFMA16(wfr[0], nfrag16(sm + L_HIMG, nodec, g), acc);
    acc = MFMA16(wfr[1], nfrag16(sm + L_HIMG, nodec, 4 + g), acc);
    const u32x2 hv = *(const u32x2*)(sm + L_HIMG + nodec * 128 + 16 * ((2 * ct + (g >> 1)) ^ ((nodec >> 1) & 7)) + 8 * (g & 1));
    const unsigned nwin = win_of(nodec);
    const char* pb = sm + L_SLOT + wave_a(nwin) * 4096 + (nodec - win_start(nwin)) * PROW + g * 32;
    const int pd2 = (wave_b(nwin) - wave_a(nwin)) * 4096;
    QSTAMP(8);
    bar();
    QSTAMP(9);
#pragma unroll
    for (int s = 0; s < 2; ++s) {
        const f32x4 m0 = (*(const f32x4*)(pb + s * 128) + *(const f32x4*)(pb + s * 128 + pd2)) * NEG_LN2;
        const f32x4 m1 = (*(const f32x4*)(pb + s * 128 + 16) + *(const f32x4*)(pb + s * 128 + 16 + pd2)) * NEG_LN2;
        acc = MFMA16(wfr[2 + s], as_frag(pk_bf16(m0[0], m0[1]), pk_bf16(m0[2], m0[3]), pk_bf16(m1[0], m1[1]), pk_bf16(m1[2], m1[3])), acc);
    }
    if (wv >= 10) {
        const int x = (wv - 10) * 64 + lane;
        if (x < 3 * BN) {
            const int nl = (x * 171) >> 9, cc = x - 3 * nl, w = win_of(nl), n = nl - win_start(w);
            const char* pp = sm + L_SLOT + PPOS + (4 * n + 1 + cc) * 4;
            const float sum = *(const float*)(pp + wave_a(w) * 4096) + *(const float*)(pp + wave_b(w) * 4096);
            const float p0 = ((const float*)(sm + L_PD))[nl * 4 + cc];
            out[NN * 64 + (b * BN + nl) * 3 + cc] = (uint16_t)(pk_bf16(p0 + sum, 0.f) & 0xffffu);
        }
    }
    QSTAMP(10);
    if (node < BN) {
        const float o0 = bf2f(hv[0] & 0xffffu) + silu_f(acc[0]), o1 = bf2f(hv[0] >> 16) + silu_f(acc[1]);
        const float o2 = bf2f(hv[1] & 0xffffu) + silu_f(acc[2]), o3 = bf2f(hv[1] >> 16) + silu_f(acc[3]);
        st_nt(out + (b * BN + node) * 64 + 16 * ct + 4 * g, u32x2{pk_bf16(o0, o1), pk_bf16(o2, o3)});
    }
    QSTAMP(11);
    STAMP_OUT(1, ws);
}

}

extern "C" void kernel_launch(void* const* d_in, const int* in_sizes, int n_in, void* d_out, int out_size, void* d_ws,
                              size_t ws_size, hipStream_t stream) {
    using namespace egnn;
    const uint16_t* h = (const uint16_t*)d_in[0];
    const uint16_t* pos = (const uint16_t*)d_in[1];
    const uint16_t* ea = (const uint16_t*)d_in[2];
    const int* src = (const int*)d_in[3];
    const int* dst = (const int*)d_in[4];
    const uint16_t* we = (const uint16_t*)d_in[5];
    const uint16_t* wx = (const uint16_t*)d_in[6];
    const uint16_t* wh = (const uint16_t*)d_in[7];
    (void)hipFuncSetAttribute((const void*)k_egnn, hipFuncAttributeMaxDynamicSharedMemorySize, L_END);
    k_egnn<<<NBK, K2_T, L_END, stream>>>(src, dst, ea, h, pos, we, wx, wh, (char*)d_ws, (uint16_t*)d_out);
}
```

```cpp
#include <hip/hip_runtime.h>
#include <stdint.h>

namespace egnn {

typedef short    bf16x8 __attribute__((ext_vector_type(8)));
typedef short    s16x4  __attribute__((ext_vector_type(4)));
typedef float    f32x16 __attribute__((ext_vector_type(16)));
typedef float    f32x4  __attribute__((ext_vector_type(4)));
typedef float    f32x2  __attribute__((ext_vector_type(2)));
typedef __bf16   bf16x2 __attribute__((ext_vector_type(2)));
typedef unsigned u32x4  __attribute__((ext_vector_type(4)));
typedef unsigned u32x2  __attribute__((ext_vector_type(2)));
#define LDS_AS __attribute__((address_space(3)))
#define DI __device__ __forceinline__
#define SBAR() __builtin_amdgcn_sched_barrier(0)
#define PIN(x) asm volatile("" : "+v"(x))
#define MFMA32(a, b, c) __builtin_amdgcn_mfma_f32_32x32x16_bf16(a, b, c, 0, 0, 0)
#define MFMA16(a, b, c) __builtin_amdgcn_mfma_f32_16x16x32_bf16(a, b, c, 0, 0, 0)

constexpr int NN = 10000;
constexpr int NE = 640000;
constexpr int BN = 40;
constexpr int NBK = 250;
constexpr int CE = 2560;
constexpr int NCH = 250;
constexpr float NEG_LOG2E = -1.4426950408889634f;
constexpr float NEG_LN2 = -0.6931471805599453f;

constexpr size_t W_E   = 0;
constexpr size_t W_T   = 4096;
constexpr size_t W_R   = 1u << 20;
constexpr size_t W_B   = (5u << 20) + (512u << 10);
constexpr size_t W_P   = 7u << 20;

DI unsigned win_of(unsigned dl) { return ((dl * 37u) >> 8) + (dl == 34u ? 1u : 0u); }
DI unsigned win_start(unsigned w) { return 7u * w - (w == 5u ? 1u : 0u); }

DI int wave_a(unsigned w) { return (0x987520u >> (4 * w)) & 15; }
DI int wave_b(unsigned w) { return (0xBA4631u >> (4 * w)) & 15; }

DI float bf2f(unsigned v16) { return __uint_as_float(v16 << 16); }
DI unsigned pk_bf16(float lo, float hi) {
    f32x2 x = {lo, hi};
    return __builtin_bit_cast(unsigned, __builtin_convertvector(x, bf16x2));
}
DI float silu_f(float x) {
    return x * __builtin_amdgcn_rcpf(1.0f + __builtin_amdgcn_exp2f(x * NEG_LOG2E));
}
DI s16x4 tr_read(const char* p) {
    return __builtin_amdgcn_ds_read_tr16_b64_v4i16((LDS_AS s16x4*)p);
}
DI s16x4 tr_read_a(unsigned a) { return __builtin_amdgcn_ds_read_tr16_b64_v4i16((LDS_AS s16x4*)(size_t)a); }
DI bf16x8 cat8(s16x4 a, s16x4 b) { return __builtin_shufflevector(a, b, 0, 1, 2, 3, 4, 5, 6, 7); }
DI void wait_vm0() { asm volatile("s_waitcnt vmcnt(0)" ::: "memory"); }
DI void wait_lgkm0() { asm volatile("s_waitcnt lgkmcnt(0)" ::: "memory"); }
DI void bar() { asm volatile("s_waitcnt lgkmcnt(0)\n\ts_barrier" ::: "memory"); }

DI void dma1(unsigned ldsaddr, const void* gbase, unsigned voff) {
    unsigned keep;
    asm volatile("s_mov_b32 %0, m0\n\ts_mov_b32 m0, %1\n\ts_nop 0\n\t"
                 "global_load_lds_dwordx4 %2, %3\n\t"
                 "s_mov_b32 m0, %0"
                 : "=&s"(keep) : "s"(ldsaddr), "v"(voff), "s"(gbase) : "memory");
}
DI void dma1_dev(unsigned ldsaddr, const void* gbase, unsigned voff) {
    unsigned keep;
    asm volatile("s_mov_b32 %0, m0\n\ts_mov_b32 m0, %1\n\ts_nop 0\n\t"
                 "global_load_lds_dwordx4 %2, %3 sc1\n\t"
                 "s_mov_b32 m0, %0"
                 : "=&s"(keep) : "s"(ldsaddr), "v"(voff), "s"(gbase) : "memory");
}
DI void dma4_dev(unsigned ldsaddr, const void* gbase, unsigned v0, unsigned v1, unsigned v2, unsigned v3) {
    unsigned keep;
    asm volatile("s_mov_b32 %0, m0\n\ts_mov_b32 m0, %1\n\ts_nop 0\n\t"
                 "global_load_lds_dwordx4 %2, %6 sc1\n\t"
                 "global_load_lds_dwordx4 %3, %6 offset:1024 sc1\n\t"
                 "global_load_lds_dwordx4 %4, %6 offset:2048 sc1\n\t"
                 "global_load_lds_dwordx4 %5, %6 offset:3072 sc1\n\t"
                 "s_mov_b32 m0, %0"
                 : "=&s"(keep) : "s"(ldsaddr), "v"(v0), "v"(v1), "v"(v2), "v"(v3), "s"(gbase) : "memory");
}
template <typename T> DI void st_nt(void* p, T v) { __builtin_nontemporal_store(v, (T*)p); }
DI void st_dev(const char* base, unsigned off, u32x4 v) {
    asm volatile("global_store_dwordx4 %0, %1, %2 sc1\n\ts_nop 1" :: "v"(off), "v"(v), "s"(base) : "memory");
}
DI unsigned div3(unsigned x) { return __umul24(x, 43691u) >> 17; }
DI unsigned mad16(unsigned x, unsigned m, unsigned a) {
    unsigned r;
    asm("v_mad_u32_u16 %0, %1, %2, %3" : "=v"(r) : "v"(x), "s"(m), "v"(a));
    return r;
}
template <typename T> DI T lds_ld(unsigned a) { return *(const LDS_AS T*)(size_t)a; }
template <typename T> DI void lds_st(unsigned a, T v) { *(LDS_AS T*)(size_t)a = v; }
template <int CTRL, int ROWMASK> DI unsigned dpp_add(unsigned x) {
    return x + (unsigned)__builtin_amdgcn_update_dpp(0, (int)x, CTRL, ROWMASK, 0xf, true);
}
DI unsigned scan32(unsigned x) {
    x = dpp_add<0x111, 0xf>(x); x = dpp_add<0x112, 0xf>(x); x = dpp_add<0x114, 0xf>(x); x = dpp_add<0x118, 0xf>(x);
    x = dpp_add<0x142, 0xa>(x);
    return x;
}
DI unsigned scan64(unsigned x) { x = scan32(x); x = dpp_add<0x143, 0xc>(x); return x; }
DI int wswz(int k) { return (k & 2) | ((k >> 1) & 4); }
DI bf16x8 wfrag16(const char* img, int s, int ct, int lane) {
    const int g = lane >> 4, q = (lane & 15) >> 2, p = lane & 3;
    const int k0 = 32 * s + 8 * g + q, k1 = k0 + 4, chn = 2 * ct + (p >> 1);
    return cat8(tr_read(img + k0 * 128 + 16 * (chn ^ wswz(k0)) + 8 * (p & 1)),
                tr_read(img + k1 * 128 + 16 * (chn ^ wswz(k1)) + 8 * (p & 1)));
}
DI bf16x8 nfrag16(const char* img, int node, int chunk) {
    return *(const bf16x8*)(img + node * 128 + 16 * (chunk ^ ((node >> 1) & 7)));
}

#define ASTAMP(i)
#define PSTAMP(i)
#define QSTAMP(i)
#define STAMP_DECL
#define STAMP(i)
#define CSTAMP(i)
#define STAMP_OUT(kern, wsp)

constexpr int K2_W = 12, K2_T = 64 * K2_W;
constexpr int WCAP = 576;
constexpr int RCAP = 6 * WCAP;
constexpr int L_SLOT = 0;
constexpr int L_STAGE = 49152;
constexpr int RALL = RCAP + 128;
constexpr int L_R32 = 73728;
constexpr int L_R8 = L_R32 + RALL * 4;
constexpr int L_AIMG = L_R8 + RALL;
constexpr int L_HIMG = L_AIMG + 6144;
constexpr int L_WH = L_HIMG + 6144;
constexpr int L_PD = L_WH + 16384;
constexpr int L_OH = L_PD + 768;
constexpr int L_WX = L_OH + 256;
constexpr int L_BINS = L_WX + 256;
constexpr int L_OFFS = L_BINS + 896;
constexpr int L_WIN = L_OFFS + 896;
constexpr int L_WF = L_WIN + 64;
constexpr int L_PB = L_WF + 64;
constexpr int L_END = L_PB + K2_W * 1024;
constexpr int A_CNT = L_SLOT, A_OFF = A_CNT + 1024, A_TOT = A_OFF + 1024, A_R = A_TOT + 16, A_W = A_R + 16384, A_O = A_W + 16384;
static_assert(A_O + BN * 128 <= L_STAGE, "phase A scratch");
constexpr unsigned MAPCAP = 128;
constexpr int PROW = 272, PPOS = 8 * PROW;

DI bf16x8 as_frag(unsigned a, unsigned b, unsigned c, unsigned d) {
    u32x4 v = {a, b, c, d};
    return __builtin_bit_cast(bf16x8, v);
}
DI float ex2(float x) { return __builtin_amdgcn_exp2f(x); }
DI float rcp(float x) { return __builtin_amdgcn_rcpf(x); }

template <class H1, class H2, class H3>
DI void half_tile(const f32x16& xc, f32x16& xn, const bf16x8& aA, const bf16x8& aB, const bf16x8& ef, const bf16x8& eB,
                  const bf16x8& i0, const bf16x8& i1, f32x16& zp, u32x4& pkp, const bf16x8& wp, f32x16& zc,
                  const bf16x8& wc, H1&& hook1, H2&& hook2, H3&& hook3) {
    f32x16 zero;
#pragma unroll
    for (int i = 0; i < 16; ++i) zero[i] = 0.f;
    f32x2 e0, e1, e2, e3;
    u32x4 pk;
#define EXP2(e, i) do { e[0] = ex2(xc[i]); PIN(e[0]); } while (0)
#define EXP2H(e, i) do { e[1] = ex2(xc[i]); PIN(e[1]); } while (0)
#define RCP2(e) do { e[0] = rcp(e[0]); PIN(e[0]); } while (0)
#define RCP2H(e) do { e[1] = rcp(e[1]); PIN(e[1]); } while (0)
#define PKMODE 0
#define ADD1(e) do { e = e + 1.0f; PIN(e); } while (0)
#define MULX(e, i) do { e = e * f32x2{xc[i], xc[i + 1]}; PIN(e); } while (0)
#define CVT(d, e) do { d = __builtin_bit_cast(unsigned, __builtin_convertvector(e, bf16x2)); PIN(d); } while (0)
    xn = MFMA32(aA, i0, zero); SBAR();
    EXP2(e0, 0); EXP2H(e0, 1); EXP2(e1, 2); SBAR();
    zp = MFMA32(__builtin_bit_cast(bf16x8, pkp), wp, zp); SBAR();
    EXP2H(e1, 3); EXP2(e2, 4); EXP2H(e2, 5); EXP2(e3, 6); EXP2H(e3, 7); SBAR();
    ADD1(e0); ADD1(e1); ADD1(e2); ADD1(e3); SBAR();
    hook1(); SBAR();
    xn = MFMA32(aB, i1, xn); SBAR();
    RCP2(e0); RCP2H(e0); RCP2(e1); SBAR();
    xn = MFMA32(ef, eB, xn); SBAR();
    RCP2H(e1); RCP2(e2); RCP2H(e2); RCP2(e3); RCP2H(e3); SBAR();
    MULX(e0, 0); MULX(e1, 2); MULX(e2, 4); MULX(e3, 6); SBAR();
    CVT(pk[0], e0); CVT(pk[1], e1); CVT(pk[2], e2); CVT(pk[3], e3); SBAR();
    EXP2(e0, 8); EXP2H(e0, 9); SBAR();
    zc = MFMA32(__builtin_bit_cast(bf16x8, pk), wc, zc); SBAR();
    EXP2(e1, 10); EXP2H(e1, 11); EXP2(e2, 12); EXP2H(e2, 13); EXP2(e3, 14); EXP2H(e3, 15); SBAR();
    ADD1(e0); ADD1(e1); ADD1(e2); ADD1(e3); SBAR();
    hook2(); SBAR();
    RCP2(e0); RCP2H(e0); RCP2(e1); RCP2H(e1); RCP2(e2); RCP2H(e2); RCP2(e3); RCP2H(e3); SBAR();
    hook3(); SBAR();
    MULX(e0, 8); MULX(e1, 10); MULX(e2, 12); MULX(e3, 14); SBAR();
    CVT(pkp[0], e0); CVT(pkp[1], e1); CVT(pkp[2], e2); CVT(pkp[3], e3); SBAR();
#undef EXP2
#undef EXP2H
#undef RCP2
#undef RCP2H
#undef ADD1
#undef MULX
#undef CVT
}

__global__ __launch_bounds__(K2_T) void k_egnn(const int* __restrict__ src, const int* __restrict__ dst,
                                               const uint16_t* __restrict__ ea, const uint16_t* __restrict__ h,
                                               const uint16_t* __restrict__ pos, const uint16_t* __restrict__ we,
                                               const uint16_t* __restrict__ wx, const uint16_t* __restrict__ wh,
                                               char* __restrict__ ws, uint16_t* __restrict__ out) {
    extern __shared__ __attribute__((aligned(16))) char sm[];
    const int tid = threadIdx.x, lane = tid & 63;
    const int wv = __builtin_amdgcn_readfirstlane(tid >> 6);
    const int xc = blockIdx.x & 7;
    const int b = 31 * xc + (xc < 2 ? xc : 2) + (blockIdx.x >> 3);
    const unsigned smb = (unsigned)(size_t)(LDS_AS char*)sm;
    asm volatile("" :: "s"(src), "s"(dst), "s"(ea), "s"(h), "s"(pos), "s"(we), "s"(wx), "s"(wh), "s"(ws), "s"(out));
    const char* Rec = ws + W_R;
    const char* Btab = ws + W_B;
    const char* Ptab = ws + W_P;
    STAMP_DECL
    STAMP(0);

    u32x2 ep;
    asm volatile("global_load_dwordx2 %0, %1, %2" : "=v"(ep) : "v"(0u), "s"(ws + W_E) : "memory");
    u32x4 d4 = {0, 0, 0, 0}, s4 = {0, 0, 0, 0};
    u32x2 a4 = {0, 0};
    unsigned sm0 = 0, sm1 = 0;
    const int r8l = lane >> 3, c8 = lane & 7;
    const int gnode = b * BN + (lane < BN ? lane : 0);
    if (wv < 10) {
        const int e0 = b * CE + 4 * tid;
        asm volatile("global_load_dwordx4 %0, %3, %4\n\tglobal_load_dwordx4 %1, %3, %5\n\tglobal_load_dwordx2 %2, %6, %7"
                     : "=&v"(d4), "=&v"(s4), "=&v"(a4)
                     : "v"(e0 * 4), "s"(dst), "s"(src), "v"(e0 * 2), "s"(ea) : "memory");
    } else if (wv == 10) {
        asm volatile("global_load_dword %0, %2, %3\n\tglobal_load_ushort %1, %2, %3 offset:4"
                     : "=&v"(sm0), "=&v"(sm1) : "v"(gnode * 6), "s"(pos) : "memory");
    } else {
        asm volatile("global_load_ushort %0, %1, %2" : "=v"(sm0) : "v"(lane * 2), "s"(wx) : "memory");
        lds_st(smb + A_CNT + lane * 16, u32x4{0, 0, 0, 0});
    }
    bar();
    {
        auto piece = [&](int p) {
            if (p < 5) dma1(smb + L_HIMG + p * 1024, h, (b * BN + 8 * p + r8l) * 128 + ((c8 ^ ((4 * p + (lane >> 4)) & 7)) * 16));
            else if (p < 21) dma1(smb + A_W + (p - 5) * 1024, we, (8 * (p - 5) + r8l) * 128 + ((c8 ^ ((r8l & 2) | (((p - 5) & 1) << 2))) * 16));
            else if (lane < 16) dma1(smb + L_AIMG + BN * 128, we, 128 * 128 + lane * 16);
        };
        piece(wv);
        if (wv < 10) piece(12 + wv);
    }
    ASTAMP(1);
    asm volatile("s_waitcnt vmcnt(0)" : "+v"(ep), "+v"(d4), "+v"(s4), "+v"(a4), "+v"(sm0), "+v"(sm1) :: "memory");
    ASTAMP(2);
    const unsigned e_lo = __builtin_amdgcn_readfirstlane(ep[0]), e_hi = __builtin_amdgcn_readfirstlane(ep[1]);
    const unsigned n_lo = e_lo + 1u, n_hi = e_hi + (n_lo == 0u ? 1u : 0u);
    const unsigned tag_w1 = (n_lo >> 8) | (n_hi << 24), tag_w0 = n_lo << 24;

    unsigned bk[4], ark[4], code = 0;
    if (wv < 10) {
#pragma unroll
        for (int i = 0; i < 4; ++i) {
            bk[i] = __umul24(d4[i], 52429u) >> 21;
            ark[i] = atomicAdd((unsigned*)(sm + A_CNT) + bk[i], 1u);
        }
#pragma unroll
        for (int i = 0; i < 4; ++i) {
            const unsigned dl = d4[i] - bk[i] * BN, w = win_of(dl);
            code |= (w * 8 + dl - win_start(w)) << (8 * i);
        }
    } else if (wv == 10) {
        const f32x4 p = {bf2f(sm0 & 0xffffu), __uint_as_float(sm0 & 0xffff0000u), bf2f(sm1), 0.f};
        if (lane < 48) lds_st(smb + L_PD + lane * 16, lane < BN ? p : f32x4{0.f, 0.f, 0.f, 0.f});
        if (lane < BN) st_dev(Ptab, gnode * 16, __builtin_bit_cast(u32x4, p));
    } else {
        lds_st(smb + L_WX + lane * 4, bf2f(sm0) * NEG_LN2);
        if (b == 0) {
            const u32x4 z = {0, 0, 0, 0};
            st_dev(Ptab, NN * 16 + lane * 16, z);
            if (lane < 8) st_dev(Btab, NN * 128 + lane * 16, z);
        }
    }
    ASTAMP(3);
    bar();
    ASTAMP(4);

    u32x4 t_lo = {0, 0, 0, 0};
    if (wv == 11) {
        const u32x4 cn = lds_ld<u32x4>(smb + A_CNT + lane * 16);
        const u32x4 tr = {div3(cn[0] + 2), div3(cn[1] + 2), div3(cn[2] + 2), div3(cn[3] + 2)};
        const unsigned tot = tr[0] + tr[1] + tr[2] + tr[3];
        const unsigned in = scan64(tot), ex = in - tot;
        const u32x4 of = {ex, ex + tr[0], ex + tr[0] + tr[1], ex + tr[0] + tr[1] + tr[2]};
        lds_st(smb + A_OFF + lane * 16, of);
        if (lane == 63) lds_st(smb + A_TOT, in);
#pragma unroll
        for (int i = 0; i < 4; ++i) t_lo[i] = tag_w0 | (cn[i] << 12) | of[i];
    } else if (wv < 8) {
        const int ct = wv & 3, g = lane >> 4, l15 = lane & 15;
        const char* wimg = sm + A_W + (wv >> 2) * 8192;
        const bf16x8 a0 = wfrag16(wimg, 0, ct, lane), a1 = wfrag16(wimg, 1, ct, lane);
        bf16x8 hb[6];
#pragma unroll
        for (int nt = 0; nt < 3; ++nt) {
            hb[2 * nt] = nfrag16(sm + L_HIMG, 16 * nt + l15, g);
            hb[2 * nt + 1] = nfrag16(sm + L_HIMG, 16 * nt + l15, 4 + g);
        }
        SBAR();
        f32x4 acc[3];
#pragma unroll
        for (int nt = 0; nt < 3; ++nt) acc[nt] = MFMA16(a0, hb[2 * nt], (f32x4{0.f, 0.f, 0.f, 0.f}));
#pragma unroll
        for (int nt = 0; nt < 3; ++nt) acc[nt] = MFMA16(a1, hb[2 * nt + 1], acc[nt]);
#pragma unroll
        for (int nt = 0; nt < 3; ++nt) {
            const int node = 16 * nt + l15;
            const u32x2 o = {pk_bf16(acc[nt][0] * NEG_LOG2E, acc[nt][1] * NEG_LOG2E), pk_bf16(acc[nt][2] * NEG_LOG2E, acc[nt][3] * NEG_LOG2E)};
            const unsigned oa = wv < 4 ? L_AIMG + node * 128 + (16 * ct + 4 * g) * 2
                                       : A_O + node * 128 + 16 * ((2 * ct + (g >> 1)) ^ (node & 7)) + 8 * (g & 1);
            lds_st(smb + (nt < 2 || l15 < BN - 32 ? oa : (unsigned)(A_R + 16320 + (lane & 7) * 8)), o);
        }
    }
    ASTAMP(5);
    bar();
    ASTAMP(6);

    if (wv < 10) {
#pragma unroll
        for (int i = 0; i < 4; ++i) {
            const unsigned q = div3(ark[i]), j = ark[i] - 3 * q;
            const unsigned t = smb + A_R + (lds_ld<unsigned>(smb + A_OFF + bk[i] * 4) + q) * 16;
            const unsigned av = (i & 1) ? (a4[i >> 1] & 0xffff0000u) : (a4[i >> 1] << 16);
            lds_st(t + j * 4, s4[i] | av);
            lds_st(t + 12 + j, (unsigned char)(code >> (8 * i)));
        }
    } else {
#pragma clang loop unroll(disable)
        for (int x = tid - 640; x < BN * 8; x += 128) {
            const int node = x >> 3, ch = (x & 7) ^ (node & 7);
            st_dev(Btab, (b * BN + node) * 128 + ch * 16, lds_ld<u32x4>(smb + A_O + x * 16));
        }
    }
    bar();
    ASTAMP(7);
    {
        const unsigned ntot = lds_ld<unsigned>(smb + A_TOT);
        if ((unsigned)tid < ntot) st_dev(Rec, b * 16384 + tid * 16, lds_ld<u32x4>(smb + A_R + tid * 16));
        if ((unsigned)tid + K2_T < ntot) st_dev(Rec, b * 16384 + (tid + K2_T) * 16, lds_ld<u32x4>(smb + A_R + (tid + K2_T) * 16));
    }
    ASTAMP(8);
    {
        const u32x4 z = {0, 0, 0, 0};
        lds_st(smb + L_STAGE + wv * 2048 + lane * 16, z);
        lds_st(smb + L_STAGE + wv * 2048 + 1024 + lane * 16, z);
        const u32x4 n32 = {NN, NN, NN, NN}, n8 = {0x08080808u, 0x08080808u, 0x08080808u, 0x08080808u};
        lds_st(smb + L_R32 + tid * 16, n32);
        if (tid < RALL / 4 - K2_T) lds_st(smb + L_R32 + (K2_T + tid) * 16, n32);
        if (wv >= 1 && wv < 5) {
            if (tid - 64 < RALL / 16) lds_st(smb + L_R8 + (tid - 64) * 16, n8);
        } else if (wv == 5) {
            if (lane < 56) lds_st(smb + L_BINS + lane * 16, z);
        } else if (wv == 6) {
            if (lane < 48) lds_st(smb + L_AIMG + (BN + 2) * 128 + lane * 16, z);
        } else if (wv == 7) {
            const int n = lane >> 2, j = lane & 3;
            if (lane < 36) lds_st(smb + L_OH + lane * 4, (n < 8 && (n >> 1) == j) ? (0x3F80u << (16 * (n & 1))) : 0u);
        }
    }
    wait_vm0();
    ASTAMP(9);
    bar();
    if (wv == 11) {
        st_dev(ws + W_T, b * 2048 + lane * 32, u32x4{t_lo[0], tag_w1, t_lo[1], tag_w1});
        st_dev(ws + W_T, b * 2048 + lane * 32 + 16, u32x4{t_lo[2], tag_w1, t_lo[3], tag_w1});
    }
    ASTAMP(10);

    const int r = lane & 31, hh = lane >> 5;
    const int win = (int)((0x545432231100ull >> (4 * wv)) & 7);
    const bool second = (0xC5A >> wv) & 1;
    const int wnode = win_start(win);
    char* part = sm + L_SLOT + wv * 4096;
    const int blk = (lane >> 4) & 1, q4 = (lane & 15) >> 2, p4 = lane & 3;
    bf16x8 i0, i1, e0, e1;
#pragma unroll
    for (int j = 0; j < 8; ++j) {
        i0[j] = (r == 8 * hh + j) ? (short)0x3F80 : (short)0;
        i1[j] = (r == 16 + 8 * hh + j) ? (short)0x3F80 : (short)0;
    }
    {
        const int row = hh ? BN + q4 : wnode + q4;
        const char* ab = sm + L_AIMG + row * 128 + (16 * blk + 4 * p4) * 2;
        e0 = cat8(tr_read(ab), tr_read(ab + 512));
        e1 = cat8(tr_read(ab + 64), tr_read(ab + 64 + 512));
    }
    unsigned aad[4], kk[4];
    {
        const int rho = 8 * (r & 3) + (r >> 2);
        const int fsw = (r & 3) ^ (((r >> 3) & 1) << 2);
#pragma unroll
        for (int s = 0; s < 4; ++s) aad[s] = smb + L_SLOT + wv * 4096 + rho * 128 + 16 * ((2 * s + hh) ^ fsw);
#pragma unroll
        for (int i = 0; i < 4; ++i) kk[i] = 16 * ((lane & 7) ^ i ^ (((lane >> 4) & 1) << 2)) + (3072 - 1024 * i);
    }
    const char* gB = Btab - 3072;
    const unsigned slot_a = smb + L_SLOT + wv * 4096;
    const unsigned pbuf_a = smb + L_PB + wv * 1024;
    unsigned src_a = smb + L_R32 + (lane >> 3) * 16, rs_a = smb + L_R32 + lane * 4, rn_a = smb + L_R8 + lane;
    unsigned stage_r = smb + L_STAGE + wv * 2048 + r * 64;
    unsigned trb_a = smb + L_STAGE + wv * 2048 + (4 * hh + q4) * 64 + blk * 32 + p4 * 8;
    const unsigned pdw = smb + L_PD + wnode * 16;
    const unsigned oh_a = smb + L_OH;
    f32x16 z0, z1;
#pragma unroll
    for (int i = 0; i < 16; ++i) { z0[i] = 0.f; z1[i] = 0.f; }
    PIN(i0); PIN(i1); PIN(e0); PIN(e1); PIN(z0); PIN(z1);
    PIN(aad[0]); PIN(aad[1]); PIN(aad[2]); PIN(aad[3]); PIN(kk[0]); PIN(kk[1]); PIN(kk[2]); PIN(kk[3]);
    PIN(src_a); PIN(rs_a); PIN(rn_a); PIN(stage_r); PIN(trb_a);

#pragma clang loop unroll(disable)
    for (int q = wv; q < 16; q += K2_W)
        dma1(smb + L_WH + q * 1024, wh, (8 * q + r8l) * 128 + ((c8 ^ ((r8l & 2) | ((q & 1) << 2))) * 16));
    unsigned tw;
    asm volatile("global_load_dword %0, %1, %2" : "=v"(tw)
                 : "v"(lane == 0 ? b * (BN * 128) : (NN * 128 + b * (BN * 6)) & ~3), "s"(out) : "memory");

    const int l3 = (lane * 171) >> 9, sub = lane - 3 * l3;
    const int cid = 21 * wv + l3;
    const bool cok = lane < 63 && cid < NCH;
    const int cidc = cok ? cid : 0;
    unsigned tval = 0;
    bool lost = true;
#pragma clang loop unroll(disable)
    for (int spin = 0; spin < (1 << 16); ++spin) {
        u32x2 tv;
        asm volatile("global_load_dwordx2 %0, %2, %3 sc1\n\ts_waitcnt vmcnt(0)"
                     : "=&v"(tv), "+v"(tw) : "v"((unsigned)W_T + cidc * 2048 + b * 8), "s"(ws) : "memory");
        tval = tv[0];
        if (__all(!cok || (tv[1] == tag_w1 && (tv[0] >> 24) == (tag_w0 >> 24)))) { lost = false; break; }
        __builtin_amdgcn_s_sleep(2);
    }
    PSTAMP(2);

    const unsigned cnt = (cok && !lost) ? ((tval >> 12) & 0xfffu) : 0u;
    const unsigned ntr = div3(cnt + 2);
    const unsigned incl = scan64(sub == 0 ? ntr : 0u);
    const unsigned mw = __builtin_amdgcn_readlane(incl, 63);
    const bool slow_w = mw > MAPCAP;
    const unsigned map_a = smb + L_SLOT + wv * 4096;
    QSTAMP(1);
    if (!slow_w) {
        unsigned g = (cidc * 1024 + (tval & 0xfffu) + sub) | ((cnt - 3 * sub) << 20);
        unsigned mp = map_a + (incl - ntr + sub) * 4;
#pragma clang loop unroll(disable) vectorize(disable) interleave(disable)
        for (unsigned i = sub; i < ntr; i += 3) { lds_st(mp, g); mp += 12; g += 3 - (9u << 20); }
    }
    PSTAMP(3);
    const unsigned mwe = slow_w ? 0u : mw;
    unsigned left[2];
    u32x4 rec[2];
#pragma unroll
    for (int k = 0; k < 2; ++k) {
        const unsigned m = lds_ld<unsigned>(map_a + (64 * k + lane) * 4);
        const bool ok = (unsigned)(64 * k + lane) < mwe;
        left[k] = ok ? m >> 20 : 0u;
        const unsigned go = ok ? (m & 0xfffffu) * 16 : 0u;
        asm volatile("global_load_dwordx4 %0, %1, %2 sc1" : "=v"(rec[k]) : "v"(go), "s"(Rec) : "memory");
    }
    if (lane == 0) lds_st(smb + L_WF + wv * 4, lost ? 2u : slow_w ? 1u : 0u);
    PSTAMP(4);
    asm volatile("s_waitcnt vmcnt(0)" : "+v"(rec[0]), "+v"(rec[1]) :: "memory");
    PSTAMP(5);
    unsigned bin[6], rk[6];
#pragma unroll
    for (int k = 0; k < 6; ++k) {
        const unsigned w = (rec[k / 3][3] >> (8 * (k % 3) + 3)) & 7u;
        bin[k] = (left[k / 3] > (unsigned)(k % 3) ? w : 6u) * 128 + (lane & 31) * 4;
        rk[k] = atomicAdd((unsigned*)(sm + L_BINS + bin[k]), 1u);
    }
    QSTAMP(3);
    PSTAMP(6);
    bar();
    PSTAMP(7);
    unsigned pf;
    {
        const unsigned part = blockIdx.x >> 3;
        const unsigned off = tid < 626 ? (unsigned)W_B + part * (313 * 128) + tid * 64
                                       : (unsigned)W_P + part * (40 * 128) + (tid < 706 ? tid - 626 : 0) * 64;
        asm volatile("global_load_dword %0, %1, %2 sc1" : "=v"(pf) : "v"(off), "s"(ws) : "memory");
    }

    if (wv < 6) {
        const unsigned c = lds_ld<unsigned>(smb + L_BINS + wv * 128 + (lane & 31) * 4);
        const unsigned in = scan32(c);
        if (lane < 32) lds_st(smb + L_OFFS + wv * 128 + lane * 4, wv * WCAP + in - c);
        if (lane == 31) lds_st(smb + L_WIN + wv * 4, (in + 31) >> 5);
    } else if (wv == 6) {
        const unsigned wfl = lds_ld<unsigned>(smb + L_WF + (lane < K2_W ? lane : 0) * 4);
        const unsigned md = __any(wfl == 2) ? 2u : __any(wfl != 0) ? 1u : 0u;
        if (lane == 0) lds_st(smb + L_WIN + 6 * 4, md);
    } else if (wv == 7) {
        if (b == 0 && lane == 0) *(u32x2*)(ws + W_E) = u32x2{n_lo, n_hi};
    }
    PSTAMP(8);
    bar();
    QSTAMP(4);
    PSTAMP(9);
    const unsigned wiv = lds_ld<unsigned>(smb + L_WIN + (lane & 7) * 4);
    int tw6[6];
#pragma unroll
    for (int w = 0; w < 6; ++w) tw6[w] = __builtin_amdgcn_readlane(wiv, w);
    const unsigned mode = __builtin_amdgcn_readlane(wiv, 6);
    if (mode == 2) { asm volatile("s_waitcnt vmcnt(0)" : "+v"(pf) :: "memory"); return; }
    constexpr int WT = WCAP / 32;
    const bool slow = mode != 0 || tw6[0] > WT || tw6[1] > WT || tw6[2] > WT || tw6[3] > WT || tw6[4] > WT || tw6[5] > WT;

    if (!slow) {
#pragma unroll
        for (int k = 0; k < 6; ++k) {
            const unsigned o = lds_ld<unsigned>(smb + L_OFFS + bin[k]) + rk[k];
            const unsigned p = left[k / 3] > (unsigned)(k % 3) ? o : (unsigned)(RALL - 1);
            lds_st(smb + L_R32 + p * 4, rec[k / 3][k % 3]);
            lds_st(smb + L_R8 + p, (unsigned char)((rec[k / 3][3] >> (8 * (k % 3))) & 7u));
        }
        const int wstart = win * WCAP;
        const int l0 = tw6[0] + tw6[3] + tw6[4], l1 = tw6[0] + tw6[2] + tw6[5];
        const int l2 = tw6[1] + tw6[2] + tw6[4], l3s = tw6[1] + tw6[3] + tw6[5];
        const int wtiles = win == 0 ? tw6[0] : win == 1 ? tw6[1] : win == 2 ? tw6[2] : win == 3 ? tw6[3] : win == 4 ? tw6[4] : tw6[5];
        const int la = win == 0 ? l0 : win == 1 ? l2 : win == 2 ? l1 : win == 3 ? l3s : win == 4 ? l0 : l1;
        const int lb = win == 0 ? l1 : win == 1 ? l3s : win == 2 ? l2 : win == 3 ? l0 : win == 4 ? l2 : l3s;
        int first = (4 * wtiles - la + lb + 4) >> 3;
        first = first < 0 ? 0 : first > wtiles ? wtiles : first;
        const int ta = second ? first : 0, tb = second ? wtiles : first;

        bar();
        QSTAMP(5);
        PSTAMP(10);

        f32x16 xa, xb;
        QSTAMP(6);
        CSTAMP(12);

        if (ta < tb) {
            int base = wstart + 32 * ta;
            const int end = wstart + 32 * tb;
            bf16x8 a0, a1, a2, a3, ef, wf0, wf1;
            unsigned rs, rn, w0, w1, ua, vb;
            unsigned saddr;
            bool valid;
            u32x4 pkp = {0, 0, 0, 0};
#pragma unroll
            for (int j = 0; j < 8; ++j) wf1[j] = 0;

            u32x4 sn, exr;
            f32x4 pd, ps;
            auto read_src = [&](int ri) { sn = lds_ld<u32x4>(src_a + ri * 4); };
            auto issue_rows = [&]() {
                const unsigned v0 = mad16(sn[0], 128, kk[0]), v1 = mad16(sn[1], 128, kk[1]);
                const unsigned v2 = mad16(sn[2], 128, kk[2]), v3 = mad16(sn[3], 128, kk[3]);
                wait_lgkm0();
                dma4_dev(slot_a, gB, v0, v1, v2, v3);
            };
            auto read_pair = [&](int ri) {
                rs = lds_ld<unsigned>(rs_a + ri * 4);
                rn = lds_ld<unsigned char>(rn_a + ri);
            };
            auto issue_pos = [&]() { dma1_dev(pbuf_a, Ptab, mad16(rs, 16, 0u)); };
            auto read_pos = [&]() {
                pd = lds_ld<f32x4>(pdw + rn * 16);
                ps = lds_ld<f32x4>(pbuf_a + lane * 16);
            };
            auto geometry = [&]() {
                const float dx = pd[0] - ps[0], dy = pd[1] - ps[1], dz = pd[2] - ps[2];
                const float d2 = (dx * dx + dy * dy + dz * dz) * NEG_LOG2E;
                w0 = pk_bf16(1.0f, dx);
                w1 = pk_bf16(dy, dz);
                const unsigned ext = pk_bf16(d2, __uint_as_float(rs & 0xffff0000u) * NEG_LOG2E);
                const auto sw = __builtin_amdgcn_permlane32_swap(oh_a + rn * 16, ext, false, false);
                ua = sw[0];
                vb = sw[1];
                saddr = stage_r + rn * 8;
                PIN(saddr); PIN(w0); PIN(w1);
                valid = rn < 8;
            };
            auto read_rows = [&]() {
                a0 = lds_ld<bf16x8>(aad[0]); a1 = lds_ld<bf16x8>(aad[1]);
                a2 = lds_ld<bf16x8>(aad[2]); a3 = lds_ld<bf16x8>(aad[3]);
            };
            auto read_ext = [&](unsigned u) { exr = lds_ld<u32x4>(hh ? oh_a : u); };
            auto fix_ext = [&](unsigned u) {
                u32x4 ex = exr;
                ex[0] = hh ? u : ex[0];
                ef = __builtin_bit_cast(bf16x8, ex);
                PIN(ef);
            };
            auto stage_weights = [&](bool wr) {
                if (wr) lds_st(saddr, u32x2{w0, w1});
                wf0 = cat8(tr_read_a(trb_a), tr_read_a(trb_a + 512));
                wf1 = cat8(tr_read_a(trb_a + 1024), tr_read_a(trb_a + 1536));
                if (wr) lds_st(saddr, u32x2{0, 0});
            };
            auto nop = [&]() {};

            if (wv >= 8) __builtin_amdgcn_s_sleep(10);
            else if (wv >= 4) __builtin_amdgcn_s_sleep(5);
            read_pair(base);
            read_src(base);
            issue_rows();
            issue_pos();
            if (base + 32 < end) read_src(base + 32);
            PSTAMP(11);
            asm volatile("s_waitcnt vmcnt(0)" : "+v"(pf) :: "memory");
            PSTAMP(12);
            read_pos();
            read_rows();
            geometry();
            read_ext(ua);
            if (base + 32 < end) issue_rows();
            fix_ext(ua);
            {
                f32x16 zero;
#pragma unroll
                for (int i = 0; i < 16; ++i) zero[i] = 0.f;
                xa = MFMA32(a0, i0, zero);
                xa = MFMA32(a1, i1, xa);
                xa = MFMA32(ef, e0, xa);
            }
            SBAR();
            PSTAMP(13);
            auto set_prio = [&](int rem) {
                if (rem >= 192) __builtin_amdgcn_s_setprio(3);
                else if (rem >= 128) __builtin_amdgcn_s_setprio(2);
                else if (rem >= 64) __builtin_amdgcn_s_setprio(1);
                else __builtin_amdgcn_s_setprio(0);
            };
            while (true) {
                set_prio(end - base);
                half_tile(xa, xb, a2, a3, ef, e1, i0, i1, z1, pkp, wf1, z0, wf0,
                          [&]() { stage_weights(valid && hh == 0); read_src(base + 64); read_pair(base + 64); },
                          [&]() { wait_vm0(); read_rows(); read_ext(vb); },
                          nop);
                half_tile(xb, xa, a0, a1, ef, e0, i0, i1, z0, pkp, wf1, z1, wf0,
                          [&]() { fix_ext(vb); if (base + 64 < end) issue_rows(); issue_pos(); }, nop, nop);
                if (base + 32 >= end) break;
                half_tile(xa, xb, a2, a3, ef, e1, i0, i1, z1, pkp, wf1, z0, wf0,
                          [&]() { stage_weights(valid && hh == 1); read_src(base + 96); },
                          [&]() { wait_vm0(); read_rows(); read_pos(); },
                          [&]() { geometry(); read_ext(ua); });
                half_tile(xb, xa, a0, a1, ef, e0, i0, i1, z0, pkp, wf1, z1, wf0,
                          [&]() { fix_ext(ua); if (base + 96 < end) issue_rows(); }, nop, nop);
                base += 64;
                if (base >= end) break;
            }
            z1 = MFMA32(__builtin_bit_cast(bf16x8, pkp), wf1, z1);
        }
        asm volatile("s_waitcnt vmcnt(0)" : "+v"(pf) :: "memory");
        CSTAMP(13);
        QSTAMP(7);
        float cs0 = 0.f, cs1 = 0.f;
#pragma unroll
        for (int g = 0; g < 4; ++g) {
            const f32x4 w0 = *(const f32x4*)(sm + L_WX + (8 * g + 4 * hh) * 4);
            const f32x4 w1 = *(const f32x4*)(sm + L_WX + (32 + 8 * g + 4 * hh) * 4);
#pragma unroll
            for (int i = 0; i < 4; ++i) { cs0 += z0[4 * g + i] * w0[i]; cs1 += z1[4 * g + i] * w1[i]; }
        }
        const float csh = cs0 + cs1;
        const auto cw = __builtin_amdgcn_permlane32_swap(__float_as_uint(csh), __float_as_uint(csh), false, false);
        const float cst = __uint_as_float(cw[0]) + __uint_as_float(cw[1]);
        wait_vm0();
        if (hh == 0) ((float*)(part + PPOS))[r] = cst;
        if ((lane & 3) == 0) {
            const int n = r >> 2;
#pragma unroll
            for (int g = 0; g < 4; ++g) {
                *(f32x4*)(part + n * PROW + (8 * g + 4 * hh) * 4) = f32x4{z0[4 * g], z0[4 * g + 1], z0[4 * g + 2], z0[4 * g + 3]};
                *(f32x4*)(part + n * PROW + (32 + 8 * g + 4 * hh) * 4) = f32x4{z1[4 * g], z1[4 * g + 1], z1[4 * g + 2], z1[4 * g + 3]};
            }
        }
    } else {
        __builtin_amdgcn_fence(__ATOMIC_ACQUIRE, "agent");
        asm volatile("s_waitcnt vmcnt(0)" : "+v"(pf) :: "memory");
        {
            const u32x4 z = {0, 0, 0, 0};
            *(u32x4*)(part + lane * 16) = z;
            *(u32x4*)(part + 1024 + lane * 16) = z;
            *(u32x4*)(part + 2048 + lane * 16) = z;
        }
        bar();
        const float wd = bf2f(we[128 * 64 + lane]), wa = bf2f(we[129 * 64 + lane]);
        const float wxl = ((const float*)(sm + L_WX))[lane];
        for (int ci = wv; ci < NCH; ci += K2_W) {
            const unsigned tv = *(const unsigned*)(ws + W_T + ci * 2048 + b * 8);
            const unsigned g0 = ci * 1024 + (tv & 0xfffu), cn = (tv >> 12) & 0xfffu;
            for (unsigned i = 0; i < cn; ++i) {
                const unsigned* tp = (const unsigned*)(Rec + (size_t)(g0 + div3(i)) * 16);
                const unsigned j = i - 3 * div3(i);
                const unsigned rc = tp[j], cd = (tp[3] >> (8 * j)) & 0xffu;
                const unsigned s = rc & 0xffffu, w = cd >> 3, n = cd & 7, dl = win_start(w) + n;
                const f32x4 ps = *(const f32x4*)(Ptab + s * 16);
                const f32x4 pd = *(const f32x4*)(sm + L_PD + dl * 16);
                const float dx = pd[0] - ps[0], dy = pd[1] - ps[1], dz = pd[2] - ps[2];
                const unsigned e = pk_bf16((dx * dx + dy * dy + dz * dz) * NEG_LOG2E, __uint_as_float(rc & 0xffff0000u) * NEG_LOG2E);
                const unsigned dxy = pk_bf16(dx, dy), dz0 = pk_bf16(dz, 0.f);
                float y = bf2f(((const uint16_t*)(sm + L_AIMG))[dl * 64 + lane]) + bf2f(((const uint16_t*)Btab)[s * 64 + lane]);
                y += bf2f(e & 0xffffu) * wd + bf2f(e >> 16) * wa;
                const float m = bf2f(pk_bf16(y * rcp(1.0f + ex2(y)), 0.f) & 0xffffu);
                char* tgt = sm + L_SLOT + wave_a(w) * 4096;
                atomicAdd((float*)(tgt + n * PROW) + lane, m);
                float t = m * wxl;
                for (int o = 32; o > 0; o >>= 1) t += __shfl_xor(t, o);
                if (lane < 3) {
                    const float dv = lane == 0 ? bf2f(dxy & 0xffffu) : lane == 1 ? bf2f(dxy >> 16) : bf2f(dz0 & 0xffffu);
                    atomicAdd((float*)(tgt + PPOS) + 4 * n + 1 + lane, dv * t);
                }
            }
        }
    }
    const int ct = wv & 3, g = lane >> 4, node = 16 * (wv >> 2) + (lane & 15), nodec = node < BN ? node : BN - 1;
    bf16x8 wfr[4];
#pragma unroll
    for (int s = 0; s < 4; ++s) wfr[s] = wfrag16(sm + L_WH, s, ct, lane);
    f32x4 acc = {0.f, 0.f, 0.f, 0.f};
    acc = MFMA16(wfr[0], nfrag16(sm + L_HIMG, nodec, g), acc);
    acc = MFMA16(wfr[1], nfrag16(sm + L_HIMG, nodec, 4 + g), acc);
    const u32x2 hv = *(const u32x2*)(sm + L_HIMG + nodec * 128 + 16 * ((2 * ct + (g >> 1)) ^ ((nodec >> 1) & 7)) + 8 * (g & 1));
    const unsigned nwin = win_of(nodec);
    const char* pb = sm + L_SLOT + wave_a(nwin) * 4096 + (nodec - win_start(nwin)) * PROW + g * 32;
    const int pd2 = (wave_b(nwin) - wave_a(nwin)) * 4096;
    QSTAMP(8);
    bar();
    QSTAMP(9);
#pragma unroll
    for (int s = 0; s < 2; ++s) {
        const f32x4 m0 = (*(const f32x4*)(pb + s * 128) + *(const f32x4*)(pb + s * 128 + pd2)) * NEG_LN2;
        const f32x4 m1 = (*(const f32x4*)(pb + s * 128 + 16) + *(const f32x4*)(pb + s * 128 + 16 + pd2)) * NEG_LN2;
        acc = MFMA16(wfr[2 + s], as_frag(pk_bf16(m0[0], m0[1]), pk_bf16(m0[2], m0[3]), pk_bf16(m1[0], m1[1]), pk_bf16(m1[2], m1[3])), acc);
    }
    if (wv >= 10) {
        const int x = (wv - 10) * 64 + lane;
        if (x < 3 * BN) {
            const int nl = (x * 171) >> 9, cc = x - 3 * nl, w = win_of(nl), n = nl - win_start(w);
            const char* pp = sm + L_SLOT + PPOS + (4 * n + 1 + cc) * 4;
            const float sum = *(const float*)(pp + wave_a(w) * 4096) + *(const float*)(pp + wave_b(w) * 4096);
            const float p0 = ((const float*)(sm + L_PD))[nl * 4 + cc];
            out[NN * 64 + (b * BN + nl) * 3 + cc] = (uint16_t)(pk_bf16(p0 + sum, 0.f) & 0xffffu);
        }
    }
    QSTAMP(10);
    if (node < BN) {
        const float o0 = bf2f(hv[0] & 0xffffu) + silu_f(acc[0]), o1 = bf2f(hv[0] >> 16) + silu_f(acc[1]);
        const float o2 = bf2f(hv[1] & 0xffffu) + silu_f(acc[2]), o3 = bf2f(hv[1] >> 16) + silu_f(acc[3]);
        st_nt(out + (b * BN + node) * 64 + 16 * ct + 4 * g, u32x2{pk_bf16(o0, o1), pk_bf16(o2, o3)});
    }
    QSTAMP(11);
    STAMP_OUT(1, ws);
}

}

extern "C" void kernel_launch(void* const* d_in, const int* in_sizes, int n_in, void* d_out, int out_size, void* d_ws,
                              size_t ws_size, hipStream_t stream) {
    using namespace egnn;
    const uint16_t* h = (const uint16_t*)d_in[0];
    const uint16_t* pos = (const uint16_t*)d_in[1];
    const uint16_t* ea = (const uint16_t*)d_in[2];
    const int* src = (const int*)d_in[3];
    const int* dst = (const int*)d_in[4];
    const uint16_t* we = (const uint16_t*)d_in[5];
    const uint16_t* wx = (const uint16_t*)d_in[6];
    const uint16_t* wh = (const uint16_t*)d_in[7];
    (void)hipFuncSetAttribute((const void*)k_egnn, hipFuncAttributeMaxDynamicSharedMemorySize, L_END);
    k_egnn<<<NBK, K2_T, L_END, stream>>>(src, dst, ea, h, pos, we, wx, wh, (char*)d_ws, (uint16_t*)d_out);
}
```

```cpp
#include <hip/hip_runtime.h>
#include <stdint.h>

namespace egnn {

typedef short    bf16x8 __attribute__((ext_vector_type(8)));
typedef short    s16x4  __attribute__((ext_vector_type(4)));
typedef float    f32x16 __attribute__((ext_vector_type(16)));
typedef float    f32x4  __attribute__((ext_vector_type(4)));
typedef float    f32x2  __attribute__((ext_vector_type(2)));
typedef __bf16   bf16x2 __attribute__((ext_vector_type(2)));
typedef unsigned u32x4  __attribute__((ext_vector_type(4)));
typedef unsigned u32x2  __attribute__((ext_vector_type(2)));
#define LDS_AS __attribute__((address_space(3)))
#define DI __device__ __forceinline__
#define SBAR() __builtin_amdgcn_sched_barrier(0)
#define PIN(x) asm volatile("" : "+v"(x))
#define MFMA32(a, b, c) __builtin_amdgcn_mfma_f32_32x32x16_bf16(a, b, c, 0, 0, 0)
#define MFMA16(a, b, c) __builtin_amdgcn_mfma_f32_16x16x32_bf16(a, b, c, 0, 0, 0)

constexpr int NN = 10000;
constexpr int NE = 640000;
constexpr int BN = 40;
constexpr int NBK = 250;
constexpr int CE = 2560;
constexpr int NCH = 250;
constexpr float NEG_LOG2E = -1.4426950408889634f;
constexpr float NEG_LN2 = -0.6931471805599453f;

constexpr size_t W_E   = 0;
constexpr size_t W_T   = 4096;
constexpr size_t W_R   = 1u << 20;
constexpr size_t W_B   = (5u << 20) + (512u << 10);
constexpr size_t W_P   = 7u << 20;

DI unsigned win_of(unsigned dl) { return ((dl * 37u) >> 8) + (dl == 34u ? 1u : 0u); }
DI unsigned win_start(unsigned w) { return 7u * w - (w == 5u ? 1u : 0u); }

DI int wave_a(unsigned w) { return (0x987520u >> (4 * w)) & 15; }
DI int wave_b(unsigned w) { return (0xBA4631u >> (4 * w)) & 15; }

DI float bf2f(unsigned v16) { return __uint_as_float(v16 << 16); }
DI unsigned pk_bf16(float lo, float hi) {
    f32x2 x = {lo, hi};
    return __builtin_bit_cast(unsigned, __builtin_convertvector(x, bf16x2));
}
DI float silu_f(float x) {
    return x * __builtin_amdgcn_rcpf(1.0f + __builtin_amdgcn_exp2f(x * NEG_LOG2E));
}
DI s16x4 tr_read(const char* p) {
    return __builtin_amdgcn_ds_read_tr16_b64_v4i16((LDS_AS s16x4*)p);
}
DI s16x4 tr_read_a(unsigned a) { return __builtin_amdgcn_ds_read_tr16_b64_v4i16((LDS_AS s16x4*)(size_t)a); }
DI bf16x8 cat8(s16x4 a, s16x4 b) { return __builtin_shufflevector(a, b, 0, 1, 2, 3, 4, 5, 6, 7); }
DI void wait_vm0() { asm volatile("s_waitcnt vmcnt(0)" ::: "memory"); }
DI void wait_lgkm0() { asm volatile("s_waitcnt lgkmcnt(0)" ::: "memory"); }
DI void bar() { asm volatile("s_waitcnt lgkmcnt(0)\n\ts_barrier" ::: "memory"); }

DI void dma1(unsigned ldsaddr, const void* gbase, unsigned voff) {
    unsigned keep;
    asm volatile("s_mov_b32 %0, m0\n\ts_mov_b32 m0, %1\n\ts_nop 0\n\t"
                 "global_load_lds_dwordx4 %2, %3\n\t"
                 "s_mov_b32 m0, %0"
                 : "=&s"(keep) : "s"(ldsaddr), "v"(voff), "s"(gbase) : "memory");
}
DI void dma1_dev(unsigned ldsaddr, const void* gbase, unsigned voff) {
    unsigned keep;
    asm volatile("s_mov_b32 %0, m0\n\ts_mov_b32 m0, %1\n\ts_nop 0\n\t"
                 "global_load_lds_dwordx4 %2, %3 sc1\n\t"
                 "s_mov_b32 m0, %0"
                 : "=&s"(keep) : "s"(ldsaddr), "v"(voff), "s"(gbase) : "memory");
}
DI void dma4_dev(unsigned ldsaddr, const void* gbase, unsigned v0, unsigned v1, unsigned v2, unsigned v3) {
    unsigned keep;
    asm volatile("s_mov_b32 %0, m0\n\ts_mov_b32 m0, %1\n\ts_nop 0\n\t"
                 "global_load_lds_dwordx4 %2, %6 sc1\n\t"
                 "global_load_lds_dwordx4 %3, %6 offset:1024 sc1\n\t"
                 "global_load_lds_dwordx4 %4, %6 offset:2048 sc1\n\t"
                 "global_load_lds_dwordx4 %5, %6 offset:3072 sc1\n\t"
                 "s_mov_b32 m0, %0"
                 : "=&s"(keep) : "s"(ldsaddr), "v"(v0), "v"(v1), "v"(v2), "v"(v3), "s"(gbase) : "memory");
}
template <typename T> DI void st_nt(void* p, T v) { __builtin_nontemporal_store(v, (T*)p); }
DI void st_dev(const char* base, unsigned off, u32x4 v) {
    asm volatile("global_store_dwordx4 %0, %1, %2 sc1\n\ts_nop 1" :: "v"(off), "v"(v), "s"(base) : "memory");
}
DI unsigned div3(unsigned x) { return __umul24(x, 43691u) >> 17; }
DI unsigned mad16(unsigned x, unsigned m, unsigned a) {
    unsigned r;
    asm("v_mad_u32_u16 %0, %1, %2, %3" : "=v"(r) : "v"(x), "s"(m), "v"(a));
    return r;
}
template <typename T> DI T lds_ld(unsigned a) { return *(const LDS_AS T*)(size_t)a; }
template <typename T> DI void lds_st(unsigned a, T v) { *(LDS_AS T*)(size_t)a = v; }
template <int CTRL, int ROWMASK> DI unsigned dpp_add(unsigned x) {
    return x + (unsigned)__builtin_amdgcn_update_dpp(0, (int)x, CTRL, ROWMASK, 0xf, true);
}
DI unsigned scan32(unsigned x) {
    x = dpp_add<0x111, 0xf>(x); x = dpp_add<0x112, 0xf>(x); x = dpp_add<0x114, 0xf>(x); x = dpp_add<0x118, 0xf>(x);
    x = dpp_add<0x142, 0xa>(x);
    return x;
}
DI unsigned scan64(unsigned x) { x = scan32(x); x = dpp_add<0x143, 0xc>(x); return x; }
DI int wswz(int k) { return (k & 2) | ((k >> 1) & 4); }
DI bf16x8 wfrag16(const char* img, int s, int ct, int lane) {
    const int g = lane >> 4, q = (lane & 15) >> 2, p = lane & 3;
    const int k0 = 32 * s + 8 * g + q, k1 = k0 + 4, chn = 2 * ct + (p >> 1);
    return cat8(tr_read(img + k0 * 128 + 16 * (chn ^ wswz(k0)) + 8 * (p & 1)),
                tr_read(img + k1 * 128 + 16 * (chn ^ wswz(k1)) + 8 * (p & 1)));
}
DI bf16x8 nfrag16(const char* img, int node, int chunk) {
    return *(const bf16x8*)(img + node * 128 + 16 * (chunk ^ ((node >> 1) & 7)));
}

#define ASTAMP(i)
#define PSTAMP(i)
#define QSTAMP(i)
#define STAMP_DECL
#define STAMP(i)
#define CSTAMP(i)
#define STAMP_OUT(kern, wsp)

constexpr int K2_W = 12, K2_T = 64 * K2_W;
constexpr int WCAP = 576;
constexpr int RCAP = 6 * WCAP;
constexpr int L_SLOT = 0;
constexpr int L_STAGE = 49152;
constexpr int RALL = RCAP + 128;
constexpr int L_R32 = 73728;
constexpr int L_R8 = L_R32 + RALL * 4;
constexpr int L_AIMG = L_R8 + RALL;
constexpr int L_HIMG = L_AIMG + 6144;
constexpr int L_WH = L_HIMG + 6144;
constexpr int L_PD = L_WH + 16384;
constexpr int L_OH = L_PD + 768;
constexpr int L_WX = L_OH + 256;
constexpr int L_BINS = L_WX + 256;
constexpr int L_WF = L_BINS + 896;
constexpr int L_PB = L_WF + 64;
constexpr int L_END = L_PB + K2_W * 1024;
constexpr int A_CNT = L_SLOT, A_OFF = A_CNT + 1024, A_TOT = A_OFF + 1024, A_R = A_TOT + 16, A_W = A_R + 16384, A_O = A_W + 16384;
static_assert(A_O + BN * 128 <= L_STAGE, "phase A scratch");
constexpr unsigned MAPCAP = 128;
constexpr int PROW = 272, PPOS = 8 * PROW;

DI bf16x8 as_frag(unsigned a, unsigned b, unsigned c, unsigned d) {
    u32x4 v = {a, b, c, d};
    return __builtin_bit_cast(bf16x8, v);
}
DI float ex2(float x) { return __builtin_amdgcn_exp2f(x); }
DI float rcp(float x) { return __builtin_amdgcn_rcpf(x); }

template <class H1, class H2, class H3>
DI void half_tile(const f32x16& xc, f32x16& xn, const bf16x8& aA, const bf16x8& aB, const bf16x8& ef, const bf16x8& eB,
                  const bf16x8& i0, const bf16x8& i1, f32x16& zp, u32x4& pkp, const bf16x8& wp, f32x16& zc,
                  const bf16x8& wc, H1&& hook1, H2&& hook2, H3&& hook3) {
    f32x16 zero;
#pragma unroll
    for (int i = 0; i < 16; ++i) zero[i] = 0.f;
    f32x2 e0, e1, e2, e3;
    u32x4 pk;
#define EXP2(e, i) do { e[0] = ex2(xc[i]); PIN(e[0]); } while (0)
#define EXP2H(e, i) do { e[1] = ex2(xc[i]); PIN(e[1]); } while (0)
#define RCP2(e) do { e[0] = rcp(e[0]); PIN(e[0]); } while (0)
#define RCP2H(e) do { e[1] = rcp(e[1]); PIN(e[1]); } while (0)
#define PKMODE 0
#define ADD1(e) do { e = e + 1.0f; PIN(e); } while (0)
#define MULX(e, i) do { e = e * f32x2{xc[i], xc[i + 1]}; PIN(e); } while (0)
#define CVT(d, e) do { d = __builtin_bit_cast(unsigned, __builtin_convertvector(e, bf16x2)); PIN(d); } while (0)
    xn = MFMA32(aA, i0, zero); SBAR();
    EXP2(e0, 0); EXP2H(e0, 1); EXP2(e1, 2); SBAR();
    zp = MFMA32(__builtin_bit_cast(bf16x8, pkp), wp, zp); SBAR();
    EXP2H(e1, 3); EXP2(e2, 4); EXP2H(e2, 5); EXP2(e3, 6); EXP2H(e3, 7); SBAR();
    ADD1(e0); ADD1(e1); ADD1(e2); ADD1(e3); SBAR();
    hook1(); SBAR();
    xn = MFMA32(aB, i1, xn); SBAR();
    RCP2(e0); RCP2H(e0); RCP2(e1); SBAR();
    xn = MFMA32(ef, eB, xn); SBAR();
    RCP2H(e1); RCP2(e2); RCP2H(e2); RCP2(e3); RCP2H(e3); SBAR();
    MULX(e0, 0); MULX(e1, 2); MULX(e2, 4); MULX(e3, 6); SBAR();
    CVT(pk[0], e0); CVT(pk[1], e1); CVT(pk[2], e2); CVT(pk[3], e3); SBAR();
    EXP2(e0, 8); EXP2H(e0, 9); SBAR();
    zc = MFMA32(__builtin_bit_cast(bf16x8, pk), wc, zc); SBAR();
    EXP2(e1, 10); EXP2H(e1, 11); EXP2(e2, 12); EXP2H(e2, 13); EXP2(e3, 14); EXP2H(e3, 15); SBAR();
    ADD1(e0); ADD1(e1); ADD1(e2); ADD1(e3); SBAR();
    hook2(); SBAR();
    RCP2(e0); RCP2H(e0); RCP2(e1); RCP2H(e1); RCP2(e2); RCP2H(e2); RCP2(e3); RCP2H(e3); SBAR();
    hook3(); SBAR();
    MULX(e0, 8); MULX(e1, 10); MULX(e2, 12); MULX(e3, 14); SBAR();
    CVT(pkp[0], e0); CVT(pkp[1], e1); CVT(pkp[2], e2); CVT(pkp[3], e3); SBAR();
#undef EXP2
#undef EXP2H
#undef RCP2
#undef RCP2H
#undef ADD1
#undef MULX
#undef CVT
}

__global__ __launch_bounds__(K2_T) void k_egnn(const int* __restrict__ src, const int* __restrict__ dst,
                                               const uint16_t* __restrict__ ea, const uint16_t* __restrict__ h,
                                               const uint16_t* __restrict__ pos, const uint16_t* __restrict__ we,
                                               const uint16_t* __restrict__ wx, const uint16_t* __restrict__ wh,
                                               char* __restrict__ ws, uint16_t* __restrict__ out) {
    extern __shared__ __attribute__((aligned(16))) char sm[];
    const int tid = threadIdx.x, lane = tid & 63;
    const int wv = __builtin_amdgcn_readfirstlane(tid >> 6);
    const int xc = blockIdx.x & 7;
    const int b = 31 * xc + (xc < 2 ? xc : 2) + (blockIdx.x >> 3);
    const unsigned smb = (unsigned)(size_t)(LDS_AS char*)sm;
    asm volatile("" :: "s"(src), "s"(dst), "s"(ea), "s"(h), "s"(pos), "s"(we), "s"(wx), "s"(wh), "s"(ws), "s"(out));
    const char* Rec = ws + W_R;
    const char* Btab = ws + W_B;
    const char* Ptab = ws + W_P;
    STAMP_DECL
    STAMP(0);

    u32x2 ep;
    asm volatile("global_load_dwordx2 %0, %1, %2" : "=v"(ep) : "v"(0u), "s"(ws + W_E) : "memory");
    u32x4 d4 = {0, 0, 0, 0}, s4 = {0, 0, 0, 0};
    u32x2 a4 = {0, 0};
    unsigned sm0 = 0, sm1 = 0;
    const int r8l = lane >> 3, c8 = lane & 7;
    const int gnode = b * BN + (lane < BN ? lane : 0);
    if (wv < 10) {
        const int e0 = b * CE + 4 * tid;
        asm volatile("global_load_dwordx4 %0, %3, %4\n\tglobal_load_dwordx4 %1, %3, %5\n\tglobal_load_dwordx2 %2, %6, %7"
                     : "=&v"(d4), "=&v"(s4), "=&v"(a4)
                     : "v"(e0 * 4), "s"(dst), "s"(src), "v"(e0 * 2), "s"(ea) : "memory");
    } else if (wv == 10) {
        asm volatile("global_load_dword %0, %2, %3\n\tglobal_load_ushort %1, %2, %3 offset:4"
                     : "=&v"(sm0), "=&v"(sm1) : "v"(gnode * 6), "s"(pos) : "memory");
    } else {
        asm volatile("global_load_ushort %0, %1, %2" : "=v"(sm0) : "v"(lane * 2), "s"(wx) : "memory");
        lds_st(smb + A_CNT + lane * 16, u32x4{0, 0, 0, 0});
    }
    bar();
    {
        auto piece = [&](int p) {
            if (p < 5) dma1(smb + L_HIMG + p * 1024, h, (b * BN + 8 * p + r8l) * 128 + ((c8 ^ ((4 * p + (lane >> 4)) & 7)) * 16));
            else if (p < 21) dma1(smb + A_W + (p - 5) * 1024, we, (8 * (p - 5) + r8l) * 128 + ((c8 ^ ((r8l & 2) | (((p - 5) & 1) << 2))) * 16));
            else if (lane < 16) dma1(smb + L_AIMG + BN * 128, we, 128 * 128 + lane * 16);
        };
        piece(wv);
        if (wv < 10) piece(12 + wv);
    }
    ASTAMP(1);
    asm volatile("s_waitcnt vmcnt(0)" : "+v"(ep), "+v"(d4), "+v"(s4), "+v"(a4), "+v"(sm0), "+v"(sm1) :: "memory");
    ASTAMP(2);
    const unsigned e_lo = __builtin_amdgcn_readfirstlane(ep[0]), e_hi = __builtin_amdgcn_readfirstlane(ep[1]);
    const unsigned n_lo = e_lo + 1u, n_hi = e_hi + (n_lo == 0u ? 1u : 0u);
    const unsigned tag_w1 = (n_lo >> 8) | (n_hi << 24), tag_w0 = n_lo << 24;

    unsigned bk[4], ark[4], code = 0;
    if (wv < 10) {
#pragma unroll
        for (int i = 0; i < 4; ++i) {
            bk[i] = __umul24(d4[i], 52429u) >> 21;
            ark[i] = atomicAdd((unsigned*)(sm + A_CNT) + bk[i], 1u);
        }
#pragma unroll
        for (int i = 0; i < 4; ++i) {
            const unsigned dl = d4[i] - bk[i] * BN, w = win_of(dl);
            code |= (w * 8 + dl - win_start(w)) << (8 * i);
        }
    } else if (wv == 10) {
        const f32x4 p = {bf2f(sm0 & 0xffffu), __uint_as_float(sm0 & 0xffff0000u), bf2f(sm1), 0.f};
        if (lane < 48) lds_st(smb + L_PD + lane * 16, lane < BN ? p : f32x4{0.f, 0.f, 0.f, 0.f});
        if (lane < BN) st_dev(Ptab, gnode * 16, __builtin_bit_cast(u32x4, p));
    } else {
        lds_st(smb + L_WX + lane * 4, bf2f(sm0) * NEG_LN2);
        if (b == 0) {
            const u32x4 z = {0, 0, 0, 0};
            st_dev(Ptab, NN * 16 + lane * 16, z);
            if (lane < 8) st_dev(Btab, NN * 128 + lane * 16, z);
        }
    }
    ASTAMP(3);
    bar();
    ASTAMP(4);

    u32x4 t_lo = {0, 0, 0, 0};
    if (wv == 11) {
        const u32x4 cn = lds_ld<u32x4>(smb + A_CNT + lane * 16);
        const u32x4 tr = {div3(cn[0] + 2), div3(cn[1] + 2), div3(cn[2] + 2), div3(cn[3] + 2)};
        const unsigned tot = tr[0] + tr[1] + tr[2] + tr[3];
        const unsigned in = scan64(tot), ex = in - tot;
        const u32x4 of = {ex, ex + tr[0], ex + tr[0] + tr[1], ex + tr[0] + tr[1] + tr[2]};
        lds_st(smb + A_OFF + lane * 16, of);
        if (lane == 63) lds_st(smb + A_TOT, in);
#pragma unroll
        for (int i = 0; i < 4; ++i) t_lo[i] = tag_w0 | (cn[i] << 12) | of[i];
    } else if (wv < 8) {
        const int ct = wv & 3, g = lane >> 4, l15 = lane & 15;
        const char* wimg = sm + A_W + (wv >> 2) * 8192;
        const bf16x8 a0 = wfrag16(wimg, 0, ct, lane), a1 = wfrag16(wimg, 1, ct, lane);
        bf16x8 hb[6];
#pragma unroll
        for (int nt = 0; nt < 3; ++nt) {
            hb[2 * nt] = nfrag16(sm + L_HIMG, 16 * nt + l15, g);
            hb[2 * nt + 1] = nfrag16(sm + L_HIMG, 16 * nt + l15, 4 + g);
        }
        SBAR();
        f32x4 acc[3];
#pragma unroll
        for (int nt = 0; nt < 3; ++nt) acc[nt] = MFMA16(a0, hb[2 * nt], (f32x4{0.f, 0.f, 0.f, 0.f}));
#pragma unroll
        for (int nt = 0; nt < 3; ++nt) acc[nt] = MFMA16(a1, hb[2 * nt + 1], acc[nt]);
#pragma unroll
        for (int nt = 0; nt < 3; ++nt) {
            const int node = 16 * nt + l15;
            const u32x2 o = {pk_bf16(acc[nt][0] * NEG_LOG2E, acc[nt][1] * NEG_LOG2E), pk_bf16(acc[nt][2] * NEG_LOG2E, acc[nt][3] * NEG_LOG2E)};
            const unsigned oa = wv < 4 ? L_AIMG + node * 128 + (16 * ct + 4 * g) * 2
                                       : A_O + node * 128 + 16 * ((2 * ct + (g >> 1)) ^ (node & 7)) + 8 * (g & 1);
            lds_st(smb + (nt < 2 || l15 < BN - 32 ? oa : (unsigned)(A_R + 16320 + (lane & 7) * 8)), o);
        }
    }
    ASTAMP(5);
    bar();
    ASTAMP(6);

    if (wv < 10) {
#pragma unroll
        for (int i = 0; i < 4; ++i) {
            const unsigned q = div3(ark[i]), j = ark[i] - 3 * q;
            const unsigned t = smb + A_R + (lds_ld<unsigned>(smb + A_OFF + bk[i] * 4) + q) * 16;
            const unsigned av = (i & 1) ? (a4[i >> 1] & 0xffff0000u) : (a4[i >> 1] << 16);
            lds_st(t + j * 4, s4[i] | av);
            lds_st(t + 12 + j, (unsigned char)(code >> (8 * i)));
        }
    } else {
#pragma clang loop unroll(disable)
        for (int x = tid - 640; x < BN * 8; x += 128) {
            const int node = x >> 3, ch = (x & 7) ^ (node & 7);
            st_dev(Btab, (b * BN + node) * 128 + ch * 16, lds_ld<u32x4>(smb + A_O + x * 16));
        }
    }
    bar();
    ASTAMP(7);
    {
        const unsigned ntot = lds_ld<unsigned>(smb + A_TOT);
        if ((unsigned)tid < ntot) st_dev(Rec, b * 16384 + tid * 16, lds_ld<u32x4>(smb + A_R + tid * 16));
        if ((unsigned)tid + K2_T < ntot) st_dev(Rec, b * 16384 + (tid + K2_T) * 16, lds_ld<u32x4>(smb + A_R + (tid + K2_T) * 16));
    }
    ASTAMP(8);
    {
        const u32x4 z = {0, 0, 0, 0};
        lds_st(smb + L_STAGE + wv * 2048 + lane * 16, z);
        lds_st(smb + L_STAGE + wv * 2048 + 1024 + lane * 16, z);
        const u32x4 n32 = {NN, NN, NN, NN}, n8 = {0x08080808u, 0x08080808u, 0x08080808u, 0x08080808u};
        lds_st(smb + L_R32 + tid * 16, n32);
        if (tid < RALL / 4 - K2_T) lds_st(smb + L_R32 + (K2_T + tid) * 16, n32);
        if (wv >= 1 && wv < 5) {
            if (tid - 64 < RALL / 16) lds_st(smb + L_R8 + (tid - 64) * 16, n8);
        } else if (wv == 5) {
            if (lane < 56) lds_st(smb + L_BINS + lane * 16, z);
        } else if (wv == 6) {
            if (lane < 48) lds_st(smb + L_AIMG + (BN + 2) * 128 + lane * 16, z);
        } else if (wv == 7) {
            const int n = lane >> 2, j = lane & 3;
            if (lane < 36) lds_st(smb + L_OH + lane * 4, (n < 8 && (n >> 1) == j) ? (0x3F80u << (16 * (n & 1))) : 0u);
        }
    }
    wait_vm0();
    ASTAMP(9);
    bar();
    if (wv == 11) {
        st_dev(ws + W_T, b * 2048 + lane * 32, u32x4{t_lo[0], tag_w1, t_lo[1], tag_w1});
        st_dev(ws + W_T, b * 2048 + lane * 32 + 16, u32x4{t_lo[2], tag_w1, t_lo[3], tag_w1});
    }
    ASTAMP(10);

    const int r = lane & 31, hh = lane >> 5;
    const int win = (int)((0x545432231100ull >> (4 * wv)) & 7);
    const bool second = (0xC5A >> wv) & 1;
    const int wnode = win_start(win);
    char* part = sm + L_SLOT + wv * 4096;
    const int blk = (lane >> 4) & 1, q4 = (lane & 15) >> 2, p4 = lane & 3;
    bf16x8 i0, i1, e0, e1;
#pragma unroll
    for (int j = 0; j < 8; ++j) {
        i0[j] = (r == 8 * hh + j) ? (short)0x3F80 : (short)0;
        i1[j] = (r == 16 + 8 * hh + j) ? (short)0x3F80 : (short)0;
    }
    {
        const int row = hh ? BN + q4 : wnode + q4;
        const char* ab = sm + L_AIMG + row * 128 + (16 * blk + 4 * p4) * 2;
        e0 = cat8(tr_read(ab), tr_read(ab + 512));
        e1 = cat8(tr_read(ab + 64), tr_read(ab + 64 + 512));
    }
    unsigned aad[4], kk[4];
    {
        const int rho = 8 * (r & 3) + (r >> 2);
        const int fsw = (r & 3) ^ (((r >> 3) & 1) << 2);
#pragma unroll
        for (int s = 0; s < 4; ++s) aad[s] = smb + L_SLOT + wv * 4096 + rho * 128 + 16 * ((2 * s + hh) ^ fsw);
#pragma unroll
        for (int i = 0; i < 4; ++i) kk[i] = 16 * ((lane & 7) ^ i ^ (((lane >> 4) & 1) << 2)) + (3072 - 1024 * i);
    }
    const char* gB = Btab - 3072;
    const unsigned slot_a = smb + L_SLOT + wv * 4096;
    const unsigned pbuf_a = smb + L_PB + wv * 1024;
    unsigned src_a = smb + L_R32 + (lane >> 3) * 16, rs_a = smb + L_R32 + lane * 4, rn_a = smb + L_R8 + lane;
    unsigned stage_r = smb + L_STAGE + wv * 2048 + r * 64;
    unsigned trb_a = smb + L_STAGE + wv * 2048 + (4 * hh + q4) * 64 + blk * 32 + p4 * 8;
    const unsigned pdw = smb + L_PD + wnode * 16;
    unsigned oh_a = smb + L_OH;
    f32x16 z0, z1;
#pragma unroll
    for (int i = 0; i < 16; ++i) { z0[i] = 0.f; z1[i] = 0.f; }
    PIN(i0); PIN(i1); PIN(e0); PIN(e1); PIN(z0); PIN(z1);
    PIN(aad[0]); PIN(aad[1]); PIN(aad[2]); PIN(aad[3]); PIN(kk[0]); PIN(kk[1]); PIN(kk[2]); PIN(kk[3]);
    PIN(src_a); PIN(rs_a); PIN(rn_a); PIN(stage_r); PIN(trb_a); PIN(oh_a);

#pragma clang loop unroll(disable)
    for (int q = wv; q < 16; q += K2_W)
        dma1(smb + L_WH + q * 1024, wh, (8 * q + r8l) * 128 + ((c8 ^ ((r8l & 2) | ((q & 1) << 2))) * 16));
    unsigned tw;
    asm volatile("global_load_dword %0, %1, %2" : "=v"(tw)
                 : "v"(lane == 0 ? b * (BN * 128) : (NN * 128 + b * (BN * 6)) & ~3), "s"(out) : "memory");

    const int l3 = (lane * 171) >> 9, sub = lane - 3 * l3;
    const int cid = 21 * wv + l3;
    const bool cok = lane < 63 && cid < NCH;
    const int cidc = cok ? cid : 0;
    unsigned tval = 0;
    bool lost = true;
#pragma clang loop unroll(disable)
    for (int spin = 0; spin < (1 << 16); ++spin) {
        u32x2 tv;
        asm volatile("global_load_dwordx2 %0, %2, %3 sc1\n\ts_waitcnt vmcnt(0)"
                     : "=&v"(tv), "+v"(tw) : "v"((unsigned)W_T + cidc * 2048 + b * 8), "s"(ws) : "memory");
        tval = tv[0];
        if (__all(!cok || (tv[1] == tag_w1 && (tv[0] >> 24) == (tag_w0 >> 24)))) { lost = false; break; }
        __builtin_amdgcn_s_sleep(1);
    }
    PSTAMP(2);

    const unsigned cnt = (cok && !lost) ? ((tval >> 12) & 0xfffu) : 0u;
    const unsigned ntr = div3(cnt + 2);
    const unsigned incl = scan64(sub == 0 ? ntr : 0u);
    const unsigned mw = __builtin_amdgcn_readlane(incl, 63);
    const bool slow_w = mw > MAPCAP;
    const unsigned map_a = smb + L_SLOT + wv * 4096;
    QSTAMP(1);
    if (!slow_w) {
        unsigned g = (cidc * 1024 + (tval & 0xfffu) + sub) | ((cnt - 3 * sub) << 20);
        unsigned mp = map_a + (incl - ntr + sub) * 4;
#pragma clang loop unroll(disable) vectorize(disable) interleave(disable)
        for (unsigned i = sub; i < ntr; i += 3) { lds_st(mp, g); mp += 12; g += 3 - (9u << 20); }
    }
    PSTAMP(3);
    const unsigned mwe = slow_w ? 0u : mw;
    unsigned left[2];
    u32x4 rec[2];
#pragma unroll
    for (int k = 0; k < 2; ++k) {
        const unsigned m = lds_ld<unsigned>(map_a + (64 * k + lane) * 4);
        const bool ok = (unsigned)(64 * k + lane) < mwe;
        left[k] = ok ? m >> 20 : 0u;
        const unsigned go = ok ? (m & 0xfffffu) * 16 : 0u;
        asm volatile("global_load_dwordx4 %0, %1, %2 sc1" : "=v"(rec[k]) : "v"(go), "s"(Rec) : "memory");
    }
    if (lane == 0) lds_st(smb + L_WF + wv * 4, lost ? 2u : slow_w ? 1u : 0u);
    PSTAMP(4);
    asm volatile("s_waitcnt vmcnt(0)" : "+v"(rec[0]), "+v"(rec[1]) :: "memory");
    PSTAMP(5);
    unsigned bin[6], rk[6];
#pragma unroll
    for (int k = 0; k < 6; ++k) {
        const unsigned w = (rec[k / 3][3] >> (8 * (k % 3) + 3)) & 7u;
        bin[k] = (left[k / 3] > (unsigned)(k % 3) ? w : 6u) * 128 + (lane & 31) * 4;
        rk[k] = atomicAdd((unsigned*)(sm + L_BINS + bin[k]), 1u);
    }
    QSTAMP(3);
    PSTAMP(6);
    bar();
    PSTAMP(7);
    unsigned pf;
    {
        const unsigned part = blockIdx.x >> 3;
        const unsigned off = tid < 626 ? (unsigned)W_B + part * (313 * 128) + tid * 64
                                       : (unsigned)W_P + part * (40 * 128) + (tid < 706 ? tid - 626 : 0) * 64;
        asm volatile("global_load_dword %0, %1, %2 sc1" : "=v"(pf) : "v"(off), "s"(ws) : "memory");
    }

    if (b == 0 && tid == 7 * 64) *(u32x2*)(ws + W_E) = u32x2{n_lo, n_hi};
    const unsigned otab = smb + L_SLOT + wv * 4096 + 1024;
    int tw6[6];
    {
        const unsigned wfl = lds_ld<unsigned>(smb + L_WF + (lane < K2_W ? lane : 0) * 4);
        unsigned c3[3], in3[3];
#pragma unroll
        for (int k = 0; k < 3; ++k) c3[k] = lds_ld<unsigned>(smb + L_BINS + (2 * k + hh) * 128 + r * 4);
#pragma unroll
        for (int k = 0; k < 3; ++k) in3[k] = scan32(c3[k]);
#pragma unroll
        for (int k = 0; k < 3; ++k) {
            lds_st(otab + (2 * k + hh) * 128 + r * 4, (2 * k + hh) * WCAP + in3[k] - c3[k]);
            tw6[2 * k] = (__builtin_amdgcn_readlane(in3[k], 31) + 31) >> 5;
            tw6[2 * k + 1] = (__builtin_amdgcn_readlane(in3[k], 63) + 31) >> 5;
        }
        if (__any(wfl == 2)) { asm volatile("s_waitcnt vmcnt(0)" : "+v"(pf) :: "memory"); return; }
        tw6[0] |= __any(wfl != 0) ? 0x100 : 0;
    }
    constexpr int WT = WCAP / 32;
    const bool slow = tw6[0] > WT || tw6[1] > WT || tw6[2] > WT || tw6[3] > WT || tw6[4] > WT || tw6[5] > WT;
    PSTAMP(9);

    if (!slow) {
#pragma unroll
        for (int k = 0; k < 6; ++k) {
            const unsigned o = lds_ld<unsigned>(otab + bin[k]) + rk[k];
            const unsigned p = left[k / 3] > (unsigned)(k % 3) ? o : (unsigned)(RALL - 1);
            lds_st(smb + L_R32 + p * 4, rec[k / 3][k % 3]);
            lds_st(smb + L_R8 + p, (unsigned char)((rec[k / 3][3] >> (8 * (k % 3))) & 7u));
        }
        const int wstart = win * WCAP;
        const int l0 = tw6[0] + tw6[3] + tw6[4], l1 = tw6[0] + tw6[2] + tw6[5];
        const int l2 = tw6[1] + tw6[2] + tw6[4], l3s = tw6[1] + tw6[3] + tw6[5];
        const int wtiles = win == 0 ? tw6[0] : win == 1 ? tw6[1] : win == 2 ? tw6[2] : win == 3 ? tw6[3] : win == 4 ? tw6[4] : tw6[5];
        const int la = win == 0 ? l0 : win == 1 ? l2 : win == 2 ? l1 : win == 3 ? l3s : win == 4 ? l0 : l1;
        const int lb = win == 0 ? l1 : win == 1 ? l3s : win == 2 ? l2 : win == 3 ? l0 : win == 4 ? l2 : l3s;
        int first = (4 * wtiles - la + lb + 4) >> 3;
        first = first < 0 ? 0 : first > wtiles ? wtiles : first;
        const int ta = second ? first : 0, tb = second ? wtiles : first;

        bar();
        QSTAMP(5);
        PSTAMP(10);

        f32x16 xa, xb;
        QSTAMP(6);
        CSTAMP(12);

        if (ta < tb) {
            int base = wstart + 32 * ta;
            const int end = wstart + 32 * tb;
            bf16x8 a0, a1, a2, a3, ef, wf0, wf1;
            unsigned rs, rn, w0, w1, ua, vb;
            unsigned saddr;
            bool valid;
            u32x4 pkp = {0, 0, 0, 0};
#pragma unroll
            for (int j = 0; j < 8; ++j) wf1[j] = 0;

            u32x4 sn, exr;
            f32x4 pd, ps;
            auto read_src = [&](int ri) { sn = lds_ld<u32x4>(src_a + ri * 4); };
            auto issue_rows = [&]() {
                const unsigned v0 = mad16(sn[0], 128, kk[0]), v1 = mad16(sn[1], 128, kk[1]);
                const unsigned v2 = mad16(sn[2], 128, kk[2]), v3 = mad16(sn[3], 128, kk[3]);
                wait_lgkm0();
                dma4_dev(slot_a, gB, v0, v1, v2, v3);
            };
            auto read_pair = [&](int ri) {
                rs = lds_ld<unsigned>(rs_a + ri * 4);
                rn = lds_ld<unsigned char>(rn_a + ri);
            };
            auto issue_pos = [&]() { dma1_dev(pbuf_a, Ptab, mad16(rs, 16, 0u)); };
            auto read_pos = [&]() {
                pd = lds_ld<f32x4>(pdw + rn * 16);
                ps = lds_ld<f32x4>(pbuf_a + lane * 16);
            };
            auto geometry = [&]() {
                const float dx = pd[0] - ps[0], dy = pd[1] - ps[1], dz = pd[2] - ps[2];
                const float d2 = (dx * dx + dy * dy + dz * dz) * NEG_LOG2E;
                w0 = pk_bf16(1.0f, dx);
                w1 = pk_bf16(dy, dz);
                const unsigned ext = pk_bf16(d2, __uint_as_float(rs & 0xffff0000u) * NEG_LOG2E);
                const auto sw = __builtin_amdgcn_permlane32_swap(oh_a + rn * 16, ext, false, false);
                ua = sw[0];
                vb = sw[1];
                saddr = stage_r + rn * 8;
                PIN(saddr); PIN(w0); PIN(w1);
                valid = rn < 8;
            };
            auto read_rows = [&]() {
                a0 = lds_ld<bf16x8>(aad[0]); a1 = lds_ld<bf16x8>(aad[1]);
                a2 = lds_ld<bf16x8>(aad[2]); a3 = lds_ld<bf16x8>(aad[3]);
            };
            auto read_ext = [&](unsigned u) { exr = lds_ld<u32x4>(hh ? oh_a : u); };
            auto fix_ext = [&](unsigned u) {
                u32x4 ex = exr;
                ex[0] = hh ? u : ex[0];
                ef = __builtin_bit_cast(bf16x8, ex);
                PIN(ef);
            };
            auto stage_weights = [&](bool wr) {
                if (wr) lds_st(saddr, u32x2{w0, w1});
                wf0 = cat8(tr_read_a(trb_a), tr_read_a(trb_a + 512));
                wf1 = cat8(tr_read_a(trb_a + 1024), tr_read_a(trb_a + 1536));
                if (wr) lds_st(saddr, u32x2{0, 0});
            };
            auto nop = [&]() {};

            if (wv >= 8) __builtin_amdgcn_s_sleep(10);
            else if (wv >= 4) __builtin_amdgcn_s_sleep(5);
            read_pair(base);
            read_src(base);
            issue_pos();
            issue_rows();
            if (base + 32 < end) read_src(base + 32);
            PSTAMP(11);
            asm volatile("s_waitcnt vmcnt(4)" : "+v"(pf) :: "memory");
            read_pos();
            geometry();
            read_ext(ua);
            wait_vm0();
            PSTAMP(12);
            read_rows();
            if (base + 32 < end) issue_rows();
            fix_ext(ua);
            {
                f32x16 zero;
#pragma unroll
                for (int i = 0; i < 16; ++i) zero[i] = 0.f;
                xa = MFMA32(a0, i0, zero);
                xa = MFMA32(a1, i1, xa);
                xa = MFMA32(ef, e0, xa);
            }
            SBAR();
            PSTAMP(13);
            auto set_prio = [&](int rem) {
                if (rem >= 192) __builtin_amdgcn_s_setprio(3);
                else if (rem >= 128) __builtin_amdgcn_s_setprio(2);
                else if (rem >= 64) __builtin_amdgcn_s_setprio(1);
                else __builtin_amdgcn_s_setprio(0);
            };
            while (true) {
                set_prio(end - base);
                half_tile(xa, xb, a2, a3, ef, e1, i0, i1, z1, pkp, wf1, z0, wf0,
                          [&]() { stage_weights(valid && hh == 0); read_src(base + 64); read_pair(base + 64); },
                          [&]() { wait_vm0(); read_rows(); read_ext(vb); },
                          nop);
                half_tile(xb, xa, a0, a1, ef, e0, i0, i1, z0, pkp, wf1, z1, wf0,
                          [&]() { fix_ext(vb); if (base + 64 < end) issue_rows(); issue_pos(); }, nop, nop);
                if (base + 32 >= end) break;
                half_tile(xa, xb, a2, a3, ef, e1, i0, i1, z1, pkp, wf1, z0, wf0,
                          [&]() { stage_weights(valid && hh == 1); read_src(base + 96); },
                          [&]() { wait_vm0(); read_rows(); read_pos(); },
                          [&]() { geometry(); read_ext(ua); });
                half_tile(xb, xa, a0, a1, ef, e0, i0, i1, z0, pkp, wf1, z1, wf0,
                          [&]() { fix_ext(ua); if (base + 96 < end) issue_rows(); }, nop, nop);
                base += 64;
                if (base >= end) break;
            }
            z1 = MFMA32(__builtin_bit_cast(bf16x8, pkp), wf1, z1);
        }
        asm volatile("s_waitcnt vmcnt(0)" : "+v"(pf) :: "memory");
        CSTAMP(13);
        QSTAMP(7);
        if ((lane & 3) == 0) {
            const int n = r >> 2;
#pragma unroll
            for (int g = 0; g < 4; ++g) {
                *(f32x4*)(part + n * PROW + (8 * g + 4 * hh) * 4) = f32x4{z0[4 * g], z0[4 * g + 1], z0[4 * g + 2], z0[4 * g + 3]};
                *(f32x4*)(part + n * PROW + (32 + 8 * g + 4 * hh) * 4) = f32x4{z1[4 * g], z1[4 * g + 1], z1[4 * g + 2], z1[4 * g + 3]};
            }
        }
        float cs0 = 0.f, cs1 = 0.f;
#pragma unroll
        for (int g = 0; g < 4; ++g) {
            const f32x4 w0 = *(const f32x4*)(sm + L_WX + (8 * g + 4 * hh) * 4);
            const f32x4 w1 = *(const f32x4*)(sm + L_WX + (32 + 8 * g + 4 * hh) * 4);
#pragma unroll
            for (int i = 0; i < 4; ++i) { cs0 += z0[4 * g + i] * w0[i]; cs1 += z1[4 * g + i] * w1[i]; }
        }
        const float csh = cs0 + cs1;
        const auto cw = __builtin_amdgcn_permlane32_swap(__float_as_uint(csh), __float_as_uint(csh), false, false);
        const float cst = __uint_as_float(cw[0]) + __uint_as_float(cw[1]);
        if (hh == 0) ((float*)(part + PPOS))[r] = cst;
    } else {
        __builtin_amdgcn_fence(__ATOMIC_ACQUIRE, "agent");
        asm volatile("s_waitcnt vmcnt(0)" : "+v"(pf) :: "memory");
        {
            const u32x4 z = {0, 0, 0, 0};
            *(u32x4*)(part + lane * 16) = z;
            *(u32x4*)(part + 1024 + lane * 16) = z;
            *(u32x4*)(part + 2048 + lane * 16) = z;
        }
        bar();
        const float wd = bf2f(we[128 * 64 + lane]), wa = bf2f(we[129 * 64 + lane]);
        const float wxl = ((const float*)(sm + L_WX))[lane];
        for (int ci = wv; ci < NCH; ci += K2_W) {
            const unsigned tv = *(const unsigned*)(ws + W_T + ci * 2048 + b * 8);
            const unsigned g0 = ci * 1024 + (tv & 0xfffu), cn = (tv >> 12) & 0xfffu;
            for (unsigned i = 0; i < cn; ++i) {
                const unsigned* tp = (const unsigned*)(Rec + (size_t)(g0 + div3(i)) * 16);
                const unsigned j = i - 3 * div3(i);
                const unsigned rc = tp[j], cd = (tp[3] >> (8 * j)) & 0xffu;
                const unsigned s = rc & 0xffffu, w = cd >> 3, n = cd & 7, dl = win_start(w) + n;
                const f32x4 ps = *(const f32x4*)(Ptab + s * 16);
                const f32x4 pd = *(const f32x4*)(sm + L_PD + dl * 16);
                const float dx = pd[0] - ps[0], dy = pd[1] - ps[1], dz = pd[2] - ps[2];
                const unsigned e = pk_bf16((dx * dx + dy * dy + dz * dz) * NEG_LOG2E, __uint_as_float(rc & 0xffff0000u) * NEG_LOG2E);
                const unsigned dxy = pk_bf16(dx, dy), dz0 = pk_bf16(dz, 0.f);
                float y = bf2f(((const uint16_t*)(sm + L_AIMG))[dl * 64 + lane]) + bf2f(((const uint16_t*)Btab)[s * 64 + lane]);
                y += bf2f(e & 0xffffu) * wd + bf2f(e >> 16) * wa;
                const float m = bf2f(pk_bf16(y * rcp(1.0f + ex2(y)), 0.f) & 0xffffu);
                char* tgt = sm + L_SLOT + wave_a(w) * 4096;
                atomicAdd((float*)(tgt + n * PROW) + lane, m);
                float t = m * wxl;
                for (int o = 32; o > 0; o >>= 1) t += __shfl_xor(t, o);
                if (lane < 3) {
                    const float dv = lane == 0 ? bf2f(dxy & 0xffffu) : lane == 1 ? bf2f(dxy >> 16) : bf2f(dz0 & 0xffffu);
                    atomicAdd((float*)(tgt + PPOS) + 4 * n + 1 + lane, dv * t);
                }
            }
        }
    }
    const int ct = wv & 3, g = lane >> 4, node = 16 * (wv >> 2) + (lane & 15), nodec = node < BN ? node : BN - 1;
    bf16x8 wfr[4];
#pragma unroll
    for (int s = 0; s < 4; ++s) wfr[s] = wfrag16(sm + L_WH, s, ct, lane);
    f32x4 acc = {0.f, 0.f, 0.f, 0.f};
    acc = MFMA16(wfr[0], nfrag16(sm + L_HIMG, nodec, g), acc);
    acc = MFMA16(wfr[1], nfrag16(sm + L_HIMG, nodec, 4 + g), acc);
    const u32x2 hv = *(const u32x2*)(sm + L_HIMG + nodec * 128 + 16 * ((2 * ct + (g >> 1)) ^ ((nodec >> 1) & 7)) + 8 * (g & 1));
    const unsigned nwin = win_of(nodec);
    const char* pb = sm + L_SLOT + wave_a(nwin) * 4096 + (nodec - win_start(nwin)) * PROW + g * 32;
    const int pd2 = (wave_b(nwin) - wave_a(nwin)) * 4096;
    const int px = (wv - 10) * 64 + lane, pxc = (px >= 0 && px < 3 * BN) ? px : 0;
    const int pnl = (pxc * 171) >> 9, pcc = pxc - 3 * pnl;
    const unsigned pw = win_of(pnl), ppo = smb + L_SLOT + PPOS + (4 * (pnl - win_start(pw)) + 1 + pcc) * 4;
    const unsigned ppa = ppo + wave_a(pw) * 4096, ppb = ppo + wave_b(pw) * 4096;
    const float p0 = lds_ld<float>(smb + L_PD + pnl * 16 + pcc * 4);
    QSTAMP(8);
    bar();
    QSTAMP(9);
    const float ps0 = lds_ld<float>(ppa), ps1 = lds_ld<float>(ppb);
#pragma unroll
    for (int s = 0; s < 2; ++s) {
        const f32x4 m0 = (*(const f32x4*)(pb + s * 128) + *(const f32x4*)(pb + s * 128 + pd2)) * NEG_LN2;
        const f32x4 m1 = (*(const f32x4*)(pb + s * 128 + 16) + *(const f32x4*)(pb + s * 128 + 16 + pd2)) * NEG_LN2;
        acc = MFMA16(wfr[2 + s], as_frag(pk_bf16(m0[0], m0[1]), pk_bf16(m0[2], m0[3]), pk_bf16(m1[0], m1[1]), pk_bf16(m1[2], m1[3])), acc);
    }
    if (wv >= 10 && px < 3 * BN) out[NN * 64 + (b * BN + pnl) * 3 + pcc] = (uint16_t)(pk_bf16(p0 + ps0 + ps1, 0.f) & 0xffffu);
    QSTAMP(10);
    if (node < BN) {
        const float o0 = bf2f(hv[0] & 0xffffu) + silu_f(acc[0]), o1 = bf2f(hv[0] >> 16) + silu_f(acc[1]);
        const float o2 = bf2f(hv[1] & 0xffffu) + silu_f(acc[2]), o3 = bf2f(hv[1] >> 16) + silu_f(acc[3]);
        st_nt(out + (b * BN + node) * 64 + 16 * ct + 4 * g, u32x2{pk_bf16(o0, o1), pk_bf16(o2, o3)});
    }
    QSTAMP(11);
    STAMP_OUT(1, ws);
}

}

extern "C" void kernel_launch(void* const* d_in, const int* in_sizes, int n_in, void* d_out, int out_size, void* d_ws,
                              size_t ws_size, hipStream_t stream) {
    using namespace egnn;
    const uint16_t* h = (const uint16_t*)d_in[0];
    const uint16_t* pos = (const uint16_t*)d_in[1];
    const uint16_t* ea = (const uint16_t*)d_in[2];
    const int* src = (const int*)d_in[3];
    const int* dst = (const int*)d_in[4];
    const uint16_t* we = (const uint16_t*)d_in[5];
    const uint16_t* wx = (const uint16_t*)d_in[6];
    const uint16_t* wh = (const uint16_t*)d_in[7];
    (void)hipFuncSetAttribute((const void*)k_egnn, hipFuncAttributeMaxDynamicSharedMemorySize, L_END);
    k_egnn<<<NBK, K2_T, L_END, stream>>>(src, dst, ea, h, pos, we, wx, wh, (char*)d_ws, (uint16_t*)d_out);
}
```

```cpp
#include <hip/hip_runtime.h>
#include <stdint.h>

namespace egnn {

typedef short    bf16x8 __attribute__((ext_vector_type(8)));
typedef short    s16x4  __attribute__((ext_vector_type(4)));
typedef float    f32x16 __attribute__((ext_vector_type(16)));
typedef float    f32x4  __attribute__((ext_vector_type(4)));
typedef float    f32x2  __attribute__((ext_vector_type(2)));
typedef __bf16   bf16x2 __attribute__((ext_vector_type(2)));
typedef unsigned u32x4  __attribute__((ext_vector_type(4)));
typedef unsigned u32x2  __attribute__((ext_vector_type(2)));
#define LDS_AS __attribute__((address_space(3)))
#define DI __device__ __forceinline__
#define SBAR() __builtin_amdgcn_sched_barrier(0)
#define PIN(x) asm volatile("" : "+v"(x))
#define MFMA32(a, b, c) __builtin_amdgcn_mfma_f32_32x32x16_bf16(a, b, c, 0, 0, 0)
#define MFMA16(a, b, c) __builtin_amdgcn_mfma_f32_16x16x32_bf16(a, b, c, 0, 0, 0)

constexpr int NN = 10000;
constexpr int NE = 640000;
constexpr int BN = 40;
constexpr int NBK = 250;
constexpr int CE = 2560;
constexpr int NCH = 250;
constexpr float NEG_LOG2E = -1.4426950408889634f;
constexpr float NEG_LN2 = -0.6931471805599453f;

constexpr size_t W_E   = 0;
constexpr size_t W_T   = 4096;
constexpr size_t W_R   = 1u << 20;
constexpr size_t W_B   = (5u << 20) + (512u << 10);
constexpr size_t W_P   = 7u << 20;

DI unsigned win_of(unsigned dl) { return ((dl * 37u) >> 8) + (dl == 34u ? 1u : 0u); }
DI unsigned win_start(unsigned w) { return 7u * w - (w == 5u ? 1u : 0u); }

DI int wave_a(unsigned w) { return (0x987520u >> (4 * w)) & 15; }
DI int wave_b(unsigned w) { return (0xBA4631u >> (4 * w)) & 15; }

DI float bf2f(unsigned v16) { return __uint_as_float(v16 << 16); }
DI unsigned pk_bf16(float lo, float hi) {
    f32x2 x = {lo, hi};
    return __builtin_bit_cast(unsigned, __builtin_convertvector(x, bf16x2));
}
DI float silu_f(float x) {
    return x * __builtin_amdgcn_rcpf(1.0f + __builtin_amdgcn_exp2f(x * NEG_LOG2E));
}
DI s16x4 tr_read(const char* p) {
    return __builtin_amdgcn_ds_read_tr16_b64_v4i16((LDS_AS s16x4*)p);
}
DI s16x4 tr_read_a(unsigned a) { return __builtin_amdgcn_ds_read_tr16_b64_v4i16((LDS_AS s16x4*)(size_t)a); }
DI bf16x8 cat8(s16x4 a, s16x4 b) { return __builtin_shufflevector(a, b, 0, 1, 2, 3, 4, 5, 6, 7); }
DI void wait_vm0() { asm volatile("s_waitcnt vmcnt(0)" ::: "memory"); }
DI void wait_lgkm0() { asm volatile("s_waitcnt lgkmcnt(0)" ::: "memory"); }
DI void bar() { asm volatile("s_waitcnt lgkmcnt(0)\n\ts_barrier" ::: "memory"); }

DI void dma1(unsigned ldsaddr, const void* gbase, unsigned voff) {
    unsigned keep;
    asm volatile("s_mov_b32 %0, m0\n\ts_mov_b32 m0, %1\n\ts_nop 0\n\t"
                 "global_load_lds_dwordx4 %2, %3\n\t"
                 "s_mov_b32 m0, %0"
                 : "=&s"(keep) : "s"(ldsaddr), "v"(voff), "s"(gbase) : "memory");
}
DI void dma1_dev(unsigned ldsaddr, const void* gbase, unsigned voff) {
    unsigned keep;
    asm volatile("s_mov_b32 %0, m0\n\ts_mov_b32 m0, %1\n\ts_nop 0\n\t"
                 "global_load_lds_dwordx4 %2, %3 sc1\n\t"
                 "s_mov_b32 m0, %0"
                 : "=&s"(keep) : "s"(ldsaddr), "v"(voff), "s"(gbase) : "memory");
}
DI void dma4_dev(unsigned ldsaddr, const void* gbase, unsigned v0, unsigned v1, unsigned v2, unsigned v3) {
    unsigned keep;
    asm volatile("s_mov_b32 %0, m0\n\ts_mov_b32 m0, %1\n\ts_nop 0\n\t"
                 "global_load_lds_dwordx4 %2, %6 sc1\n\t"
                 "global_load_lds_dwordx4 %3, %6 offset:1024 sc1\n\t"
                 "global_load_lds_dwordx4 %4, %6 offset:2048 sc1\n\t"
                 "global_load_lds_dwordx4 %5, %6 offset:3072 sc1\n\t"
                 "s_mov_b32 m0, %0"
                 : "=&s"(keep) : "s"(ldsaddr), "v"(v0), "v"(v1), "v"(v2), "v"(v3), "s"(gbase) : "memory");
}
template <typename T> DI void st_nt(void* p, T v) { __builtin_nontemporal_store(v, (T*)p); }
DI void st_dev(const char* base, unsigned off, u32x4 v) {
    asm volatile("global_store_dwordx4 %0, %1, %2 sc1\n\ts_nop 1" :: "v"(off), "v"(v), "s"(base) : "memory");
}
DI unsigned div3(unsigned x) { return __umul24(x, 43691u) >> 17; }
DI unsigned mad16(unsigned x, unsigned m, unsigned a) {
    unsigned r;
    asm("v_mad_u32_u16 %0, %1, %2, %3" : "=v"(r) : "v"(x), "s"(m), "v"(a));
    return r;
}
template <typename T> DI T lds_ld(unsigned a) { return *(const LDS_AS T*)(size_t)a; }
template <typename T> DI void lds_st(unsigned a, T v) { *(LDS_AS T*)(size_t)a = v; }
template <int CTRL, int ROWMASK> DI unsigned dpp_add(unsigned x) {
    return x + (unsigned)__builtin_amdgcn_update_dpp(0, (int)x, CTRL, ROWMASK, 0xf, true);
}
DI unsigned scan32(unsigned x) {
    x = dpp_add<0x111, 0xf>(x); x = dpp_add<0x112, 0xf>(x); x = dpp_add<0x114, 0xf>(x); x = dpp_add<0x118, 0xf>(x);
    x = dpp_add<0x142, 0xa>(x);
    return x;
}
DI unsigned scan64(unsigned x) { x = scan32(x); x = dpp_add<0x143, 0xc>(x); return x; }
DI int wswz(int k) { return (k & 2) | ((k >> 1) & 4); }
DI bf16x8 wfrag16(const char* img, int s, int ct, int lane) {
    const int g = lane >> 4, q = (lane & 15) >> 2, p = lane & 3;
    const int k0 = 32 * s + 8 * g + q, k1 = k0 + 4, chn = 2 * ct + (p >> 1);
    return cat8(tr_read(img + k0 * 128 + 16 * (chn ^ wswz(k0)) + 8 * (p & 1)),
                tr_read(img + k1 * 128 + 16 * (chn ^ wswz(k1)) + 8 * (p & 1)));
}
DI bf16x8 nfrag16(const char* img, int node, int chunk) {
    return *(const bf16x8*)(img + node * 128 + 16 * (chunk ^ ((node >> 1) & 7)));
}

#define ASTAMP(i)
#define PSTAMP(i)
#define QSTAMP(i)
#define STAMP_DECL
#define STAMP(i)
#define CSTAMP(i)
#define STAMP_OUT(kern, wsp)

constexpr int K2_W = 12, K2_T = 64 * K2_W;
constexpr int WCAP = 576;
constexpr int RCAP = 6 * WCAP;
constexpr int L_SLOT = 0;
constexpr int L_STAGE = 49152;
constexpr int RALL = RCAP + 128;
constexpr int L_R32 = 73728;
constexpr int L_R8 = L_R32 + RALL * 4;
constexpr int L_AIMG = L_R8 + RALL;
constexpr int L_HIMG = L_AIMG + 6144;
constexpr int L_WH = L_HIMG + 6144;
constexpr int L_PD = L_WH + 16384;
constexpr int L_OH = L_PD + 768;
constexpr int L_WX = L_OH + 256;
constexpr int L_BINS = L_WX + 256;
constexpr int L_WF = L_BINS + 896;
constexpr int L_PB = L_WF + 64;
constexpr int L_END = L_PB + K2_W * 1024;
constexpr int A_CNT = L_SLOT, A_OFF = A_CNT + 1024, A_TOT = A_OFF + 1024, A_R = A_TOT + 16, A_W = A_R + 16384, A_O = A_W + 16384;
static_assert(A_O + BN * 128 <= L_STAGE, "phase A scratch");
constexpr unsigned MAPCAP = 128;
constexpr int PROW = 272, PPOS = 8 * PROW;

DI bf16x8 as_frag(unsigned a, unsigned b, unsigned c, unsigned d) {
    u32x4 v = {a, b, c, d};
    return __builtin_bit_cast(bf16x8, v);
}
DI float ex2(float x) { return __builtin_amdgcn_exp2f(x); }
DI float rcp(float x) { return __builtin_amdgcn_rcpf(x); }

template <class H1, class H2, class H3>
DI void half_tile(const f32x16& xc, f32x16& xn, const bf16x8& aA, const bf16x8& aB, const bf16x8& ef, const bf16x8& eB,
                  const bf16x8& i0, const bf16x8& i1, f32x16& zp, u32x4& pkp, const bf16x8& wp, f32x16& zc,
                  const bf16x8& wc, H1&& hook1, H2&& hook2, H3&& hook3) {
    f32x16 zero;
#pragma unroll
    for (int i = 0; i < 16; ++i) zero[i] = 0.f;
    f32x2 e0, e1, e2, e3;
    u32x4 pk;
#define EXP2(e, i) do { e[0] = ex2(xc[i]); PIN(e[0]); } while (0)
#define EXP2H(e, i) do { e[1] = ex2(xc[i]); PIN(e[1]); } while (0)
#define RCP2(e) do { e[0] = rcp(e[0]); PIN(e[0]); } while (0)
#define RCP2H(e) do { e[1] = rcp(e[1]); PIN(e[1]); } while (0)
#define PKMODE 0
#define ADD1(e) do { e = e + 1.0f; PIN(e); } while (0)
#define MULX(e, i) do { e = e * f32x2{xc[i], xc[i + 1]}; PIN(e); } while (0)
#define CVT(d, e) do { d = __builtin_bit_cast(unsigned, __builtin_convertvector(e, bf16x2)); PIN(d); } while (0)
    xn = MFMA32(aA, i0, zero); SBAR();
    EXP2(e0, 0); EXP2H(e0, 1); EXP2(e1, 2); SBAR();
    zp = MFMA32(__builtin_bit_cast(bf16x8, pkp), wp, zp); SBAR();
    EXP2H(e1, 3); EXP2(e2, 4); EXP2H(e2, 5); EXP2(e3, 6); EXP2H(e3, 7); SBAR();
    ADD1(e0); ADD1(e1); ADD1(e2); ADD1(e3); SBAR();
    hook1(); SBAR();
    xn = MFMA32(aB, i1, xn); SBAR();
    RCP2(e0); RCP2H(e0); RCP2(e1); SBAR();
    xn = MFMA32(ef, eB, xn); SBAR();
    RCP2H(e1); RCP2(e2); RCP2H(e2); RCP2(e3); RCP2H(e3); SBAR();
    MULX(e0, 0); MULX(e1, 2); MULX(e2, 4); MULX(e3, 6); SBAR();
    CVT(pk[0], e0); CVT(pk[1], e1); CVT(pk[2], e2); CVT(pk[3], e3); SBAR();
    EXP2(e0, 8); EXP2H(e0, 9); SBAR();
    zc = MFMA32(__builtin_bit_cast(bf16x8, pk), wc, zc); SBAR();
    EXP2(e1, 10); EXP2H(e1, 11); EXP2(e2, 12); EXP2H(e2, 13); EXP2(e3, 14); EXP2H(e3, 15); SBAR();
    ADD1(e0); ADD1(e1); ADD1(e2); ADD1(e3); SBAR();
    hook2(); SBAR();
    RCP2(e0); RCP2H(e0); RCP2(e1); RCP2H(e1); RCP2(e2); RCP2H(e2); RCP2(e3); RCP2H(e3); SBAR();
    hook3(); SBAR();
    MULX(e0, 8); MULX(e1, 10); MULX(e2, 12); MULX(e3, 14); SBAR();
    CVT(pkp[0], e0); CVT(pkp[1], e1); CVT(pkp[2], e2); CVT(pkp[3], e3); SBAR();
#undef EXP2
#undef EXP2H
#undef RCP2
#undef RCP2H
#undef ADD1
#undef MULX
#undef CVT
}

__global__ __launch_bounds__(K2_T) void k_egnn(const int* __restrict__ src, const int* __restrict__ dst,
                                               const uint16_t* __restrict__ ea, const uint16_t* __restrict__ h,
                                               const uint16_t* __restrict__ pos, const uint16_t* __restrict__ we,
                                               const uint16_t* __restrict__ wx, const uint16_t* __restrict__ wh,
                                               char* __restrict__ ws, uint16_t* __restrict__ out) {
    extern __shared__ __attribute__((aligned(16))) char sm[];
    const int tid = threadIdx.x, lane = tid & 63;
    const int wv = __builtin_amdgcn_readfirstlane(tid >> 6);
    const int xc = blockIdx.x & 7;
    const int b = 31 * xc + (xc < 2 ? xc : 2) + (blockIdx.x >> 3);
    const unsigned smb = (unsigned)(size_t)(LDS_AS char*)sm;
    asm volatile("" :: "s"(src), "s"(dst), "s"(ea), "s"(h), "s"(pos), "s"(we), "s"(wx), "s"(wh), "s"(ws), "s"(out));
    const char* Rec = ws + W_R;
    const char* Btab = ws + W_B;
    const char* Ptab = ws + W_P;
    STAMP_DECL
    STAMP(0);

    u32x2 ep;
    asm volatile("global_load_dwordx2 %0, %1, %2" : "=v"(ep) : "v"(0u), "s"(ws + W_E) : "memory");
    u32x4 d4 = {0, 0, 0, 0}, s4 = {0, 0, 0, 0};
    u32x2 a4 = {0, 0};
    unsigned sm0 = 0, sm1 = 0;
    const int r8l = lane >> 3, c8 = lane & 7;
    const int gnode = b * BN + (lane < BN ? lane : 0);
    if (wv < 10) {
        const int e0 = b * CE + 4 * tid;
        asm volatile("global_load_dwordx4 %0, %3, %4\n\tglobal_load_dwordx4 %1, %3, %5\n\tglobal_load_dwordx2 %2, %6, %7"
                     : "=&v"(d4), "=&v"(s4), "=&v"(a4)
                     : "v"(e0 * 4), "s"(dst), "s"(src), "v"(e0 * 2), "s"(ea) : "memory");
    } else if (wv == 10) {
        asm volatile("global_load_dword %0, %2, %3\n\tglobal_load_ushort %1, %2, %3 offset:4"
                     : "=&v"(sm0), "=&v"(sm1) : "v"(gnode * 6), "s"(pos) : "memory");
    } else {
        asm volatile("global_load_ushort %0, %1, %2" : "=v"(sm0) : "v"(lane * 2), "s"(wx) : "memory");
        lds_st(smb + A_CNT + lane * 16, u32x4{0, 0, 0, 0});
    }
    bar();
    {
        auto piece = [&](int p) {
            if (p < 5) dma1(smb + L_HIMG + p * 1024, h, (b * BN + 8 * p + r8l) * 128 + ((c8 ^ ((4 * p + (lane >> 4)) & 7)) * 16));
            else if (p < 21) dma1(smb + A_W + (p - 5) * 1024, we, (8 * (p - 5) + r8l) * 128 + ((c8 ^ ((r8l & 2) | (((p - 5) & 1) << 2))) * 16));
            else if (lane < 16) dma1(smb + L_AIMG + BN * 128, we, 128 * 128 + lane * 16);
        };
        piece(wv);
        if (wv < 10) piece(12 + wv);
    }
    ASTAMP(1);
    asm volatile("s_waitcnt vmcnt(0)" : "+v"(ep), "+v"(d4), "+v"(s4), "+v"(a4), "+v"(sm0), "+v"(sm1) :: "memory");
    ASTAMP(2);
    const unsigned e_lo = __builtin_amdgcn_readfirstlane(ep[0]), e_hi = __builtin_amdgcn_readfirstlane(ep[1]);
    const unsigned n_lo = e_lo + 1u, n_hi = e_hi + (n_lo == 0u ? 1u : 0u);
    const unsigned tag_w1 = (n_lo >> 8) | (n_hi << 24), tag_w0 = n_lo << 24;

    unsigned bk[4], ark[4], code = 0;
    if (wv < 10) {
#pragma unroll
        for (int i = 0; i < 4; ++i) {
            bk[i] = __umul24(d4[i], 52429u) >> 21;
            ark[i] = atomicAdd((unsigned*)(sm + A_CNT) + bk[i], 1u);
        }
#pragma unroll
        for (int i = 0; i < 4; ++i) {
            const unsigned dl = d4[i] - bk[i] * BN, w = win_of(dl);
            code |= (w * 8 + dl - win_start(w)) << (8 * i);
        }
    } else if (wv == 10) {
        const f32x4 p = {bf2f(sm0 & 0xffffu), __uint_as_float(sm0 & 0xffff0000u), bf2f(sm1), 0.f};
        if (lane < 48) lds_st(smb + L_PD + lane * 16, lane < BN ? p : f32x4{0.f, 0.f, 0.f, 0.f});
        if (lane < BN) st_dev(Ptab, gnode * 16, __builtin_bit_cast(u32x4, p));
    } else {
        lds_st(smb + L_WX + lane * 4, bf2f(sm0) * NEG_LN2);
        if (b == 0) {
            const u32x4 z = {0, 0, 0, 0};
            st_dev(Ptab, NN * 16 + lane * 16, z);
            if (lane < 8) st_dev(Btab, NN * 128 + lane * 16, z);
        }
    }
    ASTAMP(3);
    bar();
    ASTAMP(4);

    u32x4 t_lo = {0, 0, 0, 0};
    if (wv == 11) {
        const u32x4 cn = lds_ld<u32x4>(smb + A_CNT + lane * 16);
        const u32x4 tr = {div3(cn[0] + 2), div3(cn[1] + 2), div3(cn[2] + 2), div3(cn[3] + 2)};
        const unsigned tot = tr[0] + tr[1] + tr[2] + tr[3];
        const unsigned in = scan64(tot), ex = in - tot;
        const u32x4 of = {ex, ex + tr[0], ex + tr[0] + tr[1], ex + tr[0] + tr[1] + tr[2]};
        lds_st(smb + A_OFF + lane * 16, of);
        if (lane == 63) lds_st(smb + A_TOT, in);
#pragma unroll
        for (int i = 0; i < 4; ++i) t_lo[i] = tag_w0 | (cn[i] << 12) | of[i];
    } else if (wv < 8) {
        const int ct = wv & 3, g = lane >> 4, l15 = lane & 15;
        const char* wimg = sm + A_W + (wv >> 2) * 8192;
        const bf16x8 a0 = wfrag16(wimg, 0, ct, lane), a1 = wfrag16(wimg, 1, ct, lane);
        bf16x8 hb[6];
#pragma unroll
        for (int nt = 0; nt < 3; ++nt) {
            hb[2 * nt] = nfrag16(sm + L_HIMG, 16 * nt + l15, g);
            hb[2 * nt + 1] = nfrag16(sm + L_HIMG, 16 * nt + l15, 4 + g);
        }
        SBAR();
        f32x4 acc[3];
#pragma unroll
        for (int nt = 0; nt < 3; ++nt) acc[nt] = MFMA16(a0, hb[2 * nt], (f32x4{0.f, 0.f, 0.f, 0.f}));
#pragma unroll
        for (int nt = 0; nt < 3; ++nt) acc[nt] = MFMA16(a1, hb[2 * nt + 1], acc[nt]);
#pragma unroll
        for (int nt = 0; nt < 3; ++nt) {
            const int node = 16 * nt + l15;
            const u32x2 o = {pk_bf16(acc[nt][0] * NEG_LOG2E, acc[nt][1] * NEG_LOG2E), pk_bf16(acc[nt][2] * NEG_LOG2E, acc[nt][3] * NEG_LOG2E)};
            const unsigned oa = wv < 4 ? L_AIMG + node * 128 + (16 * ct + 4 * g) * 2
                                       : A_O + node * 128 + 16 * ((2 * ct + (g >> 1)) ^ (node & 7)) + 8 * (g & 1);
            lds_st(smb + (nt < 2 || l15 < BN - 32 ? oa : (unsigned)(A_R + 16320 + (lane & 7) * 8)), o);
        }
    }
    ASTAMP(5);
    bar();
    ASTAMP(6);

    if (wv < 10) {
#pragma unroll
        for (int i = 0; i < 4; ++i) {
            const unsigned q = div3(ark[i]), j = ark[i] - 3 * q;
            const unsigned t = smb + A_R + (lds_ld<unsigned>(smb + A_OFF + bk[i] * 4) + q) * 16;
            const unsigned av = (i & 1) ? (a4[i >> 1] & 0xffff0000u) : (a4[i >> 1] << 16);
            lds_st(t + j * 4, s4[i] | av);
            lds_st(t + 12 + j, (unsigned char)(code >> (8 * i)));
        }
    } else {
#pragma clang loop unroll(disable)
        for (int x = tid - 640; x < BN * 8; x += 128) {
            const int node = x >> 3, ch = (x & 7) ^ (node & 7);
            st_dev(Btab, (b * BN + node) * 128 + ch * 16, lds_ld<u32x4>(smb + A_O + x * 16));
        }
    }
    bar();
    ASTAMP(7);
    {
        const unsigned ntot = lds_ld<unsigned>(smb + A_TOT);
        if ((unsigned)tid < ntot) st_dev(Rec, b * 16384 + tid * 16, lds_ld<u32x4>(smb + A_R + tid * 16));
        if ((unsigned)tid + K2_T < ntot) st_dev(Rec, b * 16384 + (tid + K2_T) * 16, lds_ld<u32x4>(smb + A_R + (tid + K2_T) * 16));
    }
    ASTAMP(8);
    {
        const u32x4 z = {0, 0, 0, 0};
        lds_st(smb + L_STAGE + wv * 2048 + lane * 16, z);
        lds_st(smb + L_STAGE + wv * 2048 + 1024 + lane * 16, z);
        const u32x4 n32 = {NN, NN, NN, NN}, n8 = {0x08080808u, 0x08080808u, 0x08080808u, 0x08080808u};
        lds_st(smb + L_R32 + tid * 16, n32);
        if (tid < RALL / 4 - K2_T) lds_st(smb + L_R32 + (K2_T + tid) * 16, n32);
        if (wv >= 1 && wv < 5) {
            if (tid - 64 < RALL / 16) lds_st(smb + L_R8 + (tid - 64) * 16, n8);
        } else if (wv == 5) {
            if (lane < 56) lds_st(smb + L_BINS + lane * 16, z);
        } else if (wv == 6) {
            if (lane < 48) lds_st(smb + L_AIMG + (BN + 2) * 128 + lane * 16, z);
        } else if (wv == 7) {
            const int n = lane >> 2, j = lane & 3;
            if (lane < 36) lds_st(smb + L_OH + lane * 4, (n < 8 && (n >> 1) == j) ? (0x3F80u << (16 * (n & 1))) : 0u);
        }
    }
    wait_vm0();
    ASTAMP(9);
    bar();
    if (wv == 11) {
        st_dev(ws + W_T, b * 2048 + lane * 32, u32x4{t_lo[0], tag_w1, t_lo[1], tag_w1});
        st_dev(ws + W_T, b * 2048 + lane * 32 + 16, u32x4{t_lo[2], tag_w1, t_lo[3], tag_w1});
    }
    ASTAMP(10);

    const int r = lane & 31, hh = lane >> 5;
    const int win = (int)((0x545432231100ull >> (4 * wv)) & 7);
    const bool second = (0xC5A >> wv) & 1;
    const int wnode = win_start(win);
    char* part = sm + L_SLOT + wv * 4096;
    const int blk = (lane >> 4) & 1, q4 = (lane & 15) >> 2, p4 = lane & 3;
    bf16x8 i0, i1, e0, e1;
#pragma unroll
    for (int j = 0; j < 8; ++j) {
        i0[j] = (r == 8 * hh + j) ? (short)0x3F80 : (short)0;
        i1[j] = (r == 16 + 8 * hh + j) ? (short)0x3F80 : (short)0;
    }
    {
        const int row = hh ? BN + q4 : wnode + q4;
        const char* ab = sm + L_AIMG + row * 128 + (16 * blk + 4 * p4) * 2;
        e0 = cat8(tr_read(ab), tr_read(ab + 512));
        e1 = cat8(tr_read(ab + 64), tr_read(ab + 64 + 512));
    }
    unsigned aad[4], kk[4];
    {
        const int rho = 8 * (r & 3) + (r >> 2);
        const int fsw = (r & 3) ^ (((r >> 3) & 1) << 2);
#pragma unroll
        for (int s = 0; s < 4; ++s) aad[s] = smb + L_SLOT + wv * 4096 + rho * 128 + 16 * ((2 * s + hh) ^ fsw);
#pragma unroll
        for (int i = 0; i < 4; ++i) kk[i] = 16 * ((lane & 7) ^ i ^ (((lane >> 4) & 1) << 2)) + (3072 - 1024 * i);
    }
    const char* gB = Btab - 3072;
    const unsigned slot_a = smb + L_SLOT + wv * 4096;
    const unsigned pbuf_a = smb + L_PB + wv * 1024;
    unsigned src_a = smb + L_R32 + (lane >> 3) * 16, rs_a = smb + L_R32 + lane * 4, rn_a = smb + L_R8 + lane;
    unsigned stage_r = smb + L_STAGE + wv * 2048 + r * 64;
    unsigned trb_a = smb + L_STAGE + wv * 2048 + (4 * hh + q4) * 64 + blk * 32 + p4 * 8;
    const unsigned pdw = smb + L_PD + wnode * 16;
    unsigned oh_a = smb + L_OH;
    f32x16 z0, z1;
#pragma unroll
    for (int i = 0; i < 16; ++i) { z0[i] = 0.f; z1[i] = 0.f; }
    PIN(i0); PIN(i1); PIN(e0); PIN(e1); PIN(z0); PIN(z1);
    PIN(aad[0]); PIN(aad[1]); PIN(aad[2]); PIN(aad[3]); PIN(kk[0]); PIN(kk[1]); PIN(kk[2]); PIN(kk[3]);
    PIN(src_a); PIN(rs_a); PIN(rn_a); PIN(stage_r); PIN(trb_a); PIN(oh_a);

#pragma clang loop unroll(disable)
    for (int q = wv; q < 16; q += K2_W)
        dma1(smb + L_WH + q * 1024, wh, (8 * q + r8l) * 128 + ((c8 ^ ((r8l & 2) | ((q & 1) << 2))) * 16));
    unsigned tw;
    asm volatile("global_load_dword %0, %1, %2" : "=v"(tw)
                 : "v"(lane == 0 ? b * (BN * 128) : (NN * 128 + b * (BN * 6)) & ~3), "s"(out) : "memory");

    const int l3 = (lane * 171) >> 9, sub = lane - 3 * l3;
    const int cid = 21 * wv + l3;
    const bool cok = lane < 63 && cid < NCH;
    const int cidc = cok ? cid : 0;
    unsigned tval = 0;
    bool lost = true;
#pragma clang loop unroll(disable)
    for (int spin = 0; spin < (1 << 16); ++spin) {
        u32x2 tv;
        asm volatile("global_load_dwordx2 %0, %2, %3 sc1\n\ts_waitcnt vmcnt(0)"
                     : "=&v"(tv), "+v"(tw) : "v"((unsigned)W_T + cidc * 2048 + b * 8), "s"(ws) : "memory");
        tval = tv[0];
        if (__all(!cok || (tv[1] == tag_w1 && (tv[0] >> 24) == (tag_w0 >> 24)))) { lost = false; break; }
        __builtin_amdgcn_s_sleep(1);
    }
    PSTAMP(2);

    const unsigned cnt = (cok && !lost) ? ((tval >> 12) & 0xfffu) : 0u;
    const unsigned ntr = div3(cnt + 2);
    const unsigned incl = scan64(sub == 0 ? ntr : 0u);
    const unsigned mw = __builtin_amdgcn_readlane(incl, 63);
    const bool slow_w = mw > MAPCAP;
    const unsigned map_a = smb + L_SLOT + wv * 4096;
    QSTAMP(1);
    if (!slow_w) {
        unsigned g = (cidc * 1024 + (tval & 0xfffu) + sub) | ((cnt - 3 * sub) << 20);
        unsigned mp = map_a + (incl - ntr + sub) * 4;
#pragma clang loop unroll(disable) vectorize(disable) interleave(disable)
        for (unsigned i = sub; i < ntr; i += 3) { lds_st(mp, g); mp += 12; g += 3 - (9u << 20); }
    }
    PSTAMP(3);
    const unsigned mwe = slow_w ? 0u : mw;
    unsigned left[2];
    u32x4 rec[2];
#pragma unroll
    for (int k = 0; k < 2; ++k) {
        const unsigned m = lds_ld<unsigned>(map_a + (64 * k + lane) * 4);
        const bool ok = (unsigned)(64 * k + lane) < mwe;
        left[k] = ok ? m >> 20 : 0u;
        const unsigned go = ok ? (m & 0xfffffu) * 16 : 0u;
        asm volatile("global_load_dwordx4 %0, %1, %2 sc1" : "=v"(rec[k]) : "v"(go), "s"(Rec) : "memory");
    }
    if (lane == 0) lds_st(smb + L_WF + wv * 4, lost ? 2u : slow_w ? 1u : 0u);
    PSTAMP(4);
    asm volatile("s_waitcnt vmcnt(0)" : "+v"(rec[0]), "+v"(rec[1]) :: "memory");
    PSTAMP(5);
    unsigned bin[6], rk[6];
#pragma unroll
    for (int k = 0; k < 6; ++k) {
        const unsigned w = (rec[k / 3][3] >> (8 * (k % 3) + 3)) & 7u;
        bin[k] = (left[k / 3] > (unsigned)(k % 3) ? w : 6u) * 128 + (lane & 31) * 4;
        rk[k] = atomicAdd((unsigned*)(sm + L_BINS + bin[k]), 1u);
    }
    QSTAMP(3);
    PSTAMP(6);
    bar();
    PSTAMP(7);
    unsigned pf;
    {
        const unsigned part = blockIdx.x >> 3;
        const unsigned off = tid < 626 ? (unsigned)W_B + part * (313 * 128) + tid * 64
                                       : (unsigned)W_P + part * (40 * 128) + (tid < 706 ? tid - 626 : 0) * 64;
        asm volatile("global_load_dword %0, %1, %2 sc1" : "=v"(pf) : "v"(off), "s"(ws) : "memory");
    }

    if (b == 0 && tid == 7 * 64) *(u32x2*)(ws + W_E) = u32x2{n_lo, n_hi};
    const unsigned otab = smb + L_SLOT + wv * 4096 + 1024;
    int tw6[6];
    {
        const unsigned wfl = lds_ld<unsigned>(smb + L_WF + (lane < K2_W ? lane : 0) * 4);
        unsigned c3[3], in3[3];
#pragma unroll
        for (int k = 0; k < 3; ++k) c3[k] = lds_ld<unsigned>(smb + L_BINS + (2 * k + hh) * 128 + r * 4);
#pragma unroll
        for (int k = 0; k < 3; ++k) in3[k] = scan32(c3[k]);
#pragma unroll
        for (int k = 0; k < 3; ++k) {
            lds_st(otab + (2 * k + hh) * 128 + r * 4, (2 * k + hh) * WCAP + in3[k] - c3[k]);
            tw6[2 * k] = (__builtin_amdgcn_readlane(in3[k], 31) + 31) >> 5;
            tw6[2 * k + 1] = (__builtin_amdgcn_readlane(in3[k], 63) + 31) >> 5;
        }
        if (__any(wfl == 2)) { asm volatile("s_waitcnt vmcnt(0)" : "+v"(pf) :: "memory"); return; }
        tw6[0] |= __any(wfl != 0) ? 0x100 : 0;
    }
    constexpr int WT = WCAP / 32;
    const bool slow = tw6[0] > WT || tw6[1] > WT || tw6[2] > WT || tw6[3] > WT || tw6[4] > WT || tw6[5] > WT;
    PSTAMP(9);

    if (!slow) {
#pragma unroll
        for (int k = 0; k < 6; ++k) {
            const unsigned o = lds_ld<unsigned>(otab + bin[k]) + rk[k];
            const unsigned p = left[k / 3] > (unsigned)(k % 3) ? o : (unsigned)(RALL - 1);
            lds_st(smb + L_R32 + p * 4, rec[k / 3][k % 3]);
            lds_st(smb + L_R8 + p, (unsigned char)((rec[k / 3][3] >> (8 * (k % 3))) & 7u));
        }
        const int wstart = win * WCAP;
        const int l0 = tw6[0] + tw6[3] + tw6[4], l1 = tw6[0] + tw6[2] + tw6[5];
        const int l2 = tw6[1] + tw6[2] + tw6[4], l3s = tw6[1] + tw6[3] + tw6[5];
        const int wtiles = win == 0 ? tw6[0] : win == 1 ? tw6[1] : win == 2 ? tw6[2] : win == 3 ? tw6[3] : win == 4 ? tw6[4] : tw6[5];
        const int la = win == 0 ? l0 : win == 1 ? l2 : win == 2 ? l1 : win == 3 ? l3s : win == 4 ? l0 : l1;
        const int lb = win == 0 ? l1 : win == 1 ? l3s : win == 2 ? l2 : win == 3 ? l0 : win == 4 ? l2 : l3s;
        int first = (4 * wtiles - la + lb + 4) >> 3;
        first = first < 0 ? 0 : first > wtiles ? wtiles : first;
        const int ta = second ? first : 0, tb = second ? wtiles : first;

        bar();
        QSTAMP(5);
        PSTAMP(10);

        f32x16 xa, xb;
        QSTAMP(6);
        CSTAMP(12);

        if (ta < tb) {
            int base = wstart + 32 * ta;
            const int end = wstart + 32 * tb;
            bf16x8 a0, a1, a2, a3, ef, wf0, wf1;
            unsigned rs, rn, w0, w1, ua, vb;
            unsigned saddr;
            bool valid;
            u32x4 pkp = {0, 0, 0, 0};
#pragma unroll
            for (int j = 0; j < 8; ++j) wf1[j] = 0;

            u32x4 sn, exr;
            f32x4 pd, ps;
            auto read_src = [&](int ri) { sn = lds_ld<u32x4>(src_a + ri * 4); };
            auto issue_rows = [&]() {
                const unsigned v0 = mad16(sn[0], 128, kk[0]), v1 = mad16(sn[1], 128, kk[1]);
                const unsigned v2 = mad16(sn[2], 128, kk[2]), v3 = mad16(sn[3], 128, kk[3]);
                wait_lgkm0();
                dma4_dev(slot_a, gB, v0, v1, v2, v3);
            };
            auto read_pair = [&](int ri) {
                rs = lds_ld<unsigned>(rs_a + ri * 4);
                rn = lds_ld<unsigned char>(rn_a + ri);
            };
            auto issue_pos = [&]() { dma1_dev(pbuf_a, Ptab, mad16(rs, 16, 0u)); };
            auto read_pos = [&]() {
                pd = lds_ld<f32x4>(pdw + rn * 16);
                ps = lds_ld<f32x4>(pbuf_a + lane * 16);
            };
            auto geometry = [&]() {
                const float dx = pd[0] - ps[0], dy = pd[1] - ps[1], dz = pd[2] - ps[2];
                const float d2 = (dx * dx + dy * dy + dz * dz) * NEG_LOG2E;
                w0 = pk_bf16(1.0f, dx);
                w1 = pk_bf16(dy, dz);
                const unsigned ext = pk_bf16(d2, __uint_as_float(rs & 0xffff0000u) * NEG_LOG2E);
                const auto sw = __builtin_amdgcn_permlane32_swap(oh_a + rn * 16, ext, false, false);
                ua = sw[0];
                vb = sw[1];
                saddr = stage_r + rn * 8;
                PIN(saddr); PIN(w0); PIN(w1);
                valid = rn < 8;
            };
            auto read_rows = [&]() {
                a0 = lds_ld<bf16x8>(aad[0]); a1 = lds_ld<bf16x8>(aad[1]);
                a2 = lds_ld<bf16x8>(aad[2]); a3 = lds_ld<bf16x8>(aad[3]);
            };
            auto read_ext = [&](unsigned u) { exr = lds_ld<u32x4>(hh ? oh_a : u); };
            auto fix_ext = [&](unsigned u) {
                u32x4 ex = exr;
                ex[0] = hh ? u : ex[0];
                ef = __builtin_bit_cast(bf16x8, ex);
                PIN(ef);
            };
            auto stage_weights = [&](bool wr) {
                if (wr) lds_st(saddr, u32x2{w0, w1});
                wf0 = cat8(tr_read_a(trb_a), tr_read_a(trb_a + 512));
                wf1 = cat8(tr_read_a(trb_a + 1024), tr_read_a(trb_a + 1536));
                if (wr) lds_st(saddr, u32x2{0, 0});
            };
            auto nop = [&]() {};

            if (wv >= 8) __builtin_amdgcn_s_sleep(10);
            else if (wv >= 4) __builtin_amdgcn_s_sleep(5);
            read_pair(base);
            read_src(base);
            issue_pos();
            issue_rows();
            if (base + 32 < end) read_src(base + 32);
            PSTAMP(11);
            asm volatile("s_waitcnt vmcnt(4)" : "+v"(pf) :: "memory");
            read_pos();
            geometry();
            read_ext(ua);
            wait_vm0();
            PSTAMP(12);
            read_rows();
            if (base + 32 < end) issue_rows();
            fix_ext(ua);
            {
                f32x16 zero;
#pragma unroll
                for (int i = 0; i < 16; ++i) zero[i] = 0.f;
                xa = MFMA32(a0, i0, zero);
                xa = MFMA32(a1, i1, xa);
                xa = MFMA32(ef, e0, xa);
            }
            SBAR();
            PSTAMP(13);
            auto set_prio = [&](int rem) {
                if (rem >= 224) __builtin_amdgcn_s_setprio(3);
                else if (rem >= 160) __builtin_amdgcn_s_setprio(2);
                else if (rem >= 96) __builtin_amdgcn_s_setprio(1);
                else __builtin_amdgcn_s_setprio(0);
            };
            while (true) {
                set_prio(end - base);
                half_tile(xa, xb, a2, a3, ef, e1, i0, i1, z1, pkp, wf1, z0, wf0,
                          [&]() { stage_weights(valid && hh == 0); read_src(base + 64); read_pair(base + 64); },
                          [&]() { wait_vm0(); read_rows(); read_ext(vb); },
                          nop);
                half_tile(xb, xa, a0, a1, ef, e0, i0, i1, z0, pkp, wf1, z1, wf0,
                          [&]() { fix_ext(vb); if (base + 64 < end) issue_rows(); issue_pos(); }, nop, nop);
                if (base + 32 >= end) break;
                half_tile(xa, xb, a2, a3, ef, e1, i0, i1, z1, pkp, wf1, z0, wf0,
                          [&]() { stage_weights(valid && hh == 1); read_src(base + 96); },
                          [&]() { wait_vm0(); read_rows(); read_pos(); },
                          [&]() { geometry(); read_ext(ua); });
                half_tile(xb, xa, a0, a1, ef, e0, i0, i1, z0, pkp, wf1, z1, wf0,
                          [&]() { fix_ext(ua); if (base + 96 < end) issue_rows(); }, nop, nop);
                base += 64;
                if (base >= end) break;
            }
            z1 = MFMA32(__builtin_bit_cast(bf16x8, pkp), wf1, z1);
        }
        asm volatile("s_waitcnt vmcnt(0)" : "+v"(pf) :: "memory");
        CSTAMP(13);
        QSTAMP(7);
        if ((lane & 3) == 0) {
            const int n = r >> 2;
#pragma unroll
            for (int g = 0; g < 4; ++g) {
                *(f32x4*)(part + n * PROW + (8 * g + 4 * hh) * 4) = f32x4{z0[4 * g], z0[4 * g + 1], z0[4 * g + 2], z0[4 * g + 3]};
                *(f32x4*)(part + n * PROW + (32 + 8 * g + 4 * hh) * 4) = f32x4{z1[4 * g], z1[4 * g + 1], z1[4 * g + 2], z1[4 * g + 3]};
            }
        }
        float cs0 = 0.f, cs1 = 0.f;
#pragma unroll
        for (int g = 0; g < 4; ++g) {
            const f32x4 w0 = *(const f32x4*)(sm + L_WX + (8 * g + 4 * hh) * 4);
            const f32x4 w1 = *(const f32x4*)(sm + L_WX + (32 + 8 * g + 4 * hh) * 4);
#pragma unroll
            for (int i = 0; i < 4; ++i) { cs0 += z0[4 * g + i] * w0[i]; cs1 += z1[4 * g + i] * w1[i]; }
        }
        const float csh = cs0 + cs1;
        const auto cw = __builtin_amdgcn_permlane32_swap(__float_as_uint(csh), __float_as_uint(csh), false, false);
        const float cst = __uint_as_float(cw[0]) + __uint_as_float(cw[1]);
        if (hh == 0) ((float*)(part + PPOS))[r] = cst;
    } else {
        __builtin_amdgcn_fence(__ATOMIC_ACQUIRE, "agent");
        asm volatile("s_waitcnt vmcnt(0)" : "+v"(pf) :: "memory");
        {
            const u32x4 z = {0, 0, 0, 0};
            *(u32x4*)(part + lane * 16) = z;
            *(u32x4*)(part + 1024 + lane * 16) = z;
            *(u32x4*)(part + 2048 + lane * 16) = z;
        }
        bar();
        const float wd = bf2f(we[128 * 64 + lane]), wa = bf2f(we[129 * 64 + lane]);
        const float wxl = ((const float*)(sm + L_WX))[lane];
        for (int ci = wv; ci < NCH; ci += K2_W) {
            const unsigned tv = *(const unsigned*)(ws + W_T + ci * 2048 + b * 8);
            const unsigned g0 = ci * 1024 + (tv & 0xfffu), cn = (tv >> 12) & 0xfffu;
            for (unsigned i = 0; i < cn; ++i) {
                const unsigned* tp = (const unsigned*)(Rec + (size_t)(g0 + div3(i)) * 16);
                const unsigned j = i - 3 * div3(i);
                const unsigned rc = tp[j], cd = (tp[3] >> (8 * j)) & 0xffu;
                const unsigned s = rc & 0xffffu, w = cd >> 3, n = cd & 7, dl = win_start(w) + n;
                const f32x4 ps = *(const f32x4*)(Ptab + s * 16);
                const f32x4 pd = *(const f32x4*)(sm + L_PD + dl * 16);
                const float dx = pd[0] - ps[0], dy = pd[1] - ps[1], dz = pd[2] - ps[2];
                const unsigned e = pk_bf16((dx * dx + dy * dy + dz * dz) * NEG_LOG2E, __uint_as_float(rc & 0xffff0000u) * NEG_LOG2E);
                const unsigned dxy = pk_bf16(dx, dy), dz0 = pk_bf16(dz, 0.f);
                float y = bf2f(((const uint16_t*)(sm + L_AIMG))[dl * 64 + lane]) + bf2f(((const uint16_t*)Btab)[s * 64 + lane]);
                y += bf2f(e & 0xffffu) * wd + bf2f(e >> 16) * wa;
                const float m = bf2f(pk_bf16(y * rcp(1.0f + ex2(y)), 0.f) & 0xffffu);
                char* tgt = sm + L_SLOT + wave_a(w) * 4096;
                atomicAdd((float*)(tgt + n * PROW) + lane, m);
                float t = m * wxl;
                for (int o = 32; o > 0; o >>= 1) t += __shfl_xor(t, o);
                if (lane < 3) {
                    const float dv = lane == 0 ? bf2f(dxy & 0xffffu) : lane == 1 ? bf2f(dxy >> 16) : bf2f(dz0 & 0xffffu);
                    atomicAdd((float*)(tgt + PPOS) + 4 * n + 1 + lane, dv * t);
                }
            }
        }
    }
    const int ct = wv & 3, g = lane >> 4, node = 16 * (wv >> 2) + (lane & 15), nodec = node < BN ? node : BN - 1;
    bf16x8 wfr[4];
#pragma unroll
    for (int s = 0; s < 4; ++s) wfr[s] = wfrag16(sm + L_WH, s, ct, lane);
    f32x4 acc = {0.f, 0.f, 0.f, 0.f};
    acc = MFMA16(wfr[0], nfrag16(sm + L_HIMG, nodec, g), acc);
    acc = MFMA16(wfr[1], nfrag16(sm + L_HIMG, nodec, 4 + g), acc);
    const u32x2 hv = *(const u32x2*)(sm + L_HIMG + nodec * 128 + 16 * ((2 * ct + (g >> 1)) ^ ((nodec >> 1) & 7)) + 8 * (g & 1));
    const unsigned nwin = win_of(nodec);
    const char* pb = sm + L_SLOT + wave_a(nwin) * 4096 + (nodec - win_start(nwin)) * PROW + g * 32;
    const int pd2 = (wave_b(nwin) - wave_a(nwin)) * 4096;
    const int px = (wv - 10) * 64 + lane, pxc = (px >= 0 && px < 3 * BN) ? px : 0;
    const int pnl = (pxc * 171) >> 9, pcc = pxc - 3 * pnl;
    const unsigned pw = win_of(pnl), ppo = smb + L_SLOT + PPOS + (4 * (pnl - win_start(pw)) + 1 + pcc) * 4;
    const unsigned ppa = ppo + wave_a(pw) * 4096, ppb = ppo + wave_b(pw) * 4096;
    const float p0 = lds_ld<float>(smb + L_PD + pnl * 16 + pcc * 4);
    QSTAMP(8);
    bar();
    QSTAMP(9);
    const float ps0 = lds_ld<float>(ppa), ps1 = lds_ld<float>(ppb);
#pragma unroll
    for (int s = 0; s < 2; ++s) {
        const f32x4 m0 = (*(const f32x4*)(pb + s * 128) + *(const f32x4*)(pb + s * 128 + pd2)) * NEG_LN2;
        const f32x4 m1 = (*(const f32x4*)(pb + s * 128 + 16) + *(const f32x4*)(pb + s * 128 + 16 + pd2)) * NEG_LN2;
        acc = MFMA16(wfr[2 + s], as_frag(pk_bf16(m0[0], m0[1]), pk_bf16(m0[2], m0[3]), pk_bf16(m1[0], m1[1]), pk_bf16(m1[2], m1[3])), acc);
    }
    if (wv >= 10 && px < 3 * BN) out[NN * 64 + (b * BN + pnl) * 3 + pcc] = (uint16_t)(pk_bf16(p0 + ps0 + ps1, 0.f) & 0xffffu);
    QSTAMP(10);
    if (node < BN) {
        const float o0 = bf2f(hv[0] & 0xffffu) + silu_f(acc[0]), o1 = bf2f(hv[0] >> 16) + silu_f(acc[1]);
        const float o2 = bf2f(hv[1] & 0xffffu) + silu_f(acc[2]), o3 = bf2f(hv[1] >> 16) + silu_f(acc[3]);
        st_nt(out + (b * BN + node) * 64 + 16 * ct + 4 * g, u32x2{pk_bf16(o0, o1), pk_bf16(o2, o3)});
    }
    QSTAMP(11);
    STAMP_OUT(1, ws);
}

}

extern "C" void kernel_launch(void* const* d_in, const int* in_sizes, int n_in, void* d_out, int out_size, void* d_ws,
                              size_t ws_size, hipStream_t stream) {
    using namespace egnn;
    const uint16_t* h = (const uint16_t*)d_in[0];
    const uint16_t* pos = (const uint16_t*)d_in[1];
    const uint16_t* ea = (const uint16_t*)d_in[2];
    const int* src = (const int*)d_in[3];
    const int* dst = (const int*)d_in[4];
    const uint16_t* we = (const uint16_t*)d_in[5];
    const uint16_t* wx = (const uint16_t*)d_in[6];
    const uint16_t* wh = (const uint16_t*)d_in[7];
    d_ws = (char*)d_ws + (ws_size - (8u << 20));
    (void)hipFuncSetAttribute((const void*)k_egnn, hipFuncAttributeMaxDynamicSharedMemorySize, L_END);
    k_egnn<<<NBK, K2_T, L_END, stream>>>(src, dst, ea, h, pos, we, wx, wh, (char*)d_ws, (uint16_t*)d_out);
}
```

```cpp
#include <hip/hip_runtime.h>
#include <stdint.h>

namespace egnn {

typedef short    bf16x8 __attribute__((ext_vector_type(8)));
typedef short    s16x4  __attribute__((ext_vector_type(4)));
typedef float    f32x16 __attribute__((ext_vector_type(16)));
typedef float    f32x4  __attribute__((ext_vector_type(4)));
typedef float    f32x2  __attribute__((ext_vector_type(2)));
typedef __bf16   bf16x2 __attribute__((ext_vector_type(2)));
typedef unsigned u32x4  __attribute__((ext_vector_type(4)));
typedef unsigned u32x2  __attribute__((ext_vector_type(2)));
#define LDS_AS __attribute__((address_space(3)))
#define DI __device__ __forceinline__
#define SBAR() __builtin_amdgcn_sched_barrier(0)
#define PIN(x) asm volatile("" : "+v"(x))
#define MFMA32(a, b, c) __builtin_amdgcn_mfma_f32_32x32x16_bf16(a, b, c, 0, 0, 0)
#define MFMA16(a, b, c) __builtin_amdgcn_mfma_f32_16x16x32_bf16(a, b, c, 0, 0, 0)

constexpr int NN = 10000;
constexpr int NE = 640000;
constexpr int BN = 40;
constexpr int NBK = 250;
constexpr int CE = 2560;
constexpr int NCH = 250;
constexpr float NEG_LOG2E = -1.4426950408889634f;
constexpr float NEG_LN2 = -0.6931471805599453f;

constexpr size_t W_E   = 0;
constexpr size_t W_T   = 4096;
constexpr size_t W_R   = 1u << 20;
constexpr size_t W_B   = (5u << 20) + (512u << 10);
constexpr size_t W_P   = 7u << 20;

DI unsigned win_of(unsigned dl) { return ((dl * 37u) >> 8) + (dl == 34u ? 1u : 0u); }
DI unsigned win_start(unsigned w) { return 7u * w - (w == 5u ? 1u : 0u); }

DI int wave_a(unsigned w) { return (0x987520u >> (4 * w)) & 15; }
DI int wave_b(unsigned w) { return (0xBA4631u >> (4 * w)) & 15; }

DI float bf2f(unsigned v16) { return __uint_as_float(v16 << 16); }
DI unsigned pk_bf16(float lo, float hi) {
    f32x2 x = {lo, hi};
    return __builtin_bit_cast(unsigned, __builtin_convertvector(x, bf16x2));
}
DI float silu_f(float x) {
    return x * __builtin_amdgcn_rcpf(1.0f + __builtin_amdgcn_exp2f(x * NEG_LOG2E));
}
DI s16x4 tr_read(const char* p) {
    return __builtin_amdgcn_ds_read_tr16_b64_v4i16((LDS_AS s16x4*)p);
}
DI s16x4 tr_read_a(unsigned a) { return __builtin_amdgcn_ds_read_tr16_b64_v4i16((LDS_AS s16x4*)(size_t)a); }
DI bf16x8 cat8(s16x4 a, s16x4 b) { return __builtin_shufflevector(a, b, 0, 1, 2, 3, 4, 5, 6, 7); }
DI void wait_vm0() { asm volatile("s_waitcnt vmcnt(0)" ::: "memory"); }
DI void wait_lgkm0() { asm volatile("s_waitcnt lgkmcnt(0)" ::: "memory"); }
DI void bar() { asm volatile("s_waitcnt lgkmcnt(0)\n\ts_barrier" ::: "memory"); }

DI void dma1(unsigned ldsaddr, const void* gbase, unsigned voff) {
    unsigned keep;
    asm volatile("s_mov_b32 %0, m0\n\ts_mov_b32 m0, %1\n\ts_nop 0\n\t"
                 "global_load_lds_dwordx4 %2, %3\n\t"
                 "s_mov_b32 m0, %0"
                 : "=&s"(keep) : "s"(ldsaddr), "v"(voff), "s"(gbase) : "memory");
}
DI void dma1_dev(unsigned ldsaddr, const void* gbase, unsigned voff) {
    unsigned keep;
    asm volatile("s_mov_b32 %0, m0\n\ts_mov_b32 m0, %1\n\ts_nop 0\n\t"
                 "global_load_lds_dwordx4 %2, %3 sc1\n\t"
                 "s_mov_b32 m0, %0"
                 : "=&s"(keep) : "s"(ldsaddr), "v"(voff), "s"(gbase) : "memory");
}
DI void dma4_dev(unsigned ldsaddr, const void* gbase, unsigned v0, unsigned v1, unsigned v2, unsigned v3) {
    unsigned keep;
    asm volatile("s_mov_b32 %0, m0\n\ts_mov_b32 m0, %1\n\ts_nop 0\n\t"
                 "global_load_lds_dwordx4 %2, %6 sc1\n\t"
                 "global_load_lds_dwordx4 %3, %6 offset:1024 sc1\n\t"
                 "global_load_lds_dwordx4 %4, %6 offset:2048 sc1\n\t"
                 "global_load_lds_dwordx4 %5, %6 offset:3072 sc1\n\t"
                 "s_mov_b32 m0, %0"
                 : "=&s"(keep) : "s"(ldsaddr), "v"(v0), "v"(v1), "v"(v2), "v"(v3), "s"(gbase) : "memory");
}
template <typename T> DI void st_nt(void* p, T v) { __builtin_nontemporal_store(v, (T*)p); }
DI void st_dev(const char* base, unsigned off, u32x4 v) {
    asm volatile("global_store_dwordx4 %0, %1, %2 sc1\n\ts_nop 1" :: "v"(off), "v"(v), "s"(base) : "memory");
}
DI unsigned div3(unsigned x) { return __umul24(x, 43691u) >> 17; }
DI unsigned mad16(unsigned x, unsigned m, unsigned a) {
    unsigned r;
    asm("v_mad_u32_u16 %0, %1, %2, %3" : "=v"(r) : "v"(x), "s"(m), "v"(a));
    return r;
}
template <typename T> DI T lds_ld(unsigned a) { return *(const LDS_AS T*)(size_t)a; }
template <typename T> DI void lds_st(unsigned a, T v) { *(LDS_AS T*)(size_t)a = v; }
template <int CTRL, int ROWMASK> DI unsigned dpp_add(unsigned x) {
    return x + (unsigned)__builtin_amdgcn_update_dpp(0, (int)x, CTRL, ROWMASK, 0xf, true);
}
DI unsigned scan32(unsigned x) {
    x = dpp_add<0x111, 0xf>(x); x = dpp_add<0x112, 0xf>(x); x = dpp_add<0x114, 0xf>(x); x = dpp_add<0x118, 0xf>(x);
    x = dpp_add<0x142, 0xa>(x);
    return x;
}
DI unsigned scan64(unsigned x) { x = scan32(x); x = dpp_add<0x143, 0xc>(x); return x; }
DI int wswz(int k) { return (k & 2) | ((k >> 1) & 4); }
DI bf16x8 wfrag16(const char* img, int s, int ct, int lane) {
    const int g = lane >> 4, q = (lane & 15) >> 2, p = lane & 3;
    const int k0 = 32 * s + 8 * g + q, k1 = k0 + 4, chn = 2 * ct + (p >> 1);
    return cat8(tr_read(img + k0 * 128 + 16 * (chn ^ wswz(k0)) + 8 * (p & 1)),
                tr_read(img + k1 * 128 + 16 * (chn ^ wswz(k1)) + 8 * (p & 1)));
}
DI bf16x8 nfrag16(const char* img, int node, int chunk) {
    return *(const bf16x8*)(img + node * 128 + 16 * (chunk ^ ((node >> 1) & 7)));
}

#define ASTAMP(i)
#define PSTAMP(i)
#define QSTAMP(i)
#define STAMP_DECL
#define STAMP(i)
#define CSTAMP(i)
#define STAMP_OUT(kern, wsp)

constexpr int K2_W = 12, K2_T = 64 * K2_W;
constexpr int WCAP = 576;
constexpr int RCAP = 6 * WCAP;
constexpr int L_SLOT = 0;
constexpr int L_STAGE = 49152;
constexpr int RALL = RCAP + 128;
constexpr int L_R32 = 73728;
constexpr int L_R8 = L_R32 + RALL * 4;
constexpr int L_AIMG = L_R8 + RALL;
constexpr int L_HIMG = L_AIMG + 6144;
constexpr int L_WH = L_HIMG + 6144;
constexpr int L_PD = L_WH + 16384;
constexpr int L_OH = L_PD + 768;
constexpr int L_WX = L_OH + 256;
constexpr int L_BINS = L_WX + 256;
constexpr int L_WF = L_BINS + 896;
constexpr int L_RDY = L_WF + 64;
constexpr int L_PB = L_RDY + 64;
constexpr int L_END = L_PB + K2_W * 1024;
constexpr int A_CNT = L_SLOT, A_OFF = A_CNT + 1024, A_TOT = A_OFF + 1024, A_R = A_TOT + 16, A_W = A_R + 16384, A_O = A_W + 16384;
static_assert(A_O + BN * 128 <= L_STAGE, "phase A scratch");
constexpr unsigned MAPCAP = 128;
constexpr int PROW = 272, PPOS = 8 * PROW;

DI bf16x8 as_frag(unsigned a, unsigned b, unsigned c, unsigned d) {
    u32x4 v = {a, b, c, d};
    return __builtin_bit_cast(bf16x8, v);
}
DI float ex2(float x) { return __builtin_amdgcn_exp2f(x); }
DI float rcp(float x) { return __builtin_amdgcn_rcpf(x); }

template <class H1, class H2, class H3>
DI void half_tile(const f32x16& xc, f32x16& xn, const bf16x8& aA, const bf16x8& aB, const bf16x8& ef, const bf16x8& eB,
                  const bf16x8& i0, const bf16x8& i1, f32x16& zp, u32x4& pkp, const bf16x8& wp, f32x16& zc,
                  const bf16x8& wc, H1&& hook1, H2&& hook2, H3&& hook3) {
    f32x16 zero;
#pragma unroll
    for (int i = 0; i < 16; ++i) zero[i] = 0.f;
    f32x2 e0, e1, e2, e3;
    u32x4 pk;
#define EXP2(e, i) do { e[0] = ex2(xc[i]); PIN(e[0]); } while (0)
#define EXP2H(e, i) do { e[1] = ex2(xc[i]); PIN(e[1]); } while (0)
#define RCP2(e) do { e[0] = rcp(e[0]); PIN(e[0]); } while (0)
#define RCP2H(e) do { e[1] = rcp(e[1]); PIN(e[1]); } while (0)
#define PKMODE 0
#define ADD1(e) do { e = e + 1.0f; PIN(e); } while (0)
#define MULX(e, i) do { e = e * f32x2{xc[i], xc[i + 1]}; PIN(e); } while (0)
#define CVT(d, e) do { d = __builtin_bit_cast(unsigned, __builtin_convertvector(e, bf16x2)); PIN(d); } while (0)
    xn = MFMA32(aA, i0, zero); SBAR();
    EXP2(e0, 0); EXP2H(e0, 1); EXP2(e1, 2); SBAR();
    zp = MFMA32(__builtin_bit_cast(bf16x8, pkp), wp, zp); SBAR();
    EXP2H(e1, 3); EXP2(e2, 4); EXP2H(e2, 5); EXP2(e3, 6); EXP2H(e3, 7); SBAR();
    ADD1(e0); ADD1(e1); ADD1(e2); ADD1(e3); SBAR();
    hook1(); SBAR();
    xn = MFMA32(aB, i1, xn); SBAR();
    RCP2(e0); RCP2H(e0); RCP2(e1); SBAR();
    xn = MFMA32(ef, eB, xn); SBAR();
    RCP2H(e1); RCP2(e2); RCP2H(e2); RCP2(e3); RCP2H(e3); SBAR();
    MULX(e0, 0); MULX(e1, 2); MULX(e2, 4); MULX(e3, 6); SBAR();
    CVT(pk[0], e0); CVT(pk[1], e1); CVT(pk[2], e2); CVT(pk[3], e3); SBAR();
    EXP2(e0, 8); EXP2H(e0, 9); SBAR();
    zc = MFMA32(__builtin_bit_cast(bf16x8, pk), wc, zc); SBAR();
    EXP2(e1, 10); EXP2H(e1, 11); EXP2(e2, 12); EXP2H(e2, 13); EXP2(e3, 14); EXP2H(e3, 15); SBAR();
    ADD1(e0); ADD1(e1); ADD1(e2); ADD1(e3); SBAR();
    hook2(); SBAR();
    RCP2(e0); RCP2H(e0); RCP2(e1); RCP2H(e1); RCP2(e2); RCP2H(e2); RCP2(e3); RCP2H(e3); SBAR();
    hook3(); SBAR();
    MULX(e0, 8); MULX(e1, 10); MULX(e2, 12); MULX(e3, 14); SBAR();
    CVT(pkp[0], e0); CVT(pkp[1], e1); CVT(pkp[2], e2); CVT(pkp[3], e3); SBAR();
#undef EXP2
#undef EXP2H
#undef RCP2
#undef RCP2H
#undef ADD1
#undef MULX
#undef CVT
}

__global__ __launch_bounds__(K2_T) void k_egnn(const int* __restrict__ src, const int* __restrict__ dst,
                                               const uint16_t* __restrict__ ea, const uint16_t* __restrict__ h,
                                               const uint16_t* __restrict__ pos, const uint16_t* __restrict__ we,
                                               const uint16_t* __restrict__ wx, const uint16_t* __restrict__ wh,
                                               char* __restrict__ ws, uint16_t* __restrict__ out) {
    extern __shared__ __attribute__((aligned(16))) char sm[];
    const int tid = threadIdx.x, lane = tid & 63;
    const int wv = __builtin_amdgcn_readfirstlane(tid >> 6);
    const int xc = blockIdx.x & 7;
    const int b = 31 * xc + (xc < 2 ? xc : 2) + (blockIdx.x >> 3);
    const unsigned smb = (unsigned)(size_t)(LDS_AS char*)sm;
    asm volatile("" :: "s"(src), "s"(dst), "s"(ea), "s"(h), "s"(pos), "s"(we), "s"(wx), "s"(wh), "s"(ws), "s"(out));
    const char* Rec = ws + W_R;
    const char* Btab = ws + W_B;
    const char* Ptab = ws + W_P;
    STAMP_DECL
    STAMP(0);

    u32x2 ep;
    asm volatile("global_load_dwordx2 %0, %1, %2" : "=v"(ep) : "v"(0u), "s"(ws + W_E) : "memory");
    u32x4 d4 = {0, 0, 0, 0}, s4 = {0, 0, 0, 0};
    u32x2 a4 = {0, 0};
    unsigned sm0 = 0, sm1 = 0;
    const int r8l = lane >> 3, c8 = lane & 7;
    const int gnode = b * BN + (lane < BN ? lane : 0);
    if (wv < 10) {
        const int e0 = b * CE + 4 * tid;
        asm volatile("global_load_dwordx4 %0, %3, %4\n\tglobal_load_dwordx4 %1, %3, %5\n\tglobal_load_dwordx2 %2, %6, %7"
                     : "=&v"(d4), "=&v"(s4), "=&v"(a4)
                     : "v"(e0 * 4), "s"(dst), "s"(src), "v"(e0 * 2), "s"(ea) : "memory");
    } else if (wv == 10) {
        asm volatile("global_load_dword %0, %2, %3\n\tglobal_load_ushort %1, %2, %3 offset:4"
                     : "=&v"(sm0), "=&v"(sm1) : "v"(gnode * 6), "s"(pos) : "memory");
    } else {
        asm volatile("global_load_ushort %0, %1, %2" : "=v"(sm0) : "v"(lane * 2), "s"(wx) : "memory");
        lds_st(smb + A_CNT + lane * 16, u32x4{0, 0, 0, 0});
    }
    bar();
    {
        auto piece = [&](int p) {
            if (p < 5) dma1(smb + L_HIMG + p * 1024, h, (b * BN + 8 * p + r8l) * 128 + ((c8 ^ ((4 * p + (lane >> 4)) & 7)) * 16));
            else if (p < 21) dma1(smb + A_W + (p - 5) * 1024, we, (8 * (p - 5) + r8l) * 128 + ((c8 ^ ((r8l & 2) | (((p - 5) & 1) << 2))) * 16));
            else if (lane < 16) dma1(smb + L_AIMG + BN * 128, we, 128 * 128 + lane * 16);
        };
        piece(wv);
        if (wv < 10) piece(12 + wv);
    }
    ASTAMP(1);
    asm volatile("s_waitcnt vmcnt(0)" : "+v"(ep), "+v"(d4), "+v"(s4), "+v"(a4), "+v"(sm0), "+v"(sm1) :: "memory");
    ASTAMP(2);
    const unsigned e_lo = __builtin_amdgcn_readfirstlane(ep[0]), e_hi = __builtin_amdgcn_readfirstlane(ep[1]);
    const unsigned n_lo = e_lo + 1u, n_hi = e_hi + (n_lo == 0u ? 1u : 0u);
    const unsigned tag_w1 = (n_lo >> 8) | (n_hi << 24), tag_w0 = n_lo << 24;

    unsigned bk[4], ark[4], code = 0;
    if (wv < 10) {
#pragma unroll
        for (int i = 0; i < 4; ++i) {
            bk[i] = __umul24(d4[i], 52429u) >> 21;
            ark[i] = atomicAdd((unsigned*)(sm + A_CNT) + bk[i], 1u);
        }
#pragma unroll
        for (int i = 0; i < 4; ++i) {
            const unsigned dl = d4[i] - bk[i] * BN, w = win_of(dl);
            code |= (w * 8 + dl - win_start(w)) << (8 * i);
        }
    } else if (wv == 10) {
        const f32x4 p = {bf2f(sm0 & 0xffffu), __uint_as_float(sm0 & 0xffff0000u), bf2f(sm1), 0.f};
        if (lane < 48) lds_st(smb + L_PD + lane * 16, lane < BN ? p : f32x4{0.f, 0.f, 0.f, 0.f});
        if (lane < BN) st_dev(Ptab, gnode * 16, __builtin_bit_cast(u32x4, p));
    } else {
        lds_st(smb + L_WX + lane * 4, bf2f(sm0) * NEG_LN2);
        if (b == 0) {
            const u32x4 z = {0, 0, 0, 0};
            st_dev(Ptab, NN * 16 + lane * 16, z);
            if (lane < 8) st_dev(Btab, NN * 128 + lane * 16, z);
        }
    }
    ASTAMP(3);
    bar();
    ASTAMP(4);

    u32x4 t_lo = {0, 0, 0, 0};
    if (wv == 11) {
        const u32x4 cn = lds_ld<u32x4>(smb + A_CNT + lane * 16);
        const u32x4 tr = {div3(cn[0] + 2), div3(cn[1] + 2), div3(cn[2] + 2), div3(cn[3] + 2)};
        const unsigned tot = tr[0] + tr[1] + tr[2] + tr[3];
        const unsigned in = scan64(tot), ex = in - tot;
        const u32x4 of = {ex, ex + tr[0], ex + tr[0] + tr[1], ex + tr[0] + tr[1] + tr[2]};
        lds_st(smb + A_OFF + lane * 16, of);
        if (lane == 63) lds_st(smb + A_TOT, in);
#pragma unroll
        for (int i = 0; i < 4; ++i) t_lo[i] = tag_w0 | (cn[i] << 12) | of[i];
    } else if (wv < 8) {
        const int ct = wv & 3, g = lane >> 4, l15 = lane & 15;
        const char* wimg = sm + A_W + (wv >> 2) * 8192;
        const bf16x8 a0 = wfrag16(wimg, 0, ct, lane), a1 = wfrag16(wimg, 1, ct, lane);
        bf16x8 hb[6];
#pragma unroll
        for (int nt = 0; nt < 3; ++nt) {
            hb[2 * nt] = nfrag16(sm + L_HIMG, 16 * nt + l15, g);
            hb[2 * nt + 1] = nfrag16(sm + L_HIMG, 16 * nt + l15, 4 + g);
        }
        SBAR();
        f32x4 acc[3];
#pragma unroll
        for (int nt = 0; nt < 3; ++nt) acc[nt] = MFMA16(a0, hb[2 * nt], (f32x4{0.f, 0.f, 0.f, 0.f}));
#pragma unroll
        for (int nt = 0; nt < 3; ++nt) acc[nt] = MFMA16(a1, hb[2 * nt + 1], acc[nt]);
#pragma unroll
        for (int nt = 0; nt < 3; ++nt) {
            const int node = 16 * nt + l15;
            const u32x2 o = {pk_bf16(acc[nt][0] * NEG_LOG2E, acc[nt][1] * NEG_LOG2E), pk_bf16(acc[nt][2] * NEG_LOG2E, acc[nt][3] * NEG_LOG2E)};
            const unsigned oa = wv < 4 ? L_AIMG + node * 128 + (16 * ct + 4 * g) * 2
                                       : A_O + node * 128 + 16 * ((2 * ct + (g >> 1)) ^ (node & 7)) + 8 * (g & 1);
            lds_st(smb + (nt < 2 || l15 < BN - 32 ? oa : (unsigned)(A_R + 16320 + (lane & 7) * 8)), o);
        }
    }
    ASTAMP(5);
    bar();
    ASTAMP(6);

    if (wv < 10) {
#pragma unroll
        for (int i = 0; i < 4; ++i) {
            const unsigned q = div3(ark[i]), j = ark[i] - 3 * q;
            const unsigned t = smb + A_R + (lds_ld<unsigned>(smb + A_OFF + bk[i] * 4) + q) * 16;
            const unsigned av = (i & 1) ? (a4[i >> 1] & 0xffff0000u) : (a4[i >> 1] << 16);
            lds_st(t + j * 4, s4[i] | av);
            lds_st(t + 12 + j, (unsigned char)(code >> (8 * i)));
        }
    } else {
#pragma clang loop unroll(disable)
        for (int x = tid - 640; x < BN * 8; x += 128) {
            const int node = x >> 3, ch = (x & 7) ^ (node & 7);
            st_dev(Btab, (b * BN + node) * 128 + ch * 16, lds_ld<u32x4>(smb + A_O + x * 16));
        }
    }
    bar();
    ASTAMP(7);
    {
        const unsigned ntot = lds_ld<unsigned>(smb + A_TOT);
        if ((unsigned)tid < ntot) st_dev(Rec, b * 16384 + tid * 16, lds_ld<u32x4>(smb + A_R + tid * 16));
        if ((unsigned)tid + K2_T < ntot) st_dev(Rec, b * 16384 + (tid + K2_T) * 16, lds_ld<u32x4>(smb + A_R + (tid + K2_T) * 16));
    }
    ASTAMP(8);
    {
        const u32x4 z = {0, 0, 0, 0};
        lds_st(smb + L_STAGE + wv * 2048 + lane * 16, z);
        lds_st(smb + L_STAGE + wv * 2048 + 1024 + lane * 16, z);
        const u32x4 n32 = {NN, NN, NN, NN}, n8 = {0x08080808u, 0x08080808u, 0x08080808u, 0x08080808u};
        lds_st(smb + L_R32 + tid * 16, n32);
        if (tid < RALL / 4 - K2_T) lds_st(smb + L_R32 + (K2_T + tid) * 16, n32);
        if (wv >= 1 && wv < 5) {
            if (tid - 64 < RALL / 16) lds_st(smb + L_R8 + (tid - 64) * 16, n8);
        } else if (wv == 5) {
            if (lane < 56) lds_st(smb + L_BINS + lane * 16, z);
            if (lane < 16) lds_st(smb + L_RDY + lane * 4, 0u);
        } else if (wv == 6) {
            if (lane < 48) lds_st(smb + L_AIMG + (BN + 2) * 128 + lane * 16, z);
        } else if (wv == 7) {
            const int n = lane >> 2, j = lane & 3;
            if (lane < 36) lds_st(smb + L_OH + lane * 4, (n < 8 && (n >> 1) == j) ? (0x3F80u << (16 * (n & 1))) : 0u);
        }
    }
    wait_vm0();
    ASTAMP(9);
    bar();
    if (wv == 11) {
        st_dev(ws + W_T, b * 2048 + lane * 32, u32x4{t_lo[0], tag_w1, t_lo[1], tag_w1});
        st_dev(ws + W_T, b * 2048 + lane * 32 + 16, u32x4{t_lo[2], tag_w1, t_lo[3], tag_w1});
    }
    ASTAMP(10);

    const int r = lane & 31, hh = lane >> 5;
    const int win = (int)((0x545432231100ull >> (4 * wv)) & 7);
    const bool second = (0xC5A >> wv) & 1;
    const int wnode = win_start(win);
    char* part = sm + L_SLOT + wv * 4096;
    const int blk = (lane >> 4) & 1, q4 = (lane & 15) >> 2, p4 = lane & 3;
    bf16x8 i0, i1, e0, e1;
#pragma unroll
    for (int j = 0; j < 8; ++j) {
        i0[j] = (r == 8 * hh + j) ? (short)0x3F80 : (short)0;
        i1[j] = (r == 16 + 8 * hh + j) ? (short)0x3F80 : (short)0;
    }
    {
        const int row = hh ? BN + q4 : wnode + q4;
        const char* ab = sm + L_AIMG + row * 128 + (16 * blk + 4 * p4) * 2;
        e0 = cat8(tr_read(ab), tr_read(ab + 512));
        e1 = cat8(tr_read(ab + 64), tr_read(ab + 64 + 512));
    }
    unsigned aad[4], kk[4];
    {
        const int rho = 8 * (r & 3) + (r >> 2);
        const int fsw = (r & 3) ^ (((r >> 3) & 1) << 2);
#pragma unroll
        for (int s = 0; s < 4; ++s) aad[s] = smb + L_SLOT + wv * 4096 + rho * 128 + 16 * ((2 * s + hh) ^ fsw);
#pragma unroll
        for (int i = 0; i < 4; ++i) kk[i] = 16 * ((lane & 7) ^ i ^ (((lane >> 4) & 1) << 2)) + (3072 - 1024 * i);
    }
    const char* gB = Btab - 3072;
    const unsigned slot_a = smb + L_SLOT + wv * 4096;
    const unsigned pbuf_a = smb + L_PB + wv * 1024;
    unsigned src_a = smb + L_R32 + (lane >> 3) * 16, rs_a = smb + L_R32 + lane * 4, rn_a = smb + L_R8 + lane;
    unsigned stage_r = smb + L_STAGE + wv * 2048 + r * 64;
    unsigned trb_a = smb + L_STAGE + wv * 2048 + (4 * hh + q4) * 64 + blk * 32 + p4 * 8;
    const unsigned pdw = smb + L_PD + wnode * 16;
    unsigned oh_a = smb + L_OH;
    f32x16 z0, z1;
#pragma unroll
    for (int i = 0; i < 16; ++i) { z0[i] = 0.f; z1[i] = 0.f; }
    PIN(i0); PIN(i1); PIN(e0); PIN(e1); PIN(z0); PIN(z1);
    PIN(aad[0]); PIN(aad[1]); PIN(aad[2]); PIN(aad[3]); PIN(kk[0]); PIN(kk[1]); PIN(kk[2]); PIN(kk[3]);
    PIN(src_a); PIN(rs_a); PIN(rn_a); PIN(stage_r); PIN(trb_a); PIN(oh_a);

#pragma clang loop unroll(disable)
    for (int q = wv; q < 16; q += K2_W)
        dma1(smb + L_WH + q * 1024, wh, (8 * q + r8l) * 128 + ((c8 ^ ((r8l & 2) | ((q & 1) << 2))) * 16));
    unsigned tw;
    asm volatile("global_load_dword %0, %1, %2" : "=v"(tw)
                 : "v"(lane == 0 ? b * (BN * 128) : (NN * 128 + b * (BN * 6)) & ~3), "s"(out) : "memory");

    const int l3 = (lane * 171) >> 9, sub = lane - 3 * l3;
    const int cid = 21 * wv + l3;
    const bool cok = lane < 63 && cid < NCH;
    const int cidc = cok ? cid : 0;
    unsigned tval = 0;
    bool lost = true;
#pragma clang loop unroll(disable)
    for (int spin = 0; spin < (1 << 16); ++spin) {
        u32x2 tv;
        asm volatile("global_load_dwordx2 %0, %2, %3 sc1\n\ts_waitcnt vmcnt(0)"
                     : "=&v"(tv), "+v"(tw) : "v"((unsigned)W_T + cidc * 2048 + b * 8), "s"(ws) : "memory");
        tval = tv[0];
        if (__all(!cok || (tv[1] == tag_w1 && (tv[0] >> 24) == (tag_w0 >> 24)))) { lost = false; break; }
        __builtin_amdgcn_s_sleep(1);
    }
    PSTAMP(2);

    const unsigned cnt = (cok && !lost) ? ((tval >> 12) & 0xfffu) : 0u;
    const unsigned ntr = div3(cnt + 2);
    const unsigned incl = scan64(sub == 0 ? ntr : 0u);
    const unsigned mw = __builtin_amdgcn_readlane(incl, 63);
    const bool slow_w = mw > MAPCAP;
    const unsigned map_a = smb + L_SLOT + wv * 4096;
    QSTAMP(1);
    if (!slow_w) {
        unsigned g = (cidc * 1024 + (tval & 0xfffu) + sub) | ((cnt - 3 * sub) << 20);
        unsigned mp = map_a + (incl - ntr + sub) * 4;
#pragma clang loop unroll(disable) vectorize(disable) interleave(disable)
        for (unsigned i = sub; i < ntr; i += 3) { lds_st(mp, g); mp += 12; g += 3 - (9u << 20); }
    }
    PSTAMP(3);
    const unsigned mwe = slow_w ? 0u : mw;
    unsigned left[2];
    u32x4 rec[2];
#pragma unroll
    for (int k = 0; k < 2; ++k) {
        const unsigned m = lds_ld<unsigned>(map_a + (64 * k + lane) * 4);
        const bool ok = (unsigned)(64 * k + lane) < mwe;
        left[k] = ok ? m >> 20 : 0u;
        const unsigned go = ok ? (m & 0xfffffu) * 16 : 0u;
        asm volatile("global_load_dwordx4 %0, %1, %2 sc1" : "=v"(rec[k]) : "v"(go), "s"(Rec) : "memory");
    }
    if (lane == 0) lds_st(smb + L_WF + wv * 4, lost ? 2u : slow_w ? 1u : 0u);
    PSTAMP(4);
    asm volatile("s_waitcnt vmcnt(0)" : "+v"(rec[0]), "+v"(rec[1]) :: "memory");
    PSTAMP(5);
    unsigned bin[6], rk[6];
#pragma unroll
    for (int k = 0; k < 6; ++k) {
        const unsigned w = (rec[k / 3][3] >> (8 * (k % 3) + 3)) & 7u;
        bin[k] = (left[k / 3] > (unsigned)(k % 3) ? w : 6u) * 128 + (lane & 31) * 4;
        rk[k] = atomicAdd((unsigned*)(sm + L_BINS + bin[k]), 1u);
    }
    QSTAMP(3);
    PSTAMP(6);
    bar();
    PSTAMP(7);
    unsigned pf;
    {
        const unsigned part = blockIdx.x >> 3;
        const unsigned off = tid < 626 ? (unsigned)W_B + part * (313 * 128) + tid * 64
                                       : (unsigned)W_P + part * (40 * 128) + (tid < 706 ? tid - 626 : 0) * 64;
        asm volatile("global_load_dword %0, %1, %2 sc1" : "=v"(pf) : "v"(off), "s"(ws) : "memory");
    }

    if (b == 0 && tid == 7 * 64) *(u32x2*)(ws + W_E) = u32x2{n_lo, n_hi};
    const unsigned otab = smb + L_SLOT + wv * 4096 + 1024;
    int tw6[6];
    {
        const unsigned wfl = lds_ld<unsigned>(smb + L_WF + (lane < K2_W ? lane : 0) * 4);
        unsigned c3[3], in3[3];
#pragma unroll
        for (int k = 0; k < 3; ++k) c3[k] = lds_ld<unsigned>(smb + L_BINS + (2 * k + hh) * 128 + r * 4);
#pragma unroll
        for (int k = 0; k < 3; ++k) in3[k] = scan32(c3[k]);
#pragma unroll
        for (int k = 0; k < 3; ++k) {
            lds_st(otab + (2 * k + hh) * 128 + r * 4, (2 * k + hh) * WCAP + in3[k] - c3[k]);
            tw6[2 * k] = (__builtin_amdgcn_readlane(in3[k], 31) + 31) >> 5;
            tw6[2 * k + 1] = (__builtin_amdgcn_readlane(in3[k], 63) + 31) >> 5;
        }
        if (__any(wfl == 2)) { asm volatile("s_waitcnt vmcnt(0)" : "+v"(pf) :: "memory"); return; }
        tw6[0] |= __any(wfl != 0) ? 0x100 : 0;
    }
    constexpr int WT = WCAP / 32;
    const bool slow = tw6[0] > WT || tw6[1] > WT || tw6[2] > WT || tw6[3] > WT || tw6[4] > WT || tw6[5] > WT;
    PSTAMP(9);

    if (!slow) {
#pragma unroll
        for (int k = 0; k < 6; ++k) {
            const unsigned o = lds_ld<unsigned>(otab + bin[k]) + rk[k];
            const unsigned p = left[k / 3] > (unsigned)(k % 3) ? o : (unsigned)(RALL - 1);
            lds_st(smb + L_R32 + p * 4, rec[k / 3][k % 3]);
            lds_st(smb + L_R8 + p, (unsigned char)((rec[k / 3][3] >> (8 * (k % 3))) & 7u));
        }
        const int wstart = win * WCAP;
        const int l0 = tw6[0] + tw6[3] + tw6[4], l1 = tw6[0] + tw6[2] + tw6[5];
        const int l2 = tw6[1] + tw6[2] + tw6[4], l3s = tw6[1] + tw6[3] + tw6[5];
        const int wtiles = win == 0 ? tw6[0] : win == 1 ? tw6[1] : win == 2 ? tw6[2] : win == 3 ? tw6[3] : win == 4 ? tw6[4] : tw6[5];
        const int la = win == 0 ? l0 : win == 1 ? l2 : win == 2 ? l1 : win == 3 ? l3s : win == 4 ? l0 : l1;
        const int lb = win == 0 ? l1 : win == 1 ? l3s : win == 2 ? l2 : win == 3 ? l0 : win == 4 ? l2 : l3s;
        int first = (4 * wtiles - la + lb + 4) >> 3;
        first = first < 0 ? 0 : first > wtiles ? wtiles : first;
        const int ta = second ? first : 0, tb = second ? wtiles : first;

        bar();
        QSTAMP(5);
        PSTAMP(10);

        f32x16 xa, xb;
        QSTAMP(6);
        CSTAMP(12);

        if (ta < tb) {
            int base = wstart + 32 * ta;
            const int end = wstart + 32 * tb;
            bf16x8 a0, a1, a2, a3, ef, wf0, wf1;
            unsigned rs, rn, w0, w1, ua, vb;
            unsigned saddr;
            bool valid;
            u32x4 pkp = {0, 0, 0, 0};
#pragma unroll
            for (int j = 0; j < 8; ++j) wf1[j] = 0;

            u32x4 sn, exr;
            f32x4 pd, ps;
            auto read_src = [&](int ri) { sn = lds_ld<u32x4>(src_a + ri * 4); };
            auto issue_rows = [&]() {
                const unsigned v0 = mad16(sn[0], 128, kk[0]), v1 = mad16(sn[1], 128, kk[1]);
                const unsigned v2 = mad16(sn[2], 128, kk[2]), v3 = mad16(sn[3], 128, kk[3]);
                wait_lgkm0();
                dma4_dev(slot_a, gB, v0, v1, v2, v3);
            };
            auto read_pair = [&](int ri) {
                rs = lds_ld<unsigned>(rs_a + ri * 4);
                rn = lds_ld<unsigned char>(rn_a + ri);
            };
            auto issue_pos = [&]() { dma1_dev(pbuf_a, Ptab, mad16(rs, 16, 0u)); };
            auto read_pos = [&]() {
                pd = lds_ld<f32x4>(pdw + rn * 16);
                ps = lds_ld<f32x4>(pbuf_a + lane * 16);
            };
            auto geometry = [&]() {
                const float dx = pd[0] - ps[0], dy = pd[1] - ps[1], dz = pd[2] - ps[2];
                const float d2 = (dx * dx + dy * dy + dz * dz) * NEG_LOG2E;
                w0 = pk_bf16(1.0f, dx);
                w1 = pk_bf16(dy, dz);
                const unsigned ext = pk_bf16(d2, __uint_as_float(rs & 0xffff0000u) * NEG_LOG2E);
                const auto sw = __builtin_amdgcn_permlane32_swap(oh_a + rn * 16, ext, false, false);
                ua = sw[0];
                vb = sw[1];
                saddr = stage_r + rn * 8;
                PIN(saddr); PIN(w0); PIN(w1);
                valid = rn < 8;
            };
            auto read_rows = [&]() {
                a0 = lds_ld<bf16x8>(aad[0]); a1 = lds_ld<bf16x8>(aad[1]);
                a2 = lds_ld<bf16x8>(aad[2]); a3 = lds_ld<bf16x8>(aad[3]);
            };
            auto read_ext = [&](unsigned u) { exr = lds_ld<u32x4>(hh ? oh_a : u); };
            auto fix_ext = [&](unsigned u) {
                u32x4 ex = exr;
                ex[0] = hh ? u : ex[0];
                ef = __builtin_bit_cast(bf16x8, ex);
                PIN(ef);
            };
            auto stage_weights = [&](bool wr) {
                if (wr) lds_st(saddr, u32x2{w0, w1});
                wf0 = cat8(tr_read_a(trb_a), tr_read_a(trb_a + 512));
                wf1 = cat8(tr_read_a(trb_a + 1024), tr_read_a(trb_a + 1536));
                if (wr) lds_st(saddr, u32x2{0, 0});
            };
            auto nop = [&]() {};

            if (wv >= 8) __builtin_amdgcn_s_sleep(10);
            else if (wv >= 4) __builtin_amdgcn_s_sleep(5);
            read_pair(base);
            read_src(base);
            issue_pos();
            issue_rows();
            if (base + 32 < end) read_src(base + 32);
            PSTAMP(11);
            asm volatile("s_waitcnt vmcnt(4)" : "+v"(pf) :: "memory");
            read_pos();
            geometry();
            read_ext(ua);
            wait_vm0();
            PSTAMP(12);
            read_rows();
            if (base + 32 < end) issue_rows();
            fix_ext(ua);
            {
                f32x16 zero;
#pragma unroll
                for (int i = 0; i < 16; ++i) zero[i] = 0.f;
                xa = MFMA32(a0, i0, zero);
                xa = MFMA32(a1, i1, xa);
                xa = MFMA32(ef, e0, xa);
            }
            SBAR();
            PSTAMP(13);
            auto set_prio = [&](int rem) {
                if (rem >= 192) __builtin_amdgcn_s_setprio(3);
                else if (rem >= 96) __builtin_amdgcn_s_setprio(2);
                else __builtin_amdgcn_s_setprio(1);
            };
            while (true) {
                set_prio(end - base);
                half_tile(xa, xb, a2, a3, ef, e1, i0, i1, z1, pkp, wf1, z0, wf0,
                          [&]() { stage_weights(valid && hh == 0); read_src(base + 64); read_pair(base + 64); },
                          [&]() { wait_vm0(); read_rows(); read_ext(vb); },
                          nop);
                half_tile(xb, xa, a0, a1, ef, e0, i0, i1, z0, pkp, wf1, z1, wf0,
                          [&]() { fix_ext(vb); if (base + 64 < end) issue_rows(); issue_pos(); }, nop, nop);
                if (base + 32 >= end) break;
                half_tile(xa, xb, a2, a3, ef, e1, i0, i1, z1, pkp, wf1, z0, wf0,
                          [&]() { stage_weights(valid && hh == 1); read_src(base + 96); },
                          [&]() { wait_vm0(); read_rows(); read_pos(); },
                          [&]() { geometry(); read_ext(ua); });
                half_tile(xb, xa, a0, a1, ef, e0, i0, i1, z0, pkp, wf1, z1, wf0,
                          [&]() { fix_ext(ua); if (base + 96 < end) issue_rows(); }, nop, nop);
                base += 64;
                if (base >= end) break;
            }
            z1 = MFMA32(__builtin_bit_cast(bf16x8, pkp), wf1, z1);
        }
        __builtin_amdgcn_s_setprio(0);
        asm volatile("s_waitcnt vmcnt(0)" : "+v"(pf) :: "memory");
        CSTAMP(13);
        QSTAMP(7);
        if ((lane & 3) == 0) {
            const int n = r >> 2;
#pragma unroll
            for (int g = 0; g < 4; ++g) {
                *(f32x4*)(part + n * PROW + (8 * g + 4 * hh) * 4) = f32x4{z0[4 * g], z0[4 * g + 1], z0[4 * g + 2], z0[4 * g + 3]};
                *(f32x4*)(part + n * PROW + (32 + 8 * g + 4 * hh) * 4) = f32x4{z1[4 * g], z1[4 * g + 1], z1[4 * g + 2], z1[4 * g + 3]};
            }
        }
        float cs0 = 0.f, cs1 = 0.f;
#pragma unroll
        for (int g = 0; g < 4; ++g) {
            const f32x4 w0 = *(const f32x4*)(sm + L_WX + (8 * g + 4 * hh) * 4);
            const f32x4 w1 = *(const f32x4*)(sm + L_WX + (32 + 8 * g + 4 * hh) * 4);
#pragma unroll
            for (int i = 0; i < 4; ++i) { cs0 += z0[4 * g + i] * w0[i]; cs1 += z1[4 * g + i] * w1[i]; }
        }
        const float csh = cs0 + cs1;
        const auto cw = __builtin_amdgcn_permlane32_swap(__float_as_uint(csh), __float_as_uint(csh), false, false);
        const float cst = __uint_as_float(cw[0]) + __uint_as_float(cw[1]);
        if (hh == 0) ((float*)(part + PPOS))[r] = cst;
        asm volatile("s_waitcnt lgkmcnt(0)" ::: "memory");
        if (lane == 0) *(volatile LDS_AS unsigned*)(size_t)(smb + L_RDY + wv * 4) = 1u;
    } else {
        __builtin_amdgcn_fence(__ATOMIC_ACQUIRE, "agent");
        asm volatile("s_waitcnt vmcnt(0)" : "+v"(pf) :: "memory");
        {
            const u32x4 z = {0, 0, 0, 0};
            *(u32x4*)(part + lane * 16) = z;
            *(u32x4*)(part + 1024 + lane * 16) = z;
            *(u32x4*)(part + 2048 + lane * 16) = z;
        }
        bar();
        const float wd = bf2f(we[128 * 64 + lane]), wa = bf2f(we[129 * 64 + lane]);
        const float wxl = ((const float*)(sm + L_WX))[lane];
        for (int ci = wv; ci < NCH; ci += K2_W) {
            const unsigned tv = *(const unsigned*)(ws + W_T + ci * 2048 + b * 8);
            const unsigned g0 = ci * 1024 + (tv & 0xfffu), cn = (tv >> 12) & 0xfffu;
            for (unsigned i = 0; i < cn; ++i) {
                const unsigned* tp = (const unsigned*)(Rec + (size_t)(g0 + div3(i)) * 16);
                const unsigned j = i - 3 * div3(i);
                const unsigned rc = tp[j], cd = (tp[3] >> (8 * j)) & 0xffu;
                const unsigned s = rc & 0xffffu, w = cd >> 3, n = cd & 7, dl = win_start(w) + n;
                const f32x4 ps = *(const f32x4*)(Ptab + s * 16);
                const f32x4 pd = *(const f32x4*)(sm + L_PD + dl * 16);
                const float dx = pd[0] - ps[0], dy = pd[1] - ps[1], dz = pd[2] - ps[2];
                const unsigned e = pk_bf16((dx * dx + dy * dy + dz * dz) * NEG_LOG2E, __uint_as_float(rc & 0xffff0000u) * NEG_LOG2E);
                const unsigned dxy = pk_bf16(dx, dy), dz0 = pk_bf16(dz, 0.f);
                float y = bf2f(((const uint16_t*)(sm + L_AIMG))[dl * 64 + lane]) + bf2f(((const uint16_t*)Btab)[s * 64 + lane]);
                y += bf2f(e & 0xffffu) * wd + bf2f(e >> 16) * wa;
                const float m = bf2f(pk_bf16(y * rcp(1.0f + ex2(y)), 0.f) & 0xffffu);
                char* tgt = sm + L_SLOT + wave_a(w) * 4096;
                atomicAdd((float*)(tgt + n * PROW) + lane, m);
                float t = m * wxl;
                for (int o = 32; o > 0; o >>= 1) t += __shfl_xor(t, o);
                if (lane < 3) {
                    const float dv = lane == 0 ? bf2f(dxy & 0xffffu) : lane == 1 ? bf2f(dxy >> 16) : bf2f(dz0 & 0xffffu);
                    atomicAdd((float*)(tgt + PPOS) + 4 * n + 1 + lane, dv * t);
                }
            }
        }
    }
    if (slow) bar();

    const int g = lane >> 4, l15 = lane & 15, wsize = win < 4 ? 7 : 6;
    const int node = wnode + l15, nodec = node < 48 ? node : 47, ct0 = second ? 2 : 0;
    bf16x8 wfr[2][4];
#pragma unroll
    for (int c = 0; c < 2; ++c)
#pragma unroll
        for (int s = 0; s < 4; ++s) wfr[c][s] = wfrag16(sm + L_WH, s, ct0 + c, lane);
    const bf16x8 hb0 = nfrag16(sm + L_HIMG, nodec, g), hb1 = nfrag16(sm + L_HIMG, nodec, 4 + g);
    f32x4 acc[2];
    u32x2 hv[2];
#pragma unroll
    for (int c = 0; c < 2; ++c) {
        acc[c] = MFMA16(wfr[c][0], hb0, (f32x4{0.f, 0.f, 0.f, 0.f}));
        acc[c] = MFMA16(wfr[c][1], hb1, acc[c]);
        hv[c] = *(const u32x2*)(sm + L_HIMG + nodec * 128 + 16 * ((2 * (ct0 + c) + (g >> 1)) ^ ((nodec >> 1) & 7)) + 8 * (g & 1));
    }
    const int wa = wave_a(win), wb = wave_b(win);
    const char* pb = sm + L_SLOT + wa * 4096 + (l15 & 7) * PROW + g * 32;
    const int pd2 = (wb - wa) * 4096;
    const int pnl = (lane * 171) >> 9, pcc = lane - 3 * pnl;
    const bool pok = !second && pnl < wsize;
    const unsigned ppo = smb + L_SLOT + wa * 4096 + PPOS + (4 * (pnl & 7) + 1 + pcc) * 4;
    const float p0 = lds_ld<float>(smb + L_PD + (wnode + (pok ? pnl : 0)) * 16 + pcc * 4);
    QSTAMP(8);
    if (!slow) {
        const unsigned flag = smb + L_RDY + (second ? wa : wb) * 4;
#pragma clang loop unroll(disable)
        for (int spin = 0; spin < (1 << 22); ++spin) {
            if (__builtin_amdgcn_readfirstlane(*(const volatile LDS_AS unsigned*)(size_t)flag)) break;
            __builtin_amdgcn_s_sleep(4);
        }
        asm volatile("" ::: "memory");
    }
    QSTAMP(9);
    const float ps0 = lds_ld<float>(ppo), ps1 = lds_ld<float>(ppo + pd2);
#pragma unroll
    for (int s = 0; s < 2; ++s) {
        const f32x4 m0 = (*(const f32x4*)(pb + s * 128) + *(const f32x4*)(pb + s * 128 + pd2)) * NEG_LN2;
        const f32x4 m1 = (*(const f32x4*)(pb + s * 128 + 16) + *(const f32x4*)(pb + s * 128 + 16 + pd2)) * NEG_LN2;
        const bf16x8 mf = as_frag(pk_bf16(m0[0], m0[1]), pk_bf16(m0[2], m0[3]), pk_bf16(m1[0], m1[1]), pk_bf16(m1[2], m1[3]));
#pragma unroll
        for (int c = 0; c < 2; ++c) acc[c] = MFMA16(wfr[c][2 + s], mf, acc[c]);
    }
    if (pok) out[NN * 64 + (b * BN + wnode + pnl) * 3 + pcc] = (uint16_t)(pk_bf16(p0 + ps0 + ps1, 0.f) & 0xffffu);
    QSTAMP(10);
    if (l15 < wsize) {
#pragma unroll
        for (int c = 0; c < 2; ++c) {
            const float o0 = bf2f(hv[c][0] & 0xffffu) + silu_f(acc[c][0]), o1 = bf2f(hv[c][0] >> 16) + silu_f(acc[c][1]);
            const float o2 = bf2f(hv[c][1] & 0xffffu) + silu_f(acc[c][2]), o3 = bf2f(hv[c][1] >> 16) + silu_f(acc[c][3]);
            st_nt(out + (b * BN + node) * 64 + 16 * (ct0 + c) + 4 * g, u32x2{pk_bf16(o0, o1), pk_bf16(o2, o3)});
        }
    }
    QSTAMP(11);
    STAMP_OUT(1, ws);
}

}

extern "C" void kernel_launch(void* const* d_in, const int* in_sizes, int n_in, void* d_out, int out_size, void* d_ws,
                              size_t ws_size, hipStream_t stream) {
    using namespace egnn;
    const uint16_t* h = (const uint16_t*)d_in[0];
    const uint16_t* pos = (const uint16_t*)d_in[1];
    const uint16_t* ea = (const uint16_t*)d_in[2];
    const int* src = (const int*)d_in[3];
    const int* dst = (const int*)d_in[4];
    const uint16_t* we = (const uint16_t*)d_in[5];
    const uint16_t* wx = (const uint16_t*)d_in[6];
    const uint16_t* wh = (const uint16_t*)d_in[7];
    (void)hipFuncSetAttribute((const void*)k_egnn, hipFuncAttributeMaxDynamicSharedMemorySize, L_END);
    k_egnn<<<NBK, K2_T, L_END, stream>>>(src, dst, ea, h, pos, we, wx, wh, (char*)d_ws, (uint16_t*)d_out);
}
```

```cpp
#include <hip/hip_runtime.h>
#include <stdint.h>

namespace egnn {

typedef short    bf16x8 __attribute__((ext_vector_type(8)));
typedef short    s16x4  __attribute__((ext_vector_type(4)));
typedef float    f32x16 __attribute__((ext_vector_type(16)));
typedef float    f32x4  __attribute__((ext_vector_type(4)));
typedef float    f32x2  __attribute__((ext_vector_type(2)));
typedef __bf16   bf16x2 __attribute__((ext_vector_type(2)));
typedef unsigned u32x4  __attribute__((ext_vector_type(4)));
typedef unsigned u32x2  __attribute__((ext_vector_type(2)));
#define LDS_AS __attribute__((address_space(3)))
#define DI __device__ __forceinline__
#define SBAR() __builtin_amdgcn_sched_barrier(0)
#define PIN(x) asm volatile("" : "+v"(x))
#define MFMA32(a, b, c) __builtin_amdgcn_mfma_f32_32x32x16_bf16(a, b, c, 0, 0, 0)
#define MFMA16(a, b, c) __builtin_amdgcn_mfma_f32_16x16x32_bf16(a, b, c, 0, 0, 0)

constexpr int NN = 10000;
constexpr int BN = 40;
constexpr int NBK = 250;
constexpr int CE = 2560;
constexpr int NCH = 250;
constexpr float NEG_LOG2E = -1.4426950408889634f;
constexpr float NEG_LN2 = -0.6931471805599453f;

constexpr size_t W_E   = 0;
constexpr size_t W_T   = 4096;
constexpr size_t W_R   = 1u << 20;
constexpr size_t W_B   = (5u << 20) + (512u << 10);
constexpr size_t W_P   = 7u << 20;

DI unsigned win_of(unsigned dl) { return ((dl * 37u) >> 8) + (dl == 34u ? 1u : 0u); }
DI unsigned win_start(unsigned w) { return 7u * w - (w == 5u ? 1u : 0u); }

DI int wave_a(unsigned w) { return (0x987520u >> (4 * w)) & 15; }
DI int wave_b(unsigned w) { return (0xBA4631u >> (4 * w)) & 15; }

DI float bf2f(unsigned v16) { return __uint_as_float(v16 << 16); }
DI unsigned pk_bf16(float lo, float hi) {
    f32x2 x = {lo, hi};
    return __builtin_bit_cast(unsigned, __builtin_convertvector(x, bf16x2));
}
DI float silu_f(float x) {
    return x * __builtin_amdgcn_rcpf(1.0f + __builtin_amdgcn_exp2f(x * NEG_LOG2E));
}
DI s16x4 tr_read(const char* p) {
    return __builtin_amdgcn_ds_read_tr16_b64_v4i16((LDS_AS s16x4*)p);
}
DI s16x4 tr_read_a(unsigned a) { return __builtin_amdgcn_ds_read_tr16_b64_v4i16((LDS_AS s16x4*)(size_t)a); }
DI bf16x8 cat8(s16x4 a, s16x4 b) { return __builtin_shufflevector(a, b, 0, 1, 2, 3, 4, 5, 6, 7); }
DI void wait_vm0() { asm volatile("s_waitcnt vmcnt(0)" ::: "memory"); }
DI void wait_lgkm0() { asm volatile("s_waitcnt lgkmcnt(0)" ::: "memory"); }
DI void bar() { asm volatile("s_waitcnt lgkmcnt(0)\n\ts_barrier" ::: "memory"); }

DI void dma1(unsigned ldsaddr, const void* gbase, unsigned voff) {
    unsigned keep;
    asm volatile("s_mov_b32 %0, m0\n\ts_mov_b32 m0, %1\n\ts_nop 0\n\t"
                 "global_load_lds_dwordx4 %2, %3\n\t"
                 "s_mov_b32 m0, %0"
                 : "=&s"(keep) : "s"(ldsaddr), "v"(voff), "s"(gbase) : "memory");
}
DI void dma1_dev(unsigned ldsaddr, const void* gbase, unsigned voff) {
    unsigned keep;
    asm volatile("s_mov_b32 %0, m0\n\ts_mov_b32 m0, %1\n\ts_nop 0\n\t"
                 "global_load_lds_dwordx4 %2, %3 sc1\n\t"
                 "s_mov_b32 m0, %0"
                 : "=&s"(keep) : "s"(ldsaddr), "v"(voff), "s"(gbase) : "memory");
}
DI void dma4_dev(unsigned ldsaddr, const void* gbase, unsigned v0, unsigned v1, unsigned v2, unsigned v3) {
    unsigned keep;
    asm volatile("s_mov_b32 %0, m0\n\ts_mov_b32 m0, %1\n\ts_nop 0\n\t"
                 "global_load_lds_dwordx4 %2, %6 sc1\n\t"
                 "global_load_lds_dwordx4 %3, %6 offset:1024 sc1\n\t"
                 "global_load_lds_dwordx4 %4, %6 offset:2048 sc1\n\t"
                 "global_load_lds_dwordx4 %5, %6 offset:3072 sc1\n\t"
                 "s_mov_b32 m0, %0"
                 : "=&s"(keep) : "s"(ldsaddr), "v"(v0), "v"(v1), "v"(v2), "v"(v3), "s"(gbase) : "memory");
}
DI void touch(unsigned ldsaddr, const void* gbase, unsigned voff) {
    unsigned keep;
    asm volatile("s_mov_b32 %0, m0\n\ts_mov_b32 m0, %1\n\ts_nop 0\n\t"
                 "global_load_lds_dword %2, %3 sc1\n\t"
                 "s_mov_b32 m0, %0"
                 : "=&s"(keep) : "s"(ldsaddr), "v"(voff), "s"(gbase) : "memory");
}
template <typename T> DI void st_nt(void* p, T v) { __builtin_nontemporal_store(v, (T*)p); }
DI void st_dev(const char* base, unsigned off, u32x4 v) {
    asm volatile("global_store_dwordx4 %0, %1, %2 sc1\n\ts_nop 1" :: "v"(off), "v"(v), "s"(base) : "memory");
}
DI unsigned div3(unsigned x) { return __umul24(x, 43691u) >> 17; }
DI unsigned mad16(unsigned x, unsigned m, unsigned a) {
    unsigned r;
    asm("v_mad_u32_u16 %0, %1, %2, %3" : "=v"(r) : "v"(x), "s"(m), "v"(a));
    return r;
}
template <typename T> DI T lds_ld(unsigned a) { return *(const LDS_AS T*)(size_t)a; }
template <typename T> DI void lds_st(unsigned a, T v) { *(LDS_AS T*)(size_t)a = v; }
template <int CTRL, int ROWMASK> DI unsigned dpp_add(unsigned x) {
    return x + (unsigned)__builtin_amdgcn_update_dpp(0, (int)x, CTRL, ROWMASK, 0xf, true);
}
DI unsigned scan32(unsigned x) {
    x = dpp_add<0x111, 0xf>(x); x = dpp_add<0x112, 0xf>(x); x = dpp_add<0x114, 0xf>(x); x = dpp_add<0x118, 0xf>(x);
    x = dpp_add<0x142, 0xa>(x);
    return x;
}
DI unsigned scan64(unsigned x) { x = scan32(x); x = dpp_add<0x143, 0xc>(x); return x; }
DI int wswz(int k) { return (k & 2) | ((k >> 1) & 4); }
DI bf16x8 wfrag16(const char* img, int s, int ct, int lane) {
    const int g = lane >> 4, q = (lane & 15) >> 2, p = lane & 3;
    const int k0 = 32 * s + 8 * g + q, k1 = k0 + 4, chn = 2 * ct + (p >> 1);
    return cat8(tr_read(img + k0 * 128 + 16 * (chn ^ wswz(k0)) + 8 * (p & 1)),
                tr_read(img + k1 * 128 + 16 * (chn ^ wswz(k1)) + 8 * (p & 1)));
}
DI bf16x8 nfrag16(const char* img, int node, int chunk) {
    return *(const bf16x8*)(img + node * 128 + 16 * (chunk ^ ((node >> 1) & 7)));
}

constexpr int K2_W = 12, K2_T = 64 * K2_W;
constexpr int WCAP = 576;
constexpr int RCAP = 6 * WCAP;
constexpr int L_SLOT = 0;
constexpr int L_STAGE = 49152;
constexpr int RALL = RCAP + 128;
constexpr int L_R32 = 73728;
constexpr int L_R8 = L_R32 + RALL * 4;
constexpr int L_AIMG = L_R8 + RALL;
constexpr int L_HIMG = L_AIMG + 6144;
constexpr int L_WH = L_HIMG + 6144;
constexpr int L_PD = L_WH + 16384;
constexpr int L_OH = L_PD + 768;
constexpr int L_WX = L_OH + 256;
constexpr int L_BINS = L_WX + 256;
constexpr int L_WF = L_BINS + 896;
constexpr int L_RDY = L_WF + 64;
constexpr int L_PB = L_RDY + 64;
constexpr int L_SINK = L_PB + K2_W * 1024;
constexpr int L_END = L_SINK + K2_W * 256;
constexpr int A_CNT = L_SLOT, A_OFF = A_CNT + 1024, A_TOT = A_OFF + 1024, A_R = A_TOT + 16, A_W = A_R + 16384, A_O = A_W + 16384;
static_assert(A_O + BN * 128 <= L_STAGE, "phase A scratch");
constexpr unsigned MAPCAP = 128;
constexpr int PROW = 272, PPOS = 8 * PROW;

DI bf16x8 as_frag(unsigned a, unsigned b, unsigned c, unsigned d) {
    u32x4 v = {a, b, c, d};
    return __builtin_bit_cast(bf16x8, v);
}
DI float ex2(float x) { return __builtin_amdgcn_exp2f(x); }
DI float rcp(float x) { return __builtin_amdgcn_rcpf(x); }

template <class H1, class H2, class H3>
DI void half_tile(const f32x16& xc, f32x16& xn, const bf16x8& aA, const bf16x8& aB, const bf16x8& ef, const bf16x8& eB,
                  const bf16x8& i0, const bf16x8& i1, f32x16& zp, u32x4& pkp, const bf16x8& wp, f32x16& zc,
                  const bf16x8& wc, H1&& hook1, H2&& hook2, H3&& hook3) {
    f32x16 zero;
#pragma unroll
    for (int i = 0; i < 16; ++i) zero[i] = 0.f;
    f32x2 e0, e1, e2, e3;
    u32x4 pk;
#define EXP2(e, i) do { e[0] = ex2(xc[i]); PIN(e[0]); } while (0)
#define EXP2H(e, i) do { e[1] = ex2(xc[i]); PIN(e[1]); } while (0)
#define RCP2(e) do { e[0] = rcp(e[0]); PIN(e[0]); } while (0)
#define RCP2H(e) do { e[1] = rcp(e[1]); PIN(e[1]); } while (0)
#define ADD1(e) do { e = e + 1.0f; PIN(e); } while (0)
#define MULX(e, i) do { e = e * f32x2{xc[i], xc[i + 1]}; PIN(e); } while (0)
#define CVT(d, e) do { d = __builtin_bit_cast(unsigned, __builtin_convertvector(e, bf16x2)); PIN(d); } while (0)
    xn = MFMA32(aA, i0, zero); SBAR();
    EXP2(e0, 0); EXP2H(e0, 1); EXP2(e1, 2); SBAR();
    zp = MFMA32(__builtin_bit_cast(bf16x8, pkp), wp, zp); SBAR();
    EXP2H(e1, 3); EXP2(e2, 4); EXP2H(e2, 5); EXP2(e3, 6); EXP2H(e3, 7); SBAR();
    ADD1(e0); ADD1(e1); ADD1(e2); ADD1(e3); SBAR();
    hook1(); SBAR();
    xn = MFMA32(aB, i1, xn); SBAR();
    RCP2(e0); RCP2H(e0); RCP2(e1); SBAR();
    xn = MFMA32(ef, eB, xn); SBAR();
    RCP2H(e1); RCP2(e2); RCP2H(e2); RCP2(e3); RCP2H(e3); SBAR();
    MULX(e0, 0); MULX(e1, 2); MULX(e2, 4); MULX(e3, 6); SBAR();
    CVT(pk[0], e0); CVT(pk[1], e1); CVT(pk[2], e2); CVT(pk[3], e3); SBAR();
    EXP2(e0, 8); EXP2H(e0, 9); SBAR();
    zc = MFMA32(__builtin_bit_cast(bf16x8, pk), wc, zc); SBAR();
    EXP2(e1, 10); EXP2H(e1, 11); EXP2(e2, 12); EXP2H(e2, 13); EXP2(e3, 14); EXP2H(e3, 15); SBAR();
    ADD1(e0); ADD1(e1); ADD1(e2); ADD1(e3); SBAR();
    hook2(); SBAR();
    RCP2(e0); RCP2H(e0); RCP2(e1); RCP2H(e1); RCP2(e2); RCP2H(e2); RCP2(e3); RCP2H(e3); SBAR();
    hook3(); SBAR();
    MULX(e0, 8); MULX(e1, 10); MULX(e2, 12); MULX(e3, 14); SBAR();
    CVT(pkp[0], e0); CVT(pkp[1], e1); CVT(pkp[2], e2); CVT(pkp[3], e3); SBAR();
#undef EXP2
#undef EXP2H
#undef RCP2
#undef RCP2H
#undef ADD1
#undef MULX
#undef CVT
}

__global__ __launch_bounds__(K2_T) void k_egnn(const int* __restrict__ src, const int* __restrict__ dst,
                                               const uint16_t* __restrict__ ea, const uint16_t* __restrict__ h,
                                               const uint16_t* __restrict__ pos, const uint16_t* __restrict__ we,
                                               const uint16_t* __restrict__ wx, const uint16_t* __restrict__ wh,
                                               char* __restrict__ ws, uint16_t* __restrict__ out) {
    extern __shared__ __attribute__((aligned(16))) char sm[];
    const int tid = threadIdx.x, lane = tid & 63;
    const int wv = __builtin_amdgcn_readfirstlane(tid >> 6);
    const int xc = blockIdx.x & 7;
    const int b = 31 * xc + (xc < 2 ? xc : 2) + (blockIdx.x >> 3);
    const unsigned smb = (unsigned)(size_t)(LDS_AS char*)sm;
    asm volatile("" :: "s"(src), "s"(dst), "s"(ea), "s"(h), "s"(pos), "s"(we), "s"(wx), "s"(wh), "s"(ws), "s"(out));
    const char* Rec = ws + W_R;
    const char* Btab = ws + W_B;
    const char* Ptab = ws + W_P;

    u32x2 ep;
    asm volatile("global_load_dwordx2 %0, %1, %2" : "=v"(ep) : "v"(0u), "s"(ws + W_E) : "memory");
    u32x4 d4 = {0, 0, 0, 0}, s4 = {0, 0, 0, 0};
    u32x2 a4 = {0, 0};
    unsigned sm0 = 0, sm1 = 0;
    const int r8l = lane >> 3, c8 = lane & 7;
    const int gnode = b * BN + (lane < BN ? lane : 0);
    if (wv < 10) {
        const int e0 = b * CE + 4 * tid;
        asm volatile("global_load_dwordx4 %0, %3, %4\n\tglobal_load_dwordx4 %1, %3, %5\n\tglobal_load_dwordx2 %2, %6, %7"
                     : "=&v"(d4), "=&v"(s4), "=&v"(a4)
                     : "v"(e0 * 4), "s"(dst), "s"(src), "v"(e0 * 2), "s"(ea) : "memory");
    } else if (wv == 10) {
        asm volatile("global_load_dword %0, %2, %3\n\tglobal_load_ushort %1, %2, %3 offset:4"
                     : "=&v"(sm0), "=&v"(sm1) : "v"(gnode * 6), "s"(pos) : "memory");
    } else {
        asm volatile("global_load_ushort %0, %1, %2" : "=v"(sm0) : "v"(lane * 2), "s"(wx) : "memory");
        lds_st(smb + A_CNT + lane * 16, u32x4{0, 0, 0, 0});
    }
    bar();
    {
        auto piece = [&](int p) {
            if (p < 5) dma1(smb + L_HIMG + p * 1024, h, (b * BN + 8 * p + r8l) * 128 + ((c8 ^ ((4 * p + (lane >> 4)) & 7)) * 16));
            else if (p < 21) dma1(smb + A_W + (p - 5) * 1024, we, (8 * (p - 5) + r8l) * 128 + ((c8 ^ ((r8l & 2) | (((p - 5) & 1) << 2))) * 16));
            else if (lane < 16) dma1(smb + L_AIMG + BN * 128, we, 128 * 128 + lane * 16);
        };
        piece(wv);
        if (wv < 10) piece(12 + wv);
    }
    asm volatile("s_waitcnt vmcnt(0)" : "+v"(ep), "+v"(d4), "+v"(s4), "+v"(a4), "+v"(sm0), "+v"(sm1) :: "memory");
    const unsigned e_lo = __builtin_amdgcn_readfirstlane(ep[0]), e_hi = __builtin_amdgcn_readfirstlane(ep[1]);
    const unsigned n_lo = e_lo + 1u, n_hi = e_hi + (n_lo == 0u ? 1u : 0u);
    const unsigned tag_w1 = (n_lo >> 8) | (n_hi << 24), tag_w0 = n_lo << 24;

    unsigned bk[4], ark[4], code = 0;
    if (wv < 10) {
#pragma unroll
        for (int i = 0; i < 4; ++i) {
            bk[i] = __umul24(d4[i], 52429u) >> 21;
            ark[i] = atomicAdd((unsigned*)(sm + A_CNT) + bk[i], 1u);
        }
#pragma unroll
        for (int i = 0; i < 4; ++i) {
            const unsigned dl = d4[i] - bk[i] * BN, w = win_of(dl);
            code |= (w * 8 + dl - win_start(w)) << (8 * i);
        }
    } else if (wv == 10) {
        const f32x4 p = {bf2f(sm0 & 0xffffu), __uint_as_float(sm0 & 0xffff0000u), bf2f(sm1), 0.f};
        if (lane < 48) lds_st(smb + L_PD + lane * 16, lane < BN ? p : f32x4{0.f, 0.f, 0.f, 0.f});
        if (lane < BN) st_dev(Ptab, gnode * 16, __builtin_bit_cast(u32x4, p));
    } else {
        lds_st(smb + L_WX + lane * 4, bf2f(sm0) * NEG_LN2);
        if (b == 0) {
            const u32x4 z = {0, 0, 0, 0};
            st_dev(Ptab, NN * 16 + lane * 16, z);
            if (lane < 8) st_dev(Btab, NN * 128 + lane * 16, z);
        }
    }
    bar();

    u32x4 t_lo = {0, 0, 0, 0};
    if (wv == 11) {
        const u32x4 cn = lds_ld<u32x4>(smb + A_CNT + lane * 16);
        const u32x4 tr = {div3(cn[0] + 2), div3(cn[1] + 2), div3(cn[2] + 2), div3(cn[3] + 2)};
        const unsigned tot = tr[0] + tr[1] + tr[2] + tr[3];
        const unsigned in = scan64(tot), ex = in - tot;
        const u32x4 of = {ex, ex + tr[0], ex + tr[0] + tr[1], ex + tr[0] + tr[1] + tr[2]};
        lds_st(smb + A_OFF + lane * 16, of);
        if (lane == 63) lds_st(smb + A_TOT, in);
#pragma unroll
        for (int i = 0; i < 4; ++i) t_lo[i] = tag_w0 | (cn[i] << 12) | of[i];
    } else if (wv < 8) {
        const int ct = wv & 3, g = lane >> 4, l15 = lane & 15;
        const char* wimg = sm + A_W + (wv >> 2) * 8192;
        const bf16x8 a0 = wfrag16(wimg, 0, ct, lane), a1 = wfrag16(wimg, 1, ct, lane);
        bf16x8 hb[6];
#pragma unroll
        for (int nt = 0; nt < 3; ++nt) {
            hb[2 * nt] = nfrag16(sm + L_HIMG, 16 * nt + l15, g);
            hb[2 * nt + 1] = nfrag16(sm + L_HIMG, 16 * nt + l15, 4 + g);
        }
        SBAR();
        f32x4 acc[3];
#pragma unroll
        for (int nt = 0; nt < 3; ++nt) acc[nt] = MFMA16(a0, hb[2 * nt], (f32x4{0.f, 0.f, 0.f, 0.f}));
#pragma unroll
        for (int nt = 0; nt < 3; ++nt) acc[nt] = MFMA16(a1, hb[2 * nt + 1], acc[nt]);
#pragma unroll
        for (int nt = 0; nt < 3; ++nt) {
            const int node = 16 * nt + l15;
            const u32x2 o = {pk_bf16(acc[nt][0] * NEG_LOG2E, acc[nt][1] * NEG_LOG2E), pk_bf16(acc[nt][2] * NEG_LOG2E, acc[nt][3] * NEG_LOG2E)};
            const unsigned oa = wv < 4 ? L_AIMG + node * 128 + (16 * ct + 4 * g) * 2
                                       : A_O + node * 128 + 16 * ((2 * ct + (g >> 1)) ^ (node & 7)) + 8 * (g & 1);
            lds_st(smb + (nt < 2 || l15 < BN - 32 ? oa : (unsigned)(A_R + 16320 + (lane & 7) * 8)), o);
        }
    }
    bar();

    if (wv < 10) {
#pragma unroll
        for (int i = 0; i < 4; ++i) {
            const unsigned q = div3(ark[i]), j = ark[i] - 3 * q;
            const unsigned t = smb + A_R + (lds_ld<unsigned>(smb + A_OFF + bk[i] * 4) + q) * 16;
            const unsigned av = (i & 1) ? (a4[i >> 1] & 0xffff0000u) : (a4[i >> 1] << 16);
            lds_st(t + j * 4, s4[i] | av);
            lds_st(t + 12 + j, (unsigned char)(code >> (8 * i)));
        }
    } else {
#pragma clang loop unroll(disable)
        for (int x = tid - 640; x < BN * 8; x += 128) {
            const int node = x >> 3, ch = (x & 7) ^ (node & 7);
            st_dev(Btab, (b * BN + node) * 128 + ch * 16, lds_ld<u32x4>(smb + A_O + x * 16));
        }
    }
    bar();
    {
        const unsigned ntot = lds_ld<unsigned>(smb + A_TOT);
        if ((unsigned)tid < ntot) st_dev(Rec, b * 16384 + tid * 16, lds_ld<u32x4>(smb + A_R + tid * 16));
        if ((unsigned)tid + K2_T < ntot) st_dev(Rec, b * 16384 + (tid + K2_T) * 16, lds_ld<u32x4>(smb + A_R + (tid + K2_T) * 16));
    }
    {
        const u32x4 z = {0, 0, 0, 0};
        lds_st(smb + L_STAGE + wv * 2048 + lane * 16, z);
        lds_st(smb + L_STAGE + wv * 2048 + 1024 + lane * 16, z);
        const u32x4 n32 = {NN, NN, NN, NN}, n8 = {0x08080808u, 0x08080808u, 0x08080808u, 0x08080808u};
        lds_st(smb + L_R32 + tid * 16, n32);
        if (tid < RALL / 4 - K2_T) lds_st(smb + L_R32 + (K2_T + tid) * 16, n32);
        if (wv >= 1 && wv < 5) {
            if (tid - 64 < RALL / 16) lds_st(smb + L_R8 + (tid - 64) * 16, n8);
        } else if (wv == 5) {
            if (lane < 56) lds_st(smb + L_BINS + lane * 16, z);
            if (lane < 16) lds_st(smb + L_RDY + lane * 4, 0u);
        } else if (wv == 6) {
            if (lane < 48) lds_st(smb + L_AIMG + (BN + 2) * 128 + lane * 16, z);
        } else if (wv == 7) {
            const int n = lane >> 2, j = lane & 3;
            if (lane < 36) lds_st(smb + L_OH + lane * 4, (n < 8 && (n >> 1) == j) ? (0x3F80u << (16 * (n & 1))) : 0u);
        }
    }
    wait_vm0();
    bar();
    if (wv == 11) {
        st_dev(ws + W_T, b * 2048 + lane * 32, u32x4{t_lo[0], tag_w1, t_lo[1], tag_w1});
        st_dev(ws + W_T, b * 2048 + lane * 32 + 16, u32x4{t_lo[2], tag_w1, t_lo[3], tag_w1});
    }

    const int r = lane & 31, hh = lane >> 5;
    const int win = (int)((0x545432231100ull >> (4 * wv)) & 7);
    const bool second = (0xC5A >> wv) & 1;
    const int wnode = win_start(win);
    char* part = sm + L_SLOT + wv * 4096;
    const int blk = (lane >> 4) & 1, q4 = (lane & 15) >> 2, p4 = lane & 3;
    bf16x8 i0, i1, e0, e1;
#pragma unroll
    for (int j = 0; j < 8; ++j) {
        i0[j] = (r == 8 * hh + j) ? (short)0x3F80 : (short)0;
        i1[j] = (r == 16 + 8 * hh + j) ? (short)0x3F80 : (short)0;
    }
    {
        const int row = hh ? BN + q4 : wnode + q4;
        const char* ab = sm + L_AIMG + row * 128 + (16 * blk + 4 * p4) * 2;
        e0 = cat8(tr_read(ab), tr_read(ab + 512));
        e1 = cat8(tr_read(ab + 64), tr_read(ab + 64 + 512));
    }
    unsigned aad[4], kk[4];
    {
        const int rho = 8 * (r & 3) + (r >> 2);
        const int fsw = (r & 3) ^ (((r >> 3) & 1) << 2);
#pragma unroll
        for (int s = 0; s < 4; ++s) aad[s] = smb + L_SLOT + wv * 4096 + rho * 128 + 16 * ((2 * s + hh) ^ fsw);
#pragma unroll
        for (int i = 0; i < 4; ++i) kk[i] = 16 * ((lane & 7) ^ i ^ (((lane >> 4) & 1) << 2)) + (3072 - 1024 * i);
    }
    const char* gB = Btab - 3072;
    const unsigned slot_a = smb + L_SLOT + wv * 4096;
    const unsigned pbuf_a = smb + L_PB + wv * 1024;
    unsigned src_a = smb + L_R32 + (lane >> 3) * 16, rs_a = smb + L_R32 + lane * 4, rn_a = smb + L_R8 + lane;
    unsigned stage_r = smb + L_STAGE + wv * 2048 + r * 64;
    unsigned trb_a = smb + L_STAGE + wv * 2048 + (4 * hh + q4) * 64 + blk * 32 + p4 * 8;
    const unsigned pdw = smb + L_PD + wnode * 16;
    unsigned oh_a = smb + L_OH;
    f32x16 z0, z1;
#pragma unroll
    for (int i = 0; i < 16; ++i) { z0[i] = 0.f; z1[i] = 0.f; }
    PIN(i0); PIN(i1); PIN(e0); PIN(e1); PIN(z0); PIN(z1);
    PIN(aad[0]); PIN(aad[1]); PIN(aad[2]); PIN(aad[3]); PIN(kk[0]); PIN(kk[1]); PIN(kk[2]); PIN(kk[3]);
    PIN(src_a); PIN(rs_a); PIN(rn_a); PIN(stage_r); PIN(trb_a); PIN(oh_a);

#pragma clang loop unroll(disable)
    for (int q = wv; q < 16; q += K2_W)
        dma1(smb + L_WH + q * 1024, wh, (8 * q + r8l) * 128 + ((c8 ^ ((r8l & 2) | ((q & 1) << 2))) * 16));

    const int l3 = (lane * 171) >> 9, sub = lane - 3 * l3;
    const int cid = 21 * wv + l3;
    const bool cok = lane < 63 && cid < NCH;
    const int cidc = cok ? cid : 0;
    unsigned tval = 0;
    bool lost = true;
#pragma clang loop unroll(disable)
    for (int spin = 0; spin < (1 << 16); ++spin) {
        u32x2 tv;
        asm volatile("global_load_dwordx2 %0, %1, %2 sc1\n\ts_waitcnt vmcnt(0)"
                     : "=&v"(tv) : "v"((unsigned)W_T + cidc * 2048 + b * 8), "s"(ws) : "memory");
        tval = tv[0];
        if (__all(!cok || (tv[1] == tag_w1 && (tv[0] >> 24) == (tag_w0 >> 24)))) { lost = false; break; }
        __builtin_amdgcn_s_sleep(1);
    }

    const unsigned cnt = (cok && !lost) ? ((tval >> 12) & 0xfffu) : 0u;
    const unsigned ntr = div3(cnt + 2);
    const unsigned incl = scan64(sub == 0 ? ntr : 0u);
    const unsigned mw = __builtin_amdgcn_readlane(incl, 63);
    const bool slow_w = mw > MAPCAP;
    const unsigned map_a = smb + L_SLOT + wv * 4096;
    if (!slow_w) {
        unsigned g = (cidc * 1024 + (tval & 0xfffu) + sub) | ((cnt - 3 * sub) << 20);
        unsigned mp = map_a + (incl - ntr + sub) * 4;
#pragma clang loop unroll(disable) vectorize(disable) interleave(disable)
        for (unsigned i = sub; i < ntr; i += 3) { lds_st(mp, g); mp += 12; g += 3 - (9u << 20); }
    }
    const unsigned mwe = slow_w ? 0u : mw;
    unsigned left[2];
    u32x4 rec[2];
#pragma unroll
    for (int k = 0; k < 2; ++k) {
        const unsigned m = lds_ld<unsigned>(map_a + (64 * k + lane) * 4);
        const bool ok = (unsigned)(64 * k + lane) < mwe;
        left[k] = ok ? m >> 20 : 0u;
        const unsigned go = ok ? (m & 0xfffffu) * 16 : 0u;
        asm volatile("global_load_dwordx4 %0, %1, %2 sc1" : "=v"(rec[k]) : "v"(go), "s"(Rec) : "memory");
    }
    if (lane == 0) lds_st(smb + L_WF + wv * 4, lost ? 2u : slow_w ? 1u : 0u);
    asm volatile("s_waitcnt vmcnt(0)" : "+v"(rec[0]), "+v"(rec[1]) :: "memory");
    unsigned bin[6], rk[6];
#pragma unroll
    for (int k = 0; k < 6; ++k) {
        const unsigned w = (rec[k / 3][3] >> (8 * (k % 3) + 3)) & 7u;
        bin[k] = (left[k / 3] > (unsigned)(k % 3) ? w : 6u) * 128 + (lane & 31) * 4;
        rk[k] = atomicAdd((unsigned*)(sm + L_BINS + bin[k]), 1u);
    }
    bar();
    {
        const unsigned part = blockIdx.x >> 3;
        const unsigned off = tid < 626 ? (unsigned)W_B + part * (313 * 128) + tid * 64
                                       : (unsigned)W_P + part * (40 * 128) + (tid < 706 ? tid - 626 : 0) * 64;
        touch(smb + L_SINK + wv * 256, ws, off);
    }

    if (b == 0 && tid == 7 * 64) *(u32x2*)(ws + W_E) = u32x2{n_lo, n_hi};
    const unsigned otab = smb + L_SLOT + wv * 4096 + 1024;
    int tw6[6];
    {
        const unsigned wfl = lds_ld<unsigned>(smb + L_WF + (lane < K2_W ? lane : 0) * 4);
        unsigned c3[3], in3[3];
#pragma unroll
        for (int k = 0; k < 3; ++k) c3[k] = lds_ld<unsigned>(smb + L_BINS + (2 * k + hh) * 128 + r * 4);
#pragma unroll
        for (int k = 0; k < 3; ++k) in3[k] = scan32(c3[k]);
#pragma unroll
        for (int k = 0; k < 3; ++k) {
            lds_st(otab + (2 * k + hh) * 128 + r * 4, (2 * k + hh) * WCAP + in3[k] - c3[k]);
            tw6[2 * k] = (__builtin_amdgcn_readlane(in3[k], 31) + 31) >> 5;
            tw6[2 * k + 1] = (__builtin_amdgcn_readlane(in3[k], 63) + 31) >> 5;
        }
        if (__any(wfl == 2)) { wait_vm0(); return; }
        tw6[0] |= __any(wfl != 0) ? 0x100 : 0;
    }
    constexpr int WT = WCAP / 32;
    const bool slow = tw6[0] > WT || tw6[1] > WT || tw6[2] > WT || tw6[3] > WT || tw6[4] > WT || tw6[5] > WT;

    if (!slow) {
#pragma unroll
        for (int k = 0; k < 6; ++k) {
            const unsigned o = lds_ld<unsigned>(otab + bin[k]) + rk[k];
            const unsigned p = left[k / 3] > (unsigned)(k % 3) ? o : (unsigned)(RALL - 1);
            lds_st(smb + L_R32 + p * 4, rec[k / 3][k % 3]);
            lds_st(smb + L_R8 + p, (unsigned char)((rec[k / 3][3] >> (8 * (k % 3))) & 7u));
        }
        const int wstart = win * WCAP;
        const int l0 = tw6[0] + tw6[3] + tw6[4], l1 = tw6[0] + tw6[2] + tw6[5];
        const int l2 = tw6[1] + tw6[2] + tw6[4], l3s = tw6[1] + tw6[3] + tw6[5];
        const int wtiles = win == 0 ? tw6[0] : win == 1 ? tw6[1] : win == 2 ? tw6[2] : win == 3 ? tw6[3] : win == 4 ? tw6[4] : tw6[5];
        const int la = win == 0 ? l0 : win == 1 ? l2 : win == 2 ? l1 : win == 3 ? l3s : win == 4 ? l0 : l1;
        const int lb = win == 0 ? l1 : win == 1 ? l3s : win == 2 ? l2 : win == 3 ? l0 : win == 4 ? l2 : l3s;
        int first = (4 * wtiles - la + lb + 4) >> 3;
        first = first < 0 ? 0 : first > wtiles ? wtiles : first;
        const int ta = second ? first : 0, tb = second ? wtiles : first;

        bar();

        f32x16 xa, xb;

        if (ta < tb) {
            int base = wstart + 32 * ta;
            const int end = wstart + 32 * tb;
            bf16x8 a0, a1, a2, a3, ef, wf0, wf1;
            unsigned rs, rn, w0, w1, ua, vb;
            unsigned saddr;
            bool valid;
            u32x4 pkp = {0, 0, 0, 0};
#pragma unroll
            for (int j = 0; j < 8; ++j) wf1[j] = 0;

            u32x4 sn, exr;
            f32x4 pd, ps;
            auto read_src = [&](int ri) { sn = lds_ld<u32x4>(src_a + ri * 4); };
            auto issue_rows = [&]() {
                const unsigned v0 = mad16(sn[0], 128, kk[0]), v1 = mad16(sn[1], 128, kk[1]);
                const unsigned v2 = mad16(sn[2], 128, kk[2]), v3 = mad16(sn[3], 128, kk[3]);
                wait_lgkm0();
                dma4_dev(slot_a, gB, v0, v1, v2, v3);
            };
            auto read_pair = [&](int ri) {
                rs = lds_ld<unsigned>(rs_a + ri * 4);
                rn = lds_ld<unsigned char>(rn_a + ri);
            };
            auto issue_pos = [&]() { dma1_dev(pbuf_a, Ptab, mad16(rs, 16, 0u)); };
            auto read_pos = [&]() {
                pd = lds_ld<f32x4>(pdw + rn * 16);
                ps = lds_ld<f32x4>(pbuf_a + lane * 16);
            };
            auto geometry = [&]() {
                const float dx = pd[0] - ps[0], dy = pd[1] - ps[1], dz = pd[2] - ps[2];
                const float d2 = (dx * dx + dy * dy + dz * dz) * NEG_LOG2E;
                w0 = pk_bf16(1.0f, dx);
                w1 = pk_bf16(dy, dz);
                const unsigned ext = pk_bf16(d2, __uint_as_float(rs & 0xffff0000u) * NEG_LOG2E);
                const auto sw = __builtin_amdgcn_permlane32_swap(oh_a + rn * 16, ext, false, false);
                ua = sw[0];
                vb = sw[1];
                saddr = stage_r + rn * 8;
                PIN(saddr); PIN(w0); PIN(w1);
                valid = rn < 8;
            };
            auto read_rows = [&]() {
                a0 = lds_ld<bf16x8>(aad[0]); a1 = lds_ld<bf16x8>(aad[1]);
                a2 = lds_ld<bf16x8>(aad[2]); a3 = lds_ld<bf16x8>(aad[3]);
            };
            auto read_ext = [&](unsigned u) { exr = lds_ld<u32x4>(hh ? oh_a : u); };
            auto fix_ext = [&](unsigned u) {
                u32x4 ex = exr;
                ex[0] = hh ? u : ex[0];
                ef = __builtin_bit_cast(bf16x8, ex);
                PIN(ef);
            };
            auto stage_weights = [&](bool wr) {
                if (wr) lds_st(saddr, u32x2{w0, w1});
                wf0 = cat8(tr_read_a(trb_a), tr_read_a(trb_a + 512));
                wf1 = cat8(tr_read_a(trb_a + 1024), tr_read_a(trb_a + 1536));
                if (wr) lds_st(saddr, u32x2{0, 0});
            };
            auto nop = [&]() {};

            if (wv >= 8) __builtin_amdgcn_s_sleep(10);
            else if (wv >= 4) __builtin_amdgcn_s_sleep(5);
            read_pair(base);
            read_src(base);
            issue_pos();
            issue_rows();
            if (base + 32 < end) read_src(base + 32);
            asm volatile("s_waitcnt vmcnt(4)" ::: "memory");
            read_pos();
            geometry();
            read_ext(ua);
            wait_vm0();
            read_rows();
            if (base + 32 < end) issue_rows();
            fix_ext(ua);
            {
                f32x16 zero;
#pragma unroll
                for (int i = 0; i < 16; ++i) zero[i] = 0.f;
                xa = MFMA32(a0, i0, zero);
                xa = MFMA32(a1, i1, xa);
                xa = MFMA32(ef, e0, xa);
            }
            SBAR();
            auto set_prio = [&](int rem) {
                if (rem >= 192) __builtin_amdgcn_s_setprio(3);
                else if (rem >= 96) __builtin_amdgcn_s_setprio(2);
                else __builtin_amdgcn_s_setprio(1);
            };
            while (true) {
                set_prio(end - base);
                half_tile(xa, xb, a2, a3, ef, e1, i0, i1, z1, pkp, wf1, z0, wf0,
                          [&]() { stage_weights(valid && hh == 0); read_src(base + 64); read_pair(base + 64); },
                          [&]() { wait_vm0(); read_rows(); read_ext(vb); },
                          nop);
                half_tile(xb, xa, a0, a1, ef, e0, i0, i1, z0, pkp, wf1, z1, wf0,
                          [&]() { fix_ext(vb); if (base + 64 < end) { issue_rows(); issue_pos(); } }, nop, nop);
                if (base + 32 >= end) break;
                half_tile(xa, xb, a2, a3, ef, e1, i0, i1, z1, pkp, wf1, z0, wf0,
                          [&]() { stage_weights(valid && hh == 1); read_src(base + 96); },
                          [&]() { wait_vm0(); read_rows(); read_pos(); },
                          [&]() { geometry(); read_ext(ua); });
                half_tile(xb, xa, a0, a1, ef, e0, i0, i1, z0, pkp, wf1, z1, wf0,
                          [&]() { fix_ext(ua); if (base + 96 < end) issue_rows(); }, nop, nop);
                base += 64;
                if (base >= end) break;
            }
            z1 = MFMA32(__builtin_bit_cast(bf16x8, pkp), wf1, z1);
        }
        __builtin_amdgcn_s_setprio(0);
        wait_vm0();
        if ((lane & 3) == 0) {
            const int n = r >> 2;
#pragma unroll
            for (int g = 0; g < 4; ++g) {
                *(f32x4*)(part + n * PROW + (8 * g + 4 * hh) * 4) = f32x4{z0[4 * g], z0[4 * g + 1], z0[4 * g + 2], z0[4 * g + 3]};
                *(f32x4*)(part + n * PROW + (32 + 8 * g + 4 * hh) * 4) = f32x4{z1[4 * g], z1[4 * g + 1], z1[4 * g + 2], z1[4 * g + 3]};
            }
        }
        float cs0 = 0.f, cs1 = 0.f;
#pragma unroll
        for (int g = 0; g < 4; ++g) {
            const f32x4 w0 = *(const f32x4*)(sm + L_WX + (8 * g + 4 * hh) * 4);
            const f32x4 w1 = *(const f32x4*)(sm + L_WX + (32 + 8 * g + 4 * hh) * 4);
#pragma unroll
            for (int i = 0; i < 4; ++i) { cs0 += z0[4 * g + i] * w0[i]; cs1 += z1[4 * g + i] * w1[i]; }
        }
        const float csh = cs0 + cs1;
        const auto cw = __builtin_amdgcn_permlane32_swap(__float_as_uint(csh), __float_as_uint(csh), false, false);
        const float cst = __uint_as_float(cw[0]) + __uint_as_float(cw[1]);
        if (hh == 0) ((float*)(part + PPOS))[r] = cst;
        asm volatile("s_waitcnt lgkmcnt(0)" ::: "memory");
        if (lane == 0) *(volatile LDS_AS unsigned*)(size_t)(smb + L_RDY + wv * 4) = 1u;
    } else {
        __builtin_amdgcn_fence(__ATOMIC_ACQUIRE, "agent");
        wait_vm0();
        {
            const u32x4 z = {0, 0, 0, 0};
            *(u32x4*)(part + lane * 16) = z;
            *(u32x4*)(part + 1024 + lane * 16) = z;
            *(u32x4*)(part + 2048 + lane * 16) = z;
        }
        bar();
        const float wd = bf2f(we[128 * 64 + lane]), wa = bf2f(we[129 * 64 + lane]);
        const float wxl = ((const float*)(sm + L_WX))[lane];
        for (int ci = wv; ci < NCH; ci += K2_W) {
            const unsigned tv = *(const unsigned*)(ws + W_T + ci * 2048 + b * 8);
            const unsigned g0 = ci * 1024 + (tv & 0xfffu), cn = (tv >> 12) & 0xfffu;
            for (unsigned i = 0; i < cn; ++i) {
                const unsigned* tp = (const unsigned*)(Rec + (size_t)(g0 + div3(i)) * 16);
                const unsigned j = i - 3 * div3(i);
                const unsigned rc = tp[j], cd = (tp[3] >> (8 * j)) & 0xffu;
                const unsigned s = rc & 0xffffu, w = cd >> 3, n = cd & 7, dl = win_start(w) + n;
                const f32x4 ps = *(const f32x4*)(Ptab + s * 16);
                const f32x4 pd = *(const f32x4*)(sm + L_PD + dl * 16);
                const float dx = pd[0] - ps[0], dy = pd[1] - ps[1], dz = pd[2] - ps[2];
                const unsigned e = pk_bf16((dx * dx + dy * dy + dz * dz) * NEG_LOG2E, __uint_as_float(rc & 0xffff0000u) * NEG_LOG2E);
                const unsigned dxy = pk_bf16(dx, dy), dz0 = pk_bf16(dz, 0.f);
                float y = bf2f(((const uint16_t*)(sm + L_AIMG))[dl * 64 + lane]) + bf2f(((const uint16_t*)Btab)[s * 64 + lane]);
                y += bf2f(e & 0xffffu) * wd + bf2f(e >> 16) * wa;
                const float m = bf2f(pk_bf16(y * rcp(1.0f + ex2(y)), 0.f) & 0xffffu);
                char* tgt = sm + L_SLOT + wave_a(w) * 4096;
                atomicAdd((float*)(tgt + n * PROW) + lane, m);
                float t = m * wxl;
                for (int o = 32; o > 0; o >>= 1) t += __shfl_xor(t, o);
                if (lane < 3) {
                    const float dv = lane == 0 ? bf2f(dxy & 0xffffu) : lane == 1 ? bf2f(dxy >> 16) : bf2f(dz0 & 0xffffu);
                    atomicAdd((float*)(tgt + PPOS) + 4 * n + 1 + lane, dv * t);
                }
            }
        }
    }
    if (slow) bar();

    const int g = lane >> 4, l15 = lane & 15, wsize = win < 4 ? 7 : 6;
    const int node = wnode + l15, nodec = node < 48 ? node : 47, ct0 = second ? 2 : 0;
    bf16x8 wfr[2][4];
#pragma unroll
    for (int c = 0; c < 2; ++c)
#pragma unroll
        for (int s = 0; s < 4; ++s) wfr[c][s] = wfrag16(sm + L_WH, s, ct0 + c, lane);
    const bf16x8 hb0 = nfrag16(sm + L_HIMG, nodec, g), hb1 = nfrag16(sm + L_HIMG, nodec, 4 + g);
    f32x4 acc[2];
    u32x2 hv[2];
#pragma unroll
    for (int c = 0; c < 2; ++c) {
        acc[c] = MFMA16(wfr[c][0], hb0, (f32x4{0.f, 0.f, 0.f, 0.f}));
        acc[c] = MFMA16(wfr[c][1], hb1, acc[c]);
        hv[c] = *(const u32x2*)(sm + L_HIMG + nodec * 128 + 16 * ((2 * (ct0 + c) + (g >> 1)) ^ ((nodec >> 1) & 7)) + 8 * (g & 1));
    }
    const int wa = wave_a(win), wb = wave_b(win);
    const char* pb = sm + L_SLOT + wa * 4096 + (l15 & 7) * PROW + g * 32;
    const int pd2 = (wb - wa) * 4096;
    const int pnl = (lane * 171) >> 9, pcc = lane - 3 * pnl;
    const bool pok = !second && pnl < wsize;
    const unsigned ppo = smb + L_SLOT + wa * 4096 + PPOS + (4 * (pnl & 7) + 1 + pcc) * 4;
    const float p0 = lds_ld<float>(smb + L_PD + (wnode + (pok ? pnl : 0)) * 16 + pcc * 4);
    if (!slow) {
        const unsigned flag = smb + L_RDY + (second ? wa : wb) * 4;
#pragma clang loop unroll(disable)
        for (int spin = 0; spin < (1 << 20); ++spin) {
            if (__builtin_amdgcn_readfirstlane(*(const volatile LDS_AS unsigned*)(size_t)flag)) break;
            __builtin_amdgcn_s_sleep(4);
        }
        asm volatile("" ::: "memory");
    }
    const float ps0 = lds_ld<float>(ppo), ps1 = lds_ld<float>(ppo + pd2);
#pragma unroll
    for (int s = 0; s < 2; ++s) {
        const f32x4 m0 = (*(const f32x4*)(pb + s * 128) + *(const f32x4*)(pb + s * 128 + pd2)) * NEG_LN2;
        const f32x4 m1 = (*(const f32x4*)(pb + s * 128 + 16) + *(const f32x4*)(pb + s * 128 + 16 + pd2)) * NEG_LN2;
        const bf16x8 mf = as_frag(pk_bf16(m0[0], m0[1]), pk_bf16(m0[2], m0[3]), pk_bf16(m1[0], m1[1]), pk_bf16(m1[2], m1[3]));
#pragma unroll
        for (int c = 0; c < 2; ++c) acc[c] = MFMA16(wfr[c][2 + s], mf, acc[c]);
    }
    if (pok) out[NN * 64 + (b * BN + wnode + pnl) * 3 + pcc] = (uint16_t)(pk_bf16(p0 + ps0 + ps1, 0.f) & 0xffffu);
    if (l15 < wsize) {
#pragma unroll
        for (int c = 0; c < 2; ++c) {
            const float o0 = bf2f(hv[c][0] & 0xffffu) + silu_f(acc[c][0]), o1 = bf2f(hv[c][0] >> 16) + silu_f(acc[c][1]);
            const float o2 = bf2f(hv[c][1] & 0xffffu) + silu_f(acc[c][2]), o3 = bf2f(hv[c][1] >> 16) + silu_f(acc[c][3]);
            st_nt(out + (b * BN + node) * 64 + 16 * (ct0 + c) + 4 * g, u32x2{pk_bf16(o0, o1), pk_bf16(o2, o3)});
        }
    }
}

}

extern "C" void kernel_launch(void* const* d_in, const int* in_sizes, int n_in, void* d_out, int out_size, void* d_ws,
                              size_t ws_size, hipStream_t stream) {
    using namespace egnn;
    const uint16_t* h = (const uint16_t*)d_in[0];
    const uint16_t* pos = (const uint16_t*)d_in[1];
    const uint16_t* ea = (const uint16_t*)d_in[2];
    const int* src = (const int*)d_in[3];
    const int* dst = (const int*)d_in[4];
    const uint16_t* we = (const uint16_t*)d_in[5];
    const uint16_t* wx = (const uint16_t*)d_in[6];
    const uint16_t* wh = (const uint16_t*)d_in[7];
    (void)hipFuncSetAttribute((const void*)k_egnn, hipFuncAttributeMaxDynamicSharedMemorySize, L_END);
    k_egnn<<<NBK, K2_T, L_END, stream>>>(src, dst, ea, h, pos, we, wx, wh, (char*)d_ws, (uint16_t*)d_out);
}
```

```cpp
#include <hip/hip_runtime.h>
#include <stdint.h>

namespace egnn {

typedef short    bf16x8 __attribute__((ext_vector_type(8)));
typedef short    s16x4  __attribute__((ext_vector_type(4)));
typedef float    f32x16 __attribute__((ext_vector_type(16)));
typedef float    f32x4  __attribute__((ext_vector_type(4)));
typedef float    f32x2  __attribute__((ext_vector_type(2)));
typedef __bf16   bf16x2 __attribute__((ext_vector_type(2)));
typedef unsigned u32x4  __attribute__((ext_vector_type(4)));
typedef unsigned u32x2  __attribute__((ext_vector_type(2)));
#define LDS_AS __attribute__((address_space(3)))
#define DI __device__ __forceinline__
#define SBAR() __builtin_amdgcn_sched_barrier(0)
#define PIN(x) asm volatile("" : "+v"(x))
#define MFMA32(a, b, c) __builtin_amdgcn_mfma_f32_32x32x16_bf16(a, b, c, 0, 0, 0)
#define MFMA16(a, b, c) __builtin_amdgcn_mfma_f32_16x16x32_bf16(a, b, c, 0, 0, 0)

constexpr int NN = 10000;
constexpr int BN = 40;
constexpr int NBK = 250;
constexpr int CE = 2560;
constexpr int NCH = 250;
constexpr float NEG_LOG2E = -1.4426950408889634f;
constexpr float NEG_LN2 = -0.6931471805599453f;

constexpr size_t W_E   = 0;
constexpr size_t W_T   = 4096;
constexpr size_t W_R   = 1u << 20;
constexpr size_t W_B   = (5u << 20) + (512u << 10);
constexpr size_t W_P   = 7u << 20;

DI unsigned win_of(unsigned dl) { return ((dl * 37u) >> 8) + (dl == 34u ? 1u : 0u); }
DI unsigned win_start(unsigned w) { return 7u * w - (w == 5u ? 1u : 0u); }

DI int wave_a(unsigned w) { return (0x987520u >> (4 * w)) & 15; }
DI int wave_b(unsigned w) { return (0xBA4631u >> (4 * w)) & 15; }

DI float bf2f(unsigned v16) { return __uint_as_float(v16 << 16); }
DI unsigned pk_bf16(float lo, float hi) {
    f32x2 x = {lo, hi};
    return __builtin_bit_cast(unsigned, __builtin_convertvector(x, bf16x2));
}
DI float silu_f(float x) {
    return x * __builtin_amdgcn_rcpf(1.0f + __builtin_amdgcn_exp2f(x * NEG_LOG2E));
}
DI s16x4 tr_read(const char* p) {
    return __builtin_amdgcn_ds_read_tr16_b64_v4i16((LDS_AS s16x4*)p);
}
DI s16x4 tr_read_a(unsigned a) { return __builtin_amdgcn_ds_read_tr16_b64_v4i16((LDS_AS s16x4*)(size_t)a); }
DI bf16x8 cat8(s16x4 a, s16x4 b) { return __builtin_shufflevector(a, b, 0, 1, 2, 3, 4, 5, 6, 7); }
DI void wait_vm0() { asm volatile("s_waitcnt vmcnt(0)" ::: "memory"); }
DI void wait_lgkm0() { asm volatile("s_waitcnt lgkmcnt(0)" ::: "memory"); }
DI void bar() { asm volatile("s_waitcnt lgkmcnt(0)\n\ts_barrier" ::: "memory"); }

DI void dma1(unsigned ldsaddr, const void* gbase, unsigned voff) {
    unsigned keep;
    asm volatile("s_mov_b32 %0, m0\n\ts_mov_b32 m0, %1\n\ts_nop 0\n\t"
                 "global_load_lds_dwordx4 %2, %3\n\t"
                 "s_mov_b32 m0, %0"
                 : "=&s"(keep) : "s"(ldsaddr), "v"(voff), "s"(gbase) : "memory");
}
DI void dma1_dev(unsigned ldsaddr, const void* gbase, unsigned voff) {
    unsigned keep;
    asm volatile("s_mov_b32 %0, m0\n\ts_mov_b32 m0, %1\n\ts_nop 0\n\t"
                 "global_load_lds_dwordx4 %2, %3 sc1\n\t"
                 "s_mov_b32 m0, %0"
                 : "=&s"(keep) : "s"(ldsaddr), "v"(voff), "s"(gbase) : "memory");
}
DI void dma4_dev(unsigned ldsaddr, const void* gbase, unsigned v0, unsigned v1, unsigned v2, unsigned v3) {
    unsigned keep;
    asm volatile("s_mov_b32 %0, m0\n\ts_mov_b32 m0, %1\n\ts_nop 0\n\t"
                 "global_load_lds_dwordx4 %2, %6 sc1\n\t"
                 "global_load_lds_dwordx4 %3, %6 offset:1024 sc1\n\t"
                 "global_load_lds_dwordx4 %4, %6 offset:2048 sc1\n\t"
                 "global_load_lds_dwordx4 %5, %6 offset:3072 sc1\n\t"
                 "s_mov_b32 m0, %0"
                 : "=&s"(keep) : "s"(ldsaddr), "v"(v0), "v"(v1), "v"(v2), "v"(v3), "s"(gbase) : "memory");
}
DI void touch(unsigned ldsaddr, const void* gbase, unsigned voff) {
    unsigned keep;
    asm volatile("s_mov_b32 %0, m0\n\ts_mov_b32 m0, %1\n\ts_nop 0\n\t"
                 "global_load_lds_dword %2, %3 sc1\n\t"
                 "s_mov_b32 m0, %0"
                 : "=&s"(keep) : "s"(ldsaddr), "v"(voff), "s"(gbase) : "memory");
}
template <typename T> DI void st_nt(void* p, T v) { __builtin_nontemporal_store(v, (T*)p); }
DI void st_dev(const char* base, unsigned off, u32x4 v) {
    asm volatile("global_store_dwordx4 %0, %1, %2 sc1\n\ts_nop 1" :: "v"(off), "v"(v), "s"(base) : "memory");
}
DI unsigned div3(unsigned x) { return __umul24(x, 43691u) >> 17; }
DI unsigned mad16(unsigned x, unsigned m, unsigned a) {
    unsigned r;
    asm("v_mad_u32_u16 %0, %1, %2, %3" : "=v"(r) : "v"(x), "s"(m), "v"(a));
    return r;
}
template <typename T> DI T lds_ld(unsigned a) { return *(const LDS_AS T*)(size_t)a; }
template <typename T> DI void lds_st(unsigned a, T v) { *(LDS_AS T*)(size_t)a = v; }
template <int CTRL, int ROWMASK> DI unsigned dpp_add(unsigned x) {
    return x + (unsigned)__builtin_amdgcn_update_dpp(0, (int)x, CTRL, ROWMASK, 0xf, true);
}
DI unsigned scan32(unsigned x) {
    x = dpp_add<0x111, 0xf>(x); x = dpp_add<0x112, 0xf>(x); x = dpp_add<0x114, 0xf>(x); x = dpp_add<0x118, 0xf>(x);
    x = dpp_add<0x142, 0xa>(x);
    return x;
}
DI unsigned scan64(unsigned x) { x = scan32(x); x = dpp_add<0x143, 0xc>(x); return x; }
DI int wswz(int k) { return (k & 2) | ((k >> 1) & 4); }
DI bf16x8 wfrag16(const char* img, int s, int ct, int lane) {
    const int g = lane >> 4, q = (lane & 15) >> 2, p = lane & 3;
    const int k0 = 32 * s + 8 * g + q, k1 = k0 + 4, chn = 2 * ct + (p >> 1);
    return cat8(tr_read(img + k0 * 128 + 16 * (chn ^ wswz(k0)) + 8 * (p & 1)),
                tr_read(img + k1 * 128 + 16 * (chn ^ wswz(k1)) + 8 * (p & 1)));
}
DI bf16x8 nfrag16(const char* img, int node, int chunk) {
    return *(const bf16x8*)(img + node * 128 + 16 * (chunk ^ ((node >> 1) & 7)));
}

constexpr int K2_W = 12, K2_T = 64 * K2_W;
constexpr int WCAP = 576;
constexpr int RCAP = 6 * WCAP;
constexpr int L_SLOT = 0;
constexpr int L_STAGE = 49152;
constexpr int RALL = RCAP + 128;
constexpr int L_R32 = 73728;
constexpr int L_R8 = L_R32 + RALL * 4;
constexpr int L_AIMG = L_R8 + RALL;
constexpr int L_HIMG = L_AIMG + 6144;
constexpr int L_WH = L_HIMG + 6144;
constexpr int L_PD = L_WH + 16384;
constexpr int L_OH = L_PD + 768;
constexpr int L_WX = L_OH + 256;
constexpr int L_BINS = L_WX + 256;
constexpr int L_WF = L_BINS + 896;
constexpr int L_RDY = L_WF + 64;
constexpr int L_PB = L_RDY + 64;
constexpr int L_SINK = L_PB + K2_W * 1024;
constexpr int L_END = L_SINK + K2_W * 256;
constexpr int A_CNT = L_SLOT, A_OFF = A_CNT + 1024, A_TOT = A_OFF + 1024, A_R = A_TOT + 16, A_W = A_R + 16384, A_O = A_W + 16384;
static_assert(A_O + BN * 128 <= L_STAGE, "phase A scratch");
constexpr unsigned MAPCAP = 128;
constexpr int PROW = 272, PPOS = 8 * PROW;

DI bf16x8 as_frag(unsigned a, unsigned b, unsigned c, unsigned d) {
    u32x4 v = {a, b, c, d};
    return __builtin_bit_cast(bf16x8, v);
}
DI float ex2(float x) { return __builtin_amdgcn_exp2f(x); }
DI float rcp(float x) { return __builtin_amdgcn_rcpf(x); }

template <class H1, class H2, class H3>
DI void half_tile(const f32x16& xc, f32x16& xn, const bf16x8& aA, const bf16x8& aB, const bf16x8& ef, const bf16x8& eB,
                  const bf16x8& i0, const bf16x8& i1, f32x16& zp, u32x4& pkp, const bf16x8& wp, f32x16& zc,
                  const bf16x8& wc, H1&& hook1, H2&& hook2, H3&& hook3) {
    f32x16 zero;
#pragma unroll
    for (int i = 0; i < 16; ++i) zero[i] = 0.f;
    f32x2 e0, e1, e2, e3;
    u32x4 pk;
#define EXP2(e, i) do { e[0] = ex2(xc[i]); PIN(e[0]); } while (0)
#define EXP2H(e, i) do { e[1] = ex2(xc[i]); PIN(e[1]); } while (0)
#define RCP2(e) do { e[0] = rcp(e[0]); PIN(e[0]); } while (0)
#define RCP2H(e) do { e[1] = rcp(e[1]); PIN(e[1]); } while (0)
#define ADD1(e) do { e = e + 1.0f; PIN(e); } while (0)
#define MULX(e, i) do { e = e * f32x2{xc[i], xc[i + 1]}; PIN(e); } while (0)
#define CVT(d, e) do { d = __builtin_bit_cast(unsigned, __builtin_convertvector(e, bf16x2)); PIN(d); } while (0)
    xn = MFMA32(aA, i0, zero); SBAR();
    EXP2(e0, 0); EXP2H(e0, 1); EXP2(e1, 2); SBAR();
    zp = MFMA32(__builtin_bit_cast(bf16x8, pkp), wp, zp); SBAR();
    EXP2H(e1, 3); EXP2(e2, 4); EXP2H(e2, 5); EXP2(e3, 6); EXP2H(e3, 7); SBAR();
    ADD1(e0); ADD1(e1); ADD1(e2); ADD1(e3); SBAR();
    hook1(); SBAR();
    xn = MFMA32(aB, i1, xn); SBAR();
    RCP2(e0); RCP2H(e0); RCP2(e1); SBAR();
    xn = MFMA32(ef, eB, xn); SBAR();
    RCP2H(e1); RCP2(e2); RCP2H(e2); RCP2(e3); RCP2H(e3); SBAR();
    MULX(e0, 0); MULX(e1, 2); MULX(e2, 4); MULX(e3, 6); SBAR();
    CVT(pk[0], e0); CVT(pk[1], e1); CVT(pk[2], e2); CVT(pk[3], e3); SBAR();
    EXP2(e0, 8); EXP2H(e0, 9); SBAR();
    zc = MFMA32(__builtin_bit_cast(bf16x8, pk), wc, zc); SBAR();
    EXP2(e1, 10); EXP2H(e1, 11); EXP2(e2, 12); EXP2H(e2, 13); EXP2(e3, 14); EXP2H(e3, 15); SBAR();
    ADD1(e0); ADD1(e1); ADD1(e2); ADD1(e3); SBAR();
    hook2(); SBAR();
    RCP2(e0); RCP2H(e0); RCP2(e1); RCP2H(e1); RCP2(e2); RCP2H(e2); RCP2(e3); RCP2H(e3); SBAR();
    hook3(); SBAR();
    MULX(e0, 8); MULX(e1, 10); MULX(e2, 12); MULX(e3, 14); SBAR();
    CVT(pkp[0], e0); CVT(pkp[1], e1); CVT(pkp[2], e2); CVT(pkp[3], e3); SBAR();
#undef EXP2
#undef EXP2H
#undef RCP2
#undef RCP2H
#undef ADD1
#undef MULX
#undef CVT
}

__global__ __launch_bounds__(K2_T, 3) void k_egnn(const int* __restrict__ src, const int* __restrict__ dst,
                                               const uint16_t* __restrict__ ea, const uint16_t* __restrict__ h,
                                               const uint16_t* __restrict__ pos, const uint16_t* __restrict__ we,
                                               const uint16_t* __restrict__ wx, const uint16_t* __restrict__ wh,
                                               char* __restrict__ ws, uint16_t* __restrict__ out) {
    extern __shared__ __attribute__((aligned(16))) char sm[];
    const int tid = threadIdx.x, lane = tid & 63;
    const int wv = __builtin_amdgcn_readfirstlane(tid >> 6);
    const int xc = blockIdx.x & 7;
    const int b = 31 * xc + (xc < 2 ? xc : 2) + (blockIdx.x >> 3);
    const unsigned smb = (unsigned)(size_t)(LDS_AS char*)sm;
    asm volatile("" :: "s"(src), "s"(dst), "s"(ea), "s"(h), "s"(pos), "s"(we), "s"(wx), "s"(wh), "s"(ws), "s"(out));
    const char* Rec = ws + W_R;
    const char* Btab = ws + W_B;
    const char* Ptab = ws + W_P;

    u32x2 ep;
    asm volatile("global_load_dwordx2 %0, %1, %2 sc1" : "=v"(ep) : "v"(0u), "s"(ws + W_E) : "memory");
    u32x4 d4 = {0, 0, 0, 0}, s4 = {0, 0, 0, 0};
    u32x2 a4 = {0, 0};
    unsigned sm0 = 0, sm1 = 0;
    const int r8l = lane >> 3, c8 = lane & 7;
    const int gnode = b * BN + (lane < BN ? lane : 0);
    if (wv < 10) {
        const int e0 = b * CE + 4 * tid;
        asm volatile("global_load_dwordx4 %0, %3, %4\n\tglobal_load_dwordx4 %1, %3, %5\n\tglobal_load_dwordx2 %2, %6, %7"
                     : "=&v"(d4), "=&v"(s4), "=&v"(a4)
                     : "v"(e0 * 4), "s"(dst), "s"(src), "v"(e0 * 2), "s"(ea) : "memory");
    } else if (wv == 10) {
        asm volatile("global_load_dword %0, %2, %3\n\tglobal_load_ushort %1, %2, %3 offset:4"
                     : "=&v"(sm0), "=&v"(sm1) : "v"(gnode * 6), "s"(pos) : "memory");
    } else {
        asm volatile("global_load_ushort %0, %1, %2" : "=v"(sm0) : "v"(lane * 2), "s"(wx) : "memory");
        lds_st(smb + A_CNT + lane * 16, u32x4{0, 0, 0, 0});
    }
    bar();
    {
        auto piece = [&](int p) {
            if (p < 5) dma1(smb + L_HIMG + p * 1024, h, (b * BN + 8 * p + r8l) * 128 + ((c8 ^ ((4 * p + (lane >> 4)) & 7)) * 16));
            else if (p < 21) dma1(smb + A_W + (p - 5) * 1024, we, (8 * (p - 5) + r8l) * 128 + ((c8 ^ ((r8l & 2) | (((p - 5) & 1) << 2))) * 16));
            else if (lane < 16) dma1(smb + L_AIMG + BN * 128, we, 128 * 128 + lane * 16);
        };
        piece(wv);
        if (wv < 10) piece(12 + wv);
    }
    asm volatile("s_waitcnt vmcnt(0)" : "+v"(ep), "+v"(d4), "+v"(s4), "+v"(a4), "+v"(sm0), "+v"(sm1) :: "memory");
    const unsigned e_lo = __builtin_amdgcn_readfirstlane(ep[0]), e_hi = __builtin_amdgcn_readfirstlane(ep[1]);
    const unsigned n_lo = e_lo + 1u, n_hi = e_hi + (n_lo == 0u ? 1u : 0u);
    const unsigned tag_w1 = (n_lo >> 8) | (n_hi << 24), tag_w0 = n_lo << 24;

    unsigned bk[4], ark[4], code = 0;
    if (wv < 10) {
#pragma unroll
        for (int i = 0; i < 4; ++i) {
            bk[i] = __umul24(d4[i], 52429u) >> 21;
            ark[i] = atomicAdd((unsigned*)(sm + A_CNT) + bk[i], 1u);
        }
#pragma unroll
        for (int i = 0; i < 4; ++i) {
            const unsigned dl = d4[i] - bk[i] * BN, w = win_of(dl);
            code |= (w * 8 + dl - win_start(w)) << (8 * i);
        }
    } else if (wv == 10) {
        const f32x4 p = {bf2f(sm0 & 0xffffu), __uint_as_float(sm0 & 0xffff0000u), bf2f(sm1), 0.f};
        if (lane < 48) lds_st(smb + L_PD + lane * 16, lane < BN ? p : f32x4{0.f, 0.f, 0.f, 0.f});
        if (lane < BN) st_dev(Ptab, gnode * 16, __builtin_bit_cast(u32x4, p));
    } else {
        lds_st(smb + L_WX + lane * 4, bf2f(sm0) * NEG_LN2);
        if (b == 0) {
            const u32x4 z = {0, 0, 0, 0};
            st_dev(Ptab, NN * 16 + lane * 16, z);
            if (lane < 8) st_dev(Btab, NN * 128 + lane * 16, z);
        }
    }
    bar();

    u32x4 t_lo = {0, 0, 0, 0};
    if (wv == 11) {
        __builtin_amdgcn_s_setprio(3);
        const u32x4 cn = lds_ld<u32x4>(smb + A_CNT + lane * 16);
        const u32x4 tr = {div3(cn[0] + 2), div3(cn[1] + 2), div3(cn[2] + 2), div3(cn[3] + 2)};
        const unsigned tot = tr[0] + tr[1] + tr[2] + tr[3];
        const unsigned in = scan64(tot), ex = in - tot;
        const u32x4 of = {ex, ex + tr[0], ex + tr[0] + tr[1], ex + tr[0] + tr[1] + tr[2]};
        lds_st(smb + A_OFF + lane * 16, of);
        if (lane == 63) lds_st(smb + A_TOT, in);
#pragma unroll
        for (int i = 0; i < 4; ++i) t_lo[i] = tag_w0 | (cn[i] << 12) | of[i];
        __builtin_amdgcn_s_setprio(0);
    } else if (wv < 8) {
        const int ct = wv & 3, g = lane >> 4, l15 = lane & 15;
        const char* wimg = sm + A_W + (wv >> 2) * 8192;
        const bf16x8 a0 = wfrag16(wimg, 0, ct, lane), a1 = wfrag16(wimg, 1, ct, lane);
        bf16x8 hb[6];
#pragma unroll
        for (int nt = 0; nt < 3; ++nt) {
            hb[2 * nt] = nfrag16(sm + L_HIMG, 16 * nt + l15, g);
            hb[2 * nt + 1] = nfrag16(sm + L_HIMG, 16 * nt + l15, 4 + g);
        }
        SBAR();
        f32x4 acc[3];
#pragma unroll
        for (int nt = 0; nt < 3; ++nt) acc[nt] = MFMA16(a0, hb[2 * nt], (f32x4{0.f, 0.f, 0.f, 0.f}));
#pragma unroll
        for (int nt = 0; nt < 3; ++nt) acc[nt] = MFMA16(a1, hb[2 * nt + 1], acc[nt]);
#pragma unroll
        for (int nt = 0; nt < 3; ++nt) {
            const int node = 16 * nt + l15;
            const u32x2 o = {pk_bf16(acc[nt][0] * NEG_LOG2E, acc[nt][1] * NEG_LOG2E), pk_bf16(acc[nt][2] * NEG_LOG2E, acc[nt][3] * NEG_LOG2E)};
            const unsigned oa = wv < 4 ? L_AIMG + node * 128 + (16 * ct + 4 * g) * 2
                                       : A_O + node * 128 + 16 * ((2 * ct + (g >> 1)) ^ (node & 7)) + 8 * (g & 1);
            lds_st(smb + (nt < 2 || l15 < BN - 32 ? oa : (unsigned)(A_R + 16320 + (lane & 7) * 8)), o);
        }
    }
    bar();

    if (wv < 10) {
#pragma unroll
        for (int i = 0; i < 4; ++i) {
            const unsigned q = div3(ark[i]), j = ark[i] - 3 * q;
            const unsigned t = smb + A_R + (lds_ld<unsigned>(smb + A_OFF + bk[i] * 4) + q) * 16;
            const unsigned av = (i & 1) ? (a4[i >> 1] & 0xffff0000u) : (a4[i >> 1] << 16);
            lds_st(t + j * 4, s4[i] | av);
            lds_st(t + 12 + j, (unsigned char)(code >> (8 * i)));
        }
    } else {
#pragma clang loop unroll(disable)
        for (int x = tid - 640; x < BN * 8; x += 128) {
            const int node = x >> 3, ch = (x & 7) ^ (node & 7);
            st_dev(Btab, (b * BN + node) * 128 + ch * 16, lds_ld<u32x4>(smb + A_O + x * 16));
        }
    }
    bar();
    {
        const unsigned ntot = lds_ld<unsigned>(smb + A_TOT);
        if ((unsigned)tid < ntot) st_dev(Rec, b * 16384 + tid * 16, lds_ld<u32x4>(smb + A_R + tid * 16));
        if ((unsigned)tid + K2_T < ntot) st_dev(Rec, b * 16384 + (tid + K2_T) * 16, lds_ld<u32x4>(smb + A_R + (tid + K2_T) * 16));
    }
    {
        const u32x4 z = {0, 0, 0, 0};
        lds_st(smb + L_STAGE + wv * 2048 + lane * 16, z);
        lds_st(smb + L_STAGE + wv * 2048 + 1024 + lane * 16, z);
        const u32x4 n32 = {NN, NN, NN, NN}, n8 = {0x08080808u, 0x08080808u, 0x08080808u, 0x08080808u};
        lds_st(smb + L_R32 + tid * 16, n32);
        if (tid < RALL / 4 - K2_T) lds_st(smb + L_R32 + (K2_T + tid) * 16, n32);
        if (wv >= 1 && wv < 5) {
            if (tid - 64 < RALL / 16) lds_st(smb + L_R8 + (tid - 64) * 16, n8);
        } else if (wv == 5) {
            if (lane < 56) lds_st(smb + L_BINS + lane * 16, z);
            if (lane < 16) lds_st(smb + L_RDY + lane * 4, 0u);
        } else if (wv == 6) {
            if (lane < 48) lds_st(smb + L_AIMG + (BN + 2) * 128 + lane * 16, z);
        } else if (wv == 7) {
            const int n = lane >> 2, j = lane & 3;
            if (lane < 36) lds_st(smb + L_OH + lane * 4, (n < 8 && (n >> 1) == j) ? (0x3F80u << (16 * (n & 1))) : 0u);
        }
    }
    wait_vm0();
    bar();
    if (wv == 11) {
        st_dev(ws + W_T, b * 2048 + lane * 32, u32x4{t_lo[0], tag_w1, t_lo[1], tag_w1});
        st_dev(ws + W_T, b * 2048 + lane * 32 + 16, u32x4{t_lo[2], tag_w1, t_lo[3], tag_w1});
    }

    const int r = lane & 31, hh = lane >> 5;
    const int win = (int)((0x545432231100ull >> (4 * wv)) & 7);
    const bool second = (0xC5A >> wv) & 1;
    const int wnode = win_start(win);
    char* part = sm + L_SLOT + wv * 4096;
    const int blk = (lane >> 4) & 1, q4 = (lane & 15) >> 2, p4 = lane & 3;
    bf16x8 i0, i1, e0, e1;
#pragma unroll
    for (int j = 0; j < 8; ++j) {
        i0[j] = (r == 8 * hh + j) ? (short)0x3F80 : (short)0;
        i1[j] = (r == 16 + 8 * hh + j) ? (short)0x3F80 : (short)0;
    }
    {
        const int row = hh ? BN + q4 : wnode + q4;
        const char* ab = sm + L_AIMG + row * 128 + (16 * blk + 4 * p4) * 2;
        e0 = cat8(tr_read(ab), tr_read(ab + 512));
        e1 = cat8(tr_read(ab + 64), tr_read(ab + 64 + 512));
    }
    unsigned aad[4], kk[4];
    {
        const int rho = 8 * (r & 3) + (r >> 2);
        const int fsw = (r & 3) ^ (((r >> 3) & 1) << 2);
#pragma unroll
        for (int s = 0; s < 4; ++s) aad[s] = smb + L_SLOT + wv * 4096 + rho * 128 + 16 * ((2 * s + hh) ^ fsw);
#pragma unroll
        for (int i = 0; i < 4; ++i) kk[i] = 16 * ((lane & 7) ^ i ^ (((lane >> 4) & 1) << 2)) + (3072 - 1024 * i);
    }
    const char* gB = Btab - 3072;
    const unsigned slot_a = smb + L_SLOT + wv * 4096;
    const unsigned pbuf_a = smb + L_PB + wv * 1024;
    unsigned src_a = smb + L_R32 + (lane >> 3) * 16, rs_a = smb + L_R32 + lane * 4, rn_a = smb + L_R8 + lane;
    unsigned stage_r = smb + L_STAGE + wv * 2048 + r * 64;
    unsigned trb_a = smb + L_STAGE + wv * 2048 + (4 * hh + q4) * 64 + blk * 32 + p4 * 8;
    const unsigned pdw = smb + L_PD + wnode * 16;
    unsigned oh_a = smb + L_OH;
    f32x16 z0, z1;
#pragma unroll
    for (int i = 0; i < 16; ++i) { z0[i] = 0.f; z1[i] = 0.f; }
    PIN(i0); PIN(i1); PIN(e0); PIN(e1); PIN(z0); PIN(z1);
    PIN(aad[0]); PIN(aad[1]); PIN(aad[2]); PIN(aad[3]); PIN(kk[0]); PIN(kk[1]); PIN(kk[2]); PIN(kk[3]);
    PIN(src_a); PIN(rs_a); PIN(rn_a); PIN(stage_r); PIN(trb_a); PIN(oh_a);

#pragma clang loop unroll(disable)
    for (int q = wv; q < 16; q += K2_W)
        dma1(smb + L_WH + q * 1024, wh, (8 * q + r8l) * 128 + ((c8 ^ ((r8l & 2) | ((q & 1) << 2))) * 16));

    const int l3 = (lane * 171) >> 9, sub = lane - 3 * l3;
    const int cid = 21 * wv + l3;
    const bool cok = lane < 63 && cid < NCH;
    const int cidc = cok ? cid : 0;
    unsigned tval = 0;
    bool lost = true;
#pragma clang loop unroll(disable)
    for (int spin = 0; spin < (1 << 16); ++spin) {
        u32x2 tv;
        asm volatile("global_load_dwordx2 %0, %1, %2 sc1\n\ts_waitcnt vmcnt(0)"
                     : "=&v"(tv) : "v"((unsigned)W_T + cidc * 2048 + b * 8), "s"(ws) : "memory");
        tval = tv[0];
        if (__all(!cok || (tv[1] == tag_w1 && (tv[0] >> 24) == (tag_w0 >> 24)))) { lost = false; break; }
        __builtin_amdgcn_s_sleep(1);
    }

    const unsigned cnt = (cok && !lost) ? ((tval >> 12) & 0xfffu) : 0u;
    const unsigned ntr = div3(cnt + 2);
    const unsigned incl = scan64(sub == 0 ? ntr : 0u);
    const unsigned mw = __builtin_amdgcn_readlane(incl, 63);
    const bool slow_w = mw > MAPCAP;
    const unsigned map_a = smb + L_SLOT + wv * 4096;
    if (!slow_w) {
        unsigned g = (cidc * 1024 + (tval & 0xfffu) + sub) | ((cnt - 3 * sub) << 20);
        unsigned mp = map_a + (incl - ntr + sub) * 4;
#pragma clang loop unroll(disable) vectorize(disable) interleave(disable)
        for (unsigned i = sub; i < ntr; i += 3) { lds_st(mp, g); mp += 12; g += 3 - (9u << 20); }
    }
    const unsigned mwe = slow_w ? 0u : mw;
    unsigned left[2];
    u32x4 rec[2];
#pragma unroll
    for (int k = 0; k < 2; ++k) {
        const unsigned m = lds_ld<unsigned>(map_a + (64 * k + lane) * 4);
        const bool ok = (unsigned)(64 * k + lane) < mwe;
        left[k] = ok ? m >> 20 : 0u;
        const unsigned go = ok ? (m & 0xfffffu) * 16 : 0u;
        asm volatile("global_load_dwordx4 %0, %1, %2 sc1" : "=v"(rec[k]) : "v"(go), "s"(Rec) : "memory");
    }
    if (lane == 0) lds_st(smb + L_WF + wv * 4, lost ? 2u : slow_w ? 1u : 0u);
    asm volatile("s_waitcnt vmcnt(0)" : "+v"(rec[0]), "+v"(rec[1]) :: "memory");
    unsigned bin[6], rk[6];
#pragma unroll
    for (int k = 0; k < 6; ++k) {
        const unsigned w = (rec[k / 3][3] >> (8 * (k % 3) + 3)) & 7u;
        bin[k] = (left[k / 3] > (unsigned)(k % 3) ? w : 6u) * 128 + (lane & 31) * 4;
        rk[k] = atomicAdd((unsigned*)(sm + L_BINS + bin[k]), 1u);
    }
    bar();
    {
        const unsigned part = blockIdx.x >> 3;
        const unsigned off = tid < 626 ? (unsigned)W_B + part * (313 * 128) + tid * 64
                                       : (unsigned)W_P + part * (40 * 128) + (tid < 706 ? tid - 626 : 0) * 64;
        touch(smb + L_SINK + wv * 256, ws, off);
    }

    if (b == 0 && tid == 7 * 64)
        asm volatile("global_store_dwordx2 %0, %1, %2 sc1" :: "v"((unsigned)W_E), "v"(u32x2{n_lo, n_hi}), "s"(ws) : "memory");
    const unsigned otab = smb + L_SLOT + wv * 4096 + 1024;
    int tw6[6];
    {
        const unsigned wfl = lds_ld<unsigned>(smb + L_WF + (lane < K2_W ? lane : 0) * 4);
        unsigned c3[3], in3[3];
#pragma unroll
        for (int k = 0; k < 3; ++k) c3[k] = lds_ld<unsigned>(smb + L_BINS + (2 * k + hh) * 128 + r * 4);
#pragma unroll
        for (int k = 0; k < 3; ++k) in3[k] = scan32(c3[k]);
#pragma unroll
        for (int k = 0; k < 3; ++k) {
            lds_st(otab + (2 * k + hh) * 128 + r * 4, (2 * k + hh) * WCAP + in3[k] - c3[k]);
            tw6[2 * k] = (__builtin_amdgcn_readlane(in3[k], 31) + 31) >> 5;
            tw6[2 * k + 1] = (__builtin_amdgcn_readlane(in3[k], 63) + 31) >> 5;
        }
        if (__any(wfl == 2)) { wait_vm0(); return; }
        tw6[0] |= __any(wfl != 0) ? 0x100 : 0;
    }
    constexpr int WT = WCAP / 32;
    const bool slow = tw6[0] > WT || tw6[1] > WT || tw6[2] > WT || tw6[3] > WT || tw6[4] > WT || tw6[5] > WT;

    if (!slow) {
#pragma unroll
        for (int k = 0; k < 6; ++k) {
            const unsigned o = lds_ld<unsigned>(otab + bin[k]) + rk[k];
            const unsigned p = left[k / 3] > (unsigned)(k % 3) ? o : (unsigned)(RALL - 1);
            lds_st(smb + L_R32 + p * 4, rec[k / 3][k % 3]);
            lds_st(smb + L_R8 + p, (unsigned char)((rec[k / 3][3] >> (8 * (k % 3))) & 7u));
        }
        const int wstart = win * WCAP;
        const int l0 = tw6[0] + tw6[3] + tw6[4], l1 = tw6[0] + tw6[2] + tw6[5];
        const int l2 = tw6[1] + tw6[2] + tw6[4], l3s = tw6[1] + tw6[3] + tw6[5];
        const int wtiles = win == 0 ? tw6[0] : win == 1 ? tw6[1] : win == 2 ? tw6[2] : win == 3 ? tw6[3] : win == 4 ? tw6[4] : tw6[5];
        const int la = win == 0 ? l0 : win == 1 ? l2 : win == 2 ? l1 : win == 3 ? l3s : win == 4 ? l0 : l1;
        const int lb = win == 0 ? l1 : win == 1 ? l3s : win == 2 ? l2 : win == 3 ? l0 : win == 4 ? l2 : l3s;
        int first = (4 * wtiles - la + lb + 4) >> 3;
        first = first < 0 ? 0 : first > wtiles ? wtiles : first;
        const int ta = second ? first : 0, tb = second ? wtiles : first;

        bar();

        f32x16 xa, xb;

        if (ta < tb) {
            int base = wstart + 32 * ta;
            const int end = wstart + 32 * tb;
            bf16x8 a0, a1, a2, a3, ef, wf0, wf1;
            unsigned rs, rn, w0, w1, ua, vb;
            unsigned saddr;
            bool valid;
            u32x4 pkp = {0, 0, 0, 0};
#pragma unroll
            for (int j = 0; j < 8; ++j) wf1[j] = 0;

            u32x4 sn, exr;
            f32x4 pd, ps;
            auto read_src = [&](int ri) { sn = lds_ld<u32x4>(src_a + ri * 4); };
            auto issue_rows = [&]() {
                const unsigned v0 = mad16(sn[0], 128, kk[0]), v1 = mad16(sn[1], 128, kk[1]);
                const unsigned v2 = mad16(sn[2], 128, kk[2]), v3 = mad16(sn[3], 128, kk[3]);
                wait_lgkm0();
                dma4_dev(slot_a, gB, v0, v1, v2, v3);
            };
            auto read_pair = [&](int ri) {
                rs = lds_ld<unsigned>(rs_a + ri * 4);
                rn = lds_ld<unsigned char>(rn_a + ri);
            };
            auto issue_pos = [&]() { dma1_dev(pbuf_a, Ptab, mad16(rs, 16, 0u)); };
            auto read_pos = [&]() {
                pd = lds_ld<f32x4>(pdw + rn * 16);
                ps = lds_ld<f32x4>(pbuf_a + lane * 16);
            };
            auto geometry = [&]() {
                const float dx = pd[0] - ps[0], dy = pd[1] - ps[1], dz = pd[2] - ps[2];
                const float d2 = (dx * dx + dy * dy + dz * dz) * NEG_LOG2E;
                w0 = pk_bf16(1.0f, dx);
                w1 = pk_bf16(dy, dz);
                const unsigned ext = pk_bf16(d2, __uint_as_float(rs & 0xffff0000u) * NEG_LOG2E);
                const auto sw = __builtin_amdgcn_permlane32_swap(oh_a + rn * 16, ext, false, false);
                ua = sw[0];
                vb = sw[1];
                saddr = stage_r + rn * 8;
                PIN(saddr); PIN(w0); PIN(w1);
                valid = rn < 8;
            };
            auto read_rows = [&]() {
                a0 = lds_ld<bf16x8>(aad[0]); a1 = lds_ld<bf16x8>(aad[1]);
                a2 = lds_ld<bf16x8>(aad[2]); a3 = lds_ld<bf16x8>(aad[3]);
            };
            auto read_ext = [&](unsigned u) { exr = lds_ld<u32x4>(hh ? oh_a : u); };
            auto fix_ext = [&](unsigned u) {
                u32x4 ex = exr;
                ex[0] = hh ? u : ex[0];
                ef = __builtin_bit_cast(bf16x8, ex);
                PIN(ef);
            };
            auto stage_weights = [&](bool wr) {
                if (wr) lds_st(saddr, u32x2{w0, w1});
                wf0 = cat8(tr_read_a(trb_a), tr_read_a(trb_a + 512));
                wf1 = cat8(tr_read_a(trb_a + 1024), tr_read_a(trb_a + 1536));
                if (wr) lds_st(saddr, u32x2{0, 0});
            };
            auto nop = [&]() {};

            if (wv >= 8) __builtin_amdgcn_s_sleep(10);
            else if (wv >= 4) __builtin_amdgcn_s_sleep(5);
            read_pair(base);
            read_src(base);
            issue_pos();
            issue_rows();
            if (base + 32 < end) read_src(base + 32);
            asm volatile("s_waitcnt vmcnt(4)" ::: "memory");
            read_pos();
            geometry();
            read_ext(ua);
            wait_vm0();
            read_rows();
            if (base + 32 < end) issue_rows();
            fix_ext(ua);
            {
                f32x16 zero;
#pragma unroll
                for (int i = 0; i < 16; ++i) zero[i] = 0.f;
                xa = MFMA32(a0, i0, zero);
                xa = MFMA32(a1, i1, xa);
                xa = MFMA32(ef, e0, xa);
            }
            SBAR();
            auto set_prio = [&](int rem) {
                if (rem >= 192) __builtin_amdgcn_s_setprio(3);
                else if (rem >= 96) __builtin_amdgcn_s_setprio(2);
                else __builtin_amdgcn_s_setprio(1);
            };
            while (true) {
                set_prio(end - base);
                half_tile(xa, xb, a2, a3, ef, e1, i0, i1, z1, pkp, wf1, z0, wf0,
                          [&]() { stage_weights(valid && hh == 0); read_src(base + 64); read_pair(base + 64); },
                          [&]() { wait_vm0(); read_rows(); read_ext(vb); },
                          nop);
                half_tile(xb, xa, a0, a1, ef, e0, i0, i1, z0, pkp, wf1, z1, wf0,
                          [&]() { fix_ext(vb); if (base + 64 < end) { issue_rows(); issue_pos(); } }, nop, nop);
                if (base + 32 >= end) break;
                half_tile(xa, xb, a2, a3, ef, e1, i0, i1, z1, pkp, wf1, z0, wf0,
                          [&]() { stage_weights(valid && hh == 1); read_src(base + 96); },
                          [&]() { wait_vm0(); read_rows(); read_pos(); },
                          [&]() { geometry(); read_ext(ua); });
                half_tile(xb, xa, a0, a1, ef, e0, i0, i1, z0, pkp, wf1, z1, wf0,
                          [&]() { fix_ext(ua); if (base + 96 < end) issue_rows(); }, nop, nop);
                base += 64;
                if (base >= end) break;
            }
            z1 = MFMA32(__builtin_bit_cast(bf16x8, pkp), wf1, z1);
        }
        __builtin_amdgcn_s_setprio(0);
        wait_vm0();
        if ((lane & 3) == 0) {
            const int n = r >> 2;
#pragma unroll
            for (int g = 0; g < 4; ++g) {
                *(f32x4*)(part + n * PROW + (8 * g + 4 * hh) * 4) = f32x4{z0[4 * g], z0[4 * g + 1], z0[4 * g + 2], z0[4 * g + 3]};
                *(f32x4*)(part + n * PROW + (32 + 8 * g + 4 * hh) * 4) = f32x4{z1[4 * g], z1[4 * g + 1], z1[4 * g + 2], z1[4 * g + 3]};
            }
        }
        float cs0 = 0.f, cs1 = 0.f;
#pragma unroll
        for (int g = 0; g < 4; ++g) {
            const f32x4 w0 = *(const f32x4*)(sm + L_WX + (8 * g + 4 * hh) * 4);
            const f32x4 w1 = *(const f32x4*)(sm + L_WX + (32 + 8 * g + 4 * hh) * 4);
#pragma unroll
            for (int i = 0; i < 4; ++i) { cs0 += z0[4 * g + i] * w0[i]; cs1 += z1[4 * g + i] * w1[i]; }
        }
        const float csh = cs0 + cs1;
        const auto cw = __builtin_amdgcn_permlane32_swap(__float_as_uint(csh), __float_as_uint(csh), false, false);
        const float cst = __uint_as_float(cw[0]) + __uint_as_float(cw[1]);
        if (hh == 0) ((float*)(part + PPOS))[r] = cst;
        asm volatile("s_waitcnt lgkmcnt(0)" ::: "memory");
        if (lane == 0) *(volatile LDS_AS unsigned*)(size_t)(smb + L_RDY + wv * 4) = 1u;
    } else {
        __builtin_amdgcn_fence(__ATOMIC_ACQUIRE, "agent");
        wait_vm0();
        {
            const u32x4 z = {0, 0, 0, 0};
            *(u32x4*)(part + lane * 16) = z;
            *(u32x4*)(part + 1024 + lane * 16) = z;
            *(u32x4*)(part + 2048 + lane * 16) = z;
        }
        bar();
        const float wd = bf2f(we[128 * 64 + lane]), wa = bf2f(we[129 * 64 + lane]);
        const float wxl = ((const float*)(sm + L_WX))[lane];
        for (int ci = 0; ci < NCH; ++ci) {
            const unsigned tv = *(const unsigned*)(ws + W_T + ci * 2048 + b * 8);
            const unsigned g0 = ci * 1024 + (tv & 0xfffu), cn = (tv >> 12) & 0xfffu;
            for (unsigned i = (wv + ci) % K2_W; i < cn; i += K2_W) {
                const unsigned* tp = (const unsigned*)(Rec + (size_t)(g0 + div3(i)) * 16);
                const unsigned j = i - 3 * div3(i);
                const unsigned rc = tp[j], cd = (tp[3] >> (8 * j)) & 0xffu;
                const unsigned s = rc & 0xffffu, w = cd >> 3, n = cd & 7, dl = win_start(w) + n;
                const f32x4 ps = *(const f32x4*)(Ptab + s * 16);
                const f32x4 pd = *(const f32x4*)(sm + L_PD + dl * 16);
                const float dx = pd[0] - ps[0], dy = pd[1] - ps[1], dz = pd[2] - ps[2];
                const unsigned e = pk_bf16((dx * dx + dy * dy + dz * dz) * NEG_LOG2E, __uint_as_float(rc & 0xffff0000u) * NEG_LOG2E);
                const unsigned dxy = pk_bf16(dx, dy), dz0 = pk_bf16(dz, 0.f);
                float y = bf2f(((const uint16_t*)(sm + L_AIMG))[dl * 64 + lane]) + bf2f(((const uint16_t*)Btab)[s * 64 + lane]);
                y += bf2f(e & 0xffffu) * wd + bf2f(e >> 16) * wa;
                const float m = bf2f(pk_bf16(y * rcp(1.0f + ex2(y)), 0.f) & 0xffffu);
                char* tgt = sm + L_SLOT + wave_a(w) * 4096;
                atomicAdd((float*)(tgt + n * PROW) + lane, m);
                float t = m * wxl;
                for (int o = 32; o > 0; o >>= 1) t += __shfl_xor(t, o);
                if (lane < 3) {
                    const float dv = lane == 0 ? bf2f(dxy & 0xffffu) : lane == 1 ? bf2f(dxy >> 16) : bf2f(dz0 & 0xffffu);
                    atomicAdd((float*)(tgt + PPOS) + 4 * n + 1 + lane, dv * t);
                }
            }
        }
    }
    if (slow) bar();

    const int g = lane >> 4, l15 = lane & 15, wsize = win < 4 ? 7 : 6;
    const int node = wnode + l15, nodec = node < 48 ? node : 47, ct0 = second ? 2 : 0;
    bf16x8 wfr[2][4];
#pragma unroll
    for (int c = 0; c < 2; ++c)
#pragma unroll
        for (int s = 0; s < 4; ++s) wfr[c][s] = wfrag16(sm + L_WH, s, ct0 + c, lane);
    const bf16x8 hb0 = nfrag16(sm + L_HIMG, nodec, g), hb1 = nfrag16(sm + L_HIMG, nodec, 4 + g);
    f32x4 acc[2];
    u32x2 hv[2];
#pragma unroll
    for (int c = 0; c < 2; ++c) {
        acc[c] = MFMA16(wfr[c][0], hb0, (f32x4{0.f, 0.f, 0.f, 0.f}));
        acc[c] = MFMA16(wfr[c][1], hb1, acc[c]);
        hv[c] = *(const u32x2*)(sm + L_HIMG + nodec * 128 + 16 * ((2 * (ct0 + c) + (g >> 1)) ^ ((nodec >> 1) & 7)) + 8 * (g & 1));
    }
    const int wa = wave_a(win), wb = wave_b(win);
    const char* pb = sm + L_SLOT + wa * 4096 + (l15 & 7) * PROW + g * 32;
    const int pd2 = (wb - wa) * 4096;
    const int pnl = (lane * 171) >> 9, pcc = lane - 3 * pnl;
    const bool pok = !second && pnl < wsize;
    const unsigned ppo = smb + L_SLOT + wa * 4096 + PPOS + (4 * (pnl & 7) + 1 + pcc) * 4;
    const float p0 = lds_ld<float>(smb + L_PD + (wnode + (pok ? pnl : 0)) * 16 + pcc * 4);
    if (!slow) {
        const unsigned flag = smb + L_RDY + (second ? wa : wb) * 4;
#pragma clang loop unroll(disable)
        for (int spin = 0; spin < (1 << 20); ++spin) {
            if (__builtin_amdgcn_readfirstlane(*(const volatile LDS_AS unsigned*)(size_t)flag)) break;
            __builtin_amdgcn_s_sleep(4);
        }
        asm volatile("" ::: "memory");
    }
    const float ps0 = lds_ld<float>(ppo), ps1 = lds_ld<float>(ppo + pd2);
#pragma unroll
    for (int s = 0; s < 2; ++s) {
        const f32x4 m0 = (*(const f32x4*)(pb + s * 128) + *(const f32x4*)(pb + s * 128 + pd2)) * NEG_LN2;
        const f32x4 m1 = (*(const f32x4*)(pb + s * 128 + 16) + *(const f32x4*)(pb + s * 128 + 16 + pd2)) * NEG_LN2;
        const bf16x8 mf = as_frag(pk_bf16(m0[0], m0[1]), pk_bf16(m0[2], m0[3]), pk_bf16(m1[0], m1[1]), pk_bf16(m1[2], m1[3]));
#pragma unroll
        for (int c = 0; c < 2; ++c) acc[c] = MFMA16(wfr[c][2 + s], mf, acc[c]);
    }
    if (pok) out[NN * 64 + (b * BN + wnode + pnl) * 3 + pcc] = (uint16_t)(pk_bf16(p0 + ps0 + ps1, 0.f) & 0xffffu);
    if (l15 < wsize) {
#pragma unroll
        for (int c = 0; c < 2; ++c) {
            const float o0 = bf2f(hv[c][0] & 0xffffu) + silu_f(acc[c][0]), o1 = bf2f(hv[c][0] >> 16) + silu_f(acc[c][1]);
            const float o2 = bf2f(hv[c][1] & 0xffffu) + silu_f(acc[c][2]), o3 = bf2f(hv[c][1] >> 16) + silu_f(acc[c][3]);
            st_nt(out + (b * BN + node) * 64 + 16 * (ct0 + c) + 4 * g, u32x2{pk_bf16(o0, o1), pk_bf16(o2, o3)});
        }
    }
}

}

extern "C" void kernel_launch(void* const* d_in, const int* in_sizes, int n_in, void* d_out, int out_size, void* d_ws,
                              size_t ws_size, hipStream_t stream) {
    using namespace egnn;
    const uint16_t* h = (const uint16_t*)d_in[0];
    const uint16_t* pos = (const uint16_t*)d_in[1];
    const uint16_t* ea = (const uint16_t*)d_in[2];
    const int* src = (const int*)d_in[3];
    const int* dst = (const int*)d_in[4];
    const uint16_t* we = (const uint16_t*)d_in[5];
    const uint16_t* wx = (const uint16_t*)d_in[6];
    const uint16_t* wh = (const uint16_t*)d_in[7];
    (void)hipFuncSetAttribute((const void*)k_egnn, hipFuncAttributeMaxDynamicSharedMemorySize, L_END);
    k_egnn<<<NBK, K2_T, L_END, stream>>>(src, dst, ea, h, pos, we, wx, wh, (char*)d_ws, (uint16_t*)d_out);
}
```
